```python
import torch

def _softmax(x, axis=-1):
    return torch.softmax(x, dim=axis)
def _gelu(x):
    return torch.nn.functional.gelu(x, approximate="tanh")
def _silu(x):
    return torch.nn.functional.silu(x)
def _sigmoid(x):
    return torch.sigmoid(x)
def _layernorm(x, axis=-1, eps=1e-5):
    m = x.mean(dim=axis, keepdim=True)
    v = x.var(dim=axis, keepdim=True, unbiased=False)
    return (x - m) / torch.sqrt(v + eps)
def _scatter_add(tgt, idx, src, axis=0):
    return tgt.clone().index_add_(axis, idx, src)
def _segment_sum(src, seg, n):
    out = torch.zeros((n,) + src.shape[1:], dtype=src.dtype)
    return out.index_add_(0, seg, src)
def _pad(x, pw):
    flat = tuple(v for pair in reversed(pw) for v in pair)
    return torch.nn.functional.pad(x, flat)

kernel_name = 'recurrent_scan__linear_kernel_hedgehog__glu_gated_glu__prompt'

def setup_inputs():

    _w = (lambda a: (lambda t: t.to(torch.bfloat16) if t.is_floating_point() else t)(torch.as_tensor(a)))
    def _A_setup():
        B, S, D, H = 1, 2048, 1024, 32
        Dh = D // H
        return {
            "x": _w((lambda *s: torch.randn(*s, dtype=torch.bfloat16) * 0.05)(B, S, D)),
            "wqkv": _w((lambda *s: torch.randn(*s, dtype=torch.bfloat16) * 0.2)(D, 3 * D)),
            "w_phi": _w((lambda *s: torch.randn(*s, dtype=torch.bfloat16) * 0.2)(H, Dh, Dh) * (Dh ** -0.5)),
            "wo": _w((lambda *s: torch.randn(*s, dtype=torch.bfloat16) * 0.05)(D, D)),
        }
    _A_in = _A_setup()

    def _B_setup():
        B, S, D, F = 1, 2048, 1024, 3584
        return {
            "x": _w((lambda *s: torch.randn(*s, dtype=torch.bfloat16) * 0.05)(B, S, D)),
            "w_gate": _w((lambda *s: torch.randn(*s, dtype=torch.bfloat16) * 0.05)(D, F)),
            "w_up": _w((lambda *s: torch.randn(*s, dtype=torch.bfloat16) * 0.05)(D, F)),
            "w_down": _w((lambda *s: torch.randn(*s, dtype=torch.bfloat16) * 0.05)(F, D)),
        }
    _B_in = _B_setup()

    return {
        "A_x": _A_in["x"],
        "A_wqkv": _A_in["wqkv"],
        "A_w_phi": _A_in["w_phi"],
        "A_wo": _A_in["wo"],
        "B_x": _B_in["x"],
        "B_w_gate": _B_in["w_gate"],
        "B_w_up": _B_in["w_up"],
        "B_w_down": _B_in["w_down"],
    }

def reference(A_x, A_wqkv, A_w_phi, A_wo, B_x, B_w_gate, B_w_up, B_w_down):
    def _A_ref(x, wqkv, w_phi, wo):
        B, S, D = x.shape
        H = 32; Dh = D // H
        qkv = (lambda x, *d: x.permute(*d))((x @ wqkv).reshape(B, S, 3, H, Dh), 2, 0, 3, 1, 4)
        q, k, v = qkv[0], qkv[1], qkv[2]
        pq = torch.einsum("bhsd,hdf->bhsf", q, w_phi)
        pk = torch.einsum("bhsd,hdf->bhsf", k, w_phi)
        phi_q = torch.concatenate([_softmax(pq, axis=-1), _softmax(-pq, axis=-1)], axis=-1)
        phi_k = torch.concatenate([_softmax(pk, axis=-1), _softmax(-pk, axis=-1)], axis=-1)
        kv = torch.einsum("bhsf,bhsd->bhfd", phi_k, v)
        z  = torch.einsum("bhsf->bhf", phi_k)
        num = torch.einsum("bhqf,bhfd->bhqd", phi_q, kv)
        den = torch.einsum("bhqf,bhf->bhq", phi_q, z)[..., None] + 1e-6
        o = num / den
        return (lambda x, *d: x.permute(*d))(o, 0, 2, 1, 3).reshape(B, S, D) @ wo

    def _B_ref(x, w_gate, w_up, w_down):
        g = torch.einsum("bsd,df->bsf", x, w_gate)
        u = torch.einsum("bsd,df->bsf", x, w_up)
        h = _sigmoid(g) * u
        return torch.einsum("bsf,fd->bsd", h, w_down)

    x = A_x
    xn = _layernorm(x, axis=-1)
    s = _A_ref(xn, A_wqkv, A_w_phi, A_wo)
    g = _silu(_B_ref(xn, B_w_gate, B_w_up, B_w_down))
    return x + s * g
```

```cpp
#include <hip/hip_runtime.h>
#include <hip/hip_bf16.h>
typedef unsigned short u16;
typedef short bf16x8 __attribute__((ext_vector_type(8)));
typedef float f32x4 __attribute__((ext_vector_type(4)));
#define DI __device__ __forceinline__
DI float bf2f(u16 b){ return __uint_as_float(((unsigned)b)<<16); }
DI u16 f2bf(float f){ unsigned u=__float_as_uint(f); u += 0x7fffu + ((u>>16)&1u); return (u16)(u>>16); }
constexpr int S=2048, D=1024, H=32, DH=32, F=3584;

__global__ __launch_bounds__(256) void k_ln(const u16* x, u16* xn){
  int row = blockIdx.x*4 + (threadIdx.x>>6), lane = threadIdx.x&63;
  const u16* p = x + (size_t)row*D;
  float v[16]; float s=0;
  for(int i=0;i<2;i++){ bf16x8 t = *(const bf16x8*)(p + i*512 + lane*8); for(int j=0;j<8;j++){ v[i*8+j]=bf2f((u16)t[j]); s+=v[i*8+j]; } }
  for(int o=32;o>=1;o>>=1) s += __shfl_xor(s,o);
  float m = s*(1.0f/D), q=0;
  for(int i=0;i<16;i++){ float d=v[i]-m; q+=d*d; }
  for(int o=32;o>=1;o>>=1) q += __shfl_xor(q,o);
  float r = 1.0f/sqrtf(q*(1.0f/D)+1e-5f);
  for(int i=0;i<2;i++){ bf16x8 t; for(int j=0;j<8;j++) t[j]=(short)f2bf((v[i*8+j]-m)*r); *(bf16x8*)(xn+(size_t)row*D+i*512+lane*8)=t; }
}
__global__ __launch_bounds__(256) void k_gemm(const u16* A, const u16* B, float* C, int M, int N, int K){
  __shared__ u16 sA[64][40]; __shared__ u16 sB[64][40];
  int tid=threadIdx.x, lane=tid&63, w=tid>>6, wr=w>>1, wc=w&1;
  int m0=blockIdx.y*64, n0=blockIdx.x*64;
  f32x4 acc[2][2]={};
  for(int k0=0;k0<K;k0+=32){
    { int r=tid>>2, c=(tid&3)*8; *(bf16x8*)&sA[r][c] = *(const bf16x8*)(A+(size_t)(m0+r)*K+k0+c); }
    { int k=tid>>3, c=(tid&7)*8; bf16x8 t=*(const bf16x8*)(B+(size_t)(k0+k)*N+n0+c); for(int j=0;j<8;j++) sB[c+j][k]=(u16)t[j]; }
    __syncthreads();
    bf16x8 a[2], b[2];
    for(int i=0;i<2;i++){ a[i]=*(const bf16x8*)&sA[wr*32+i*16+(lane&15)][(lane>>4)*8]; b[i]=*(const bf16x8*)&sB[wc*32+i*16+(lane&15)][(lane>>4)*8]; }
    for(int i=0;i<2;i++) for(int j=0;j<2;j++) acc[i][j]=__builtin_amdgcn_mfma_f32_16x16x32_bf16(a[i],b[j],acc[i][j],0,0,0);
    __syncthreads();
  }
  for(int i=0;i<2;i++) for(int j=0;j<2;j++) for(int r=0;r<4;r++)
    C[(size_t)(m0+wr*32+i*16+(lane>>4)*4+r)*N + n0+wc*32+j*16+(lane&15)] = acc[i][j][r];
}
__global__ void k_glu(const float* g, const float* u, u16* h, int n){
  int i=blockIdx.x*blockDim.x+threadIdx.x; if(i<n){ float s=1.0f/(1.0f+__expf(-g[i])); h[i]=f2bf(s*u[i]); }
}
__global__ void k_phi(const float* qkv, const u16* wphi, u16* phi  ){
  int i=blockIdx.x*blockDim.x+threadIdx.x; int s=i%S, h=(i/S)%H, wch=i/(S*H);
  float q[32]; for(int d=0;d<32;d++) q[d]=bf2f(f2bf(qkv[(size_t)s*3*D+wch*D+h*32+d]));
  float p[32]; float mx=-1e30f, mn=1e30f;
  for(int f=0;f<32;f++){ float a=0; for(int d=0;d<32;d++) a+=q[d]*bf2f(wphi[h*1024+d*32+f]); p[f]=a; mx=fmaxf(mx,a); mn=fminf(mn,a);}
  float s1=0,s2=0; for(int f=0;f<32;f++){ s1+=__expf(p[f]-mx); s2+=__expf(mn-p[f]); }
  u16* o=phi+(((size_t)wch*H+h)*S+s)*64;
  for(int f=0;f<32;f++){ o[f]=f2bf(__expf(p[f]-mx)/s1); o[32+f]=f2bf(__expf(mn-p[f])/s2); }
}
__global__ void k_kv(const float* qkv, const u16* phi, float* kv){
  int h=blockIdx.x; int f=blockIdx.y; int d=threadIdx.x;
  const u16* pk=phi+((size_t)H+h)*S*64; float a=0;
  if(d<=32){ for(int s=0;s<S;s++){ float v = d<32? bf2f(f2bf(qkv[(size_t)s*3*D+2*D+h*32+d])):1.0f; a+=bf2f(pk[s*64+f])*v; } kv[(h*64+f)*33+d]=a; }
}
__global__ void k_o(const u16* phi, const float* kv, u16* o){
  int i=blockIdx.x*blockDim.x+threadIdx.x; int d=i&31, h=(i>>5)&31, s=i>>10;
  const u16* pq=phi+((size_t)h*S+s)*64; float n=0,z=0;
  for(int f=0;f<64;f++){ float p=bf2f(pq[f]); n+=p*bf2f(f2bf(kv[(h*64+f)*33+d])); z+=p*bf2f(f2bf(kv[(h*64+f)*33+32])); }
  o[i]=f2bf(n/(z+1e-6f));
}
__global__ void k_fin(const u16* x, const float* s, const float* dn, u16* out, int n){
  int i=blockIdx.x*blockDim.x+threadIdx.x; if(i<n){ float d=dn[i]; float g=d/(1.0f+__expf(-d)); out[i]=f2bf(bf2f(x[i])+s[i]*g); }
}
extern "C" void kernel_launch(void* const* d_in, const int* in_sizes, int n_in, void* d_out, int out_size, void* d_ws, size_t ws_size, hipStream_t stream){
  const u16* x=(const u16*)d_in[0]; const u16* wqkv=(const u16*)d_in[1]; const u16* wphi=(const u16*)d_in[2]; const u16* wo=(const u16*)d_in[3];
  const u16* wg=(const u16*)d_in[5]; const u16* wu=(const u16*)d_in[6]; const u16* wd=(const u16*)d_in[7];
  char* ws=(char*)d_ws; size_t off=0; auto al=[&](size_t b){ void* p=ws+off; off+=(b+255)&~(size_t)255; return p; };
  u16* xn=(u16*)al((size_t)S*D*2); float* qkv=(float*)al((size_t)S*3*D*4); float* g=(float*)al((size_t)S*F*4); float* u=(float*)al((size_t)S*F*4);
  u16* h=(u16*)al((size_t)S*F*2); u16* phi=(u16*)al((size_t)2*H*S*64*2); float* kv=(float*)al(H*64*33*4); u16* o=(u16*)al((size_t)S*D*2);
  float* s=(float*)al((size_t)S*D*4); float* dn=(float*)al((size_t)S*D*4);
  k_ln<<<S/4,256,0,stream>>>(x,xn);
  k_gemm<<<dim3(3*D/64,S/64),256,0,stream>>>(xn,wqkv,qkv,S,3*D,D);
  k_gemm<<<dim3(F/64,S/64),256,0,stream>>>(xn,wg,g,S,F,D);
  k_gemm<<<dim3(F/64,S/64),256,0,stream>>>(xn,wu,u,S,F,D);
  k_glu<<<S*F/256,256,0,stream>>>(g,u,h,S*F);
  k_phi<<<2*H*S/256,256,0,stream>>>(qkv,wphi,phi);
  k_kv<<<dim3(H,64),64,0,stream>>>(qkv,phi,kv);
  k_o<<<S*D/256,256,0,stream>>>(phi,kv,o);
  k_gemm<<<dim3(D/64,S/64),256,0,stream>>>(o,wo,s,S,D,D);
  k_gemm<<<dim3(D/64,S/64),256,0,stream>>>(h,wd,dn,S,D,F);
  k_fin<<<S*D/256,256,0,stream>>>(x,s,dn,(u16*)d_out,S*D);
}
```

```cpp
#include <hip/hip_runtime.h>
typedef unsigned u32; typedef unsigned short u16; typedef unsigned long long u64;
typedef u32 u32x2 __attribute__((ext_vector_type(2)));
typedef u32 u32x4 __attribute__((ext_vector_type(4)));
typedef u32 u32x8 __attribute__((ext_vector_type(8)));
typedef u32 u32x16 __attribute__((ext_vector_type(16)));
#define DI __device__ __forceinline__
constexpr u32 WS_XN=0, WS_WPHI=0x400000, WS_FLAG=0x420000, WS_Z=0x480000, WS_KVP=0x500000, WS_O=0x800000, WS_H=0xC00000, WS_DP=0x1A00000, WS_END=0x2200000;
DI float bf2f(u32 v){ return __uint_as_float(v<<16); }
DI u32 pk2(float a,float b){ u32 r; asm("v_cvt_pk_bf16_f32 %0, %1, %2":"=v"(r):"v"(a),"v"(b)); return r; }
DI u32 rfl(u32 x){ u32 y=__builtin_amdgcn_readfirstlane(x); asm("":"+s"(y)); return y; }
DI u32x4 mksrd(const void* p, long off, u32 nrec){ u64 a=(u64)p+off; u32x4 r; r[0]=rfl((u32)a); r[1]=rfl((u32)(a>>32)&0xffff); r[2]=rfl(nrec); r[3]=0x00020000; return r; }
DI u32 dsb_full(int l,int c){ int g=l>>4,q=(l&15)>>2,p=l&3,r=4*(g&1)+q; return (u32)((g>>1)*5120+r*128+((c^((r>>1)&3))*32)+p*8); }
DI u32 dsb_half(int l,int cb){ int g=l>>4,q=(l&15)>>2,p=l&3; return (u32)((4*g+q)*64+((cb^(g&1))*32)+p*8); }

constexpr u32 WS_KV2=0x700000, LREG=61440, BIG=0x70000000u;
DI u32 shd(u32 r){ r&=7; return 0x2400000-384+128*(r==3?5:r); }
DI void put4(u32x16& d,int k,u32x4 s){ d[4*k]=s[0]; d[4*k+1]=s[1]; d[4*k+2]=s[2]; d[4*k+3]=s[3]; }
__global__ __launch_bounds__(256,1) void k_f(const u16* __restrict__ x,const u16* __restrict__ wqkv,const u16* __restrict__ wo,const u16* __restrict__ wg,const u16* __restrict__ wu,const u16* __restrict__ wd,u16* out,char* ws,const u16* __restrict__ wphi){
  extern __shared__ __attribute__((aligned(16))) char lds[];

  const int b=blockIdx.x, xcd=b&7, idx=b>>3, p=idx&7, j=4*xcd+(idx>>3), i=j&15;
  u32x16 xr; u32x8 ph; u32 wv; u32x2 pf;
  { const u32 t=threadIdx.x, o0=(256*p+8*j+(t&7))*2048+(t>>3)*16+(t>>6)*384, o2=i*4096+(t>>7)*2048+(t&64)/2+(t&15)*2+(t&48)*16;
    asm volatile("global_load_dwordx4 v[96:99], v32, %[x] offset:0\nglobal_load_dwordx4 v[100:103], v32, %[x] offset:128\nglobal_load_dwordx4 v[104:107], v32, %[x] offset:256\nglobal_load_dwordx4 v[108:111], v32, %[x] offset:384\nglobal_load_dword v136, v33, %[w] sc1\nglobal_load_ushort v128, v34, %[h] offset:0\nglobal_load_ushort v129, v34, %[h] offset:64\nglobal_load_ushort v130, v34, %[h] offset:128\n"
"global_load_ushort v131, v34, %[h] offset:192\nglobal_load_ushort v132, v34, %[h] offset:1024\nglobal_load_ushort v133, v34, %[h] offset:1088\nglobal_load_ushort v134, v34, %[h] offset:1152\nglobal_load_ushort v135, v34, %[h] offset:1216\ns_getpc_b64 s[20:21]\nLE0:\nv_lshlrev_b32 v141, 5, %[t]\n"
"v_and_b32 v141, 0xffffff80, v141\nglobal_load_dword v140, v141, s[20:21]\ns_bitcmp1_b32 %[b], 2\ns_mov_b32 s22, LQ0-LE0\ns_cmov_b32 s22, LKV0-LE0\ns_add_u32 s20, s20, s22\ns_addc_u32 s21, s21, 0\nglobal_load_dword v141, v141, s[20:21]\n":"={v[96:111]}"(xr),"={v[128:135]}"(ph),"={v136}"(wv),"={v[140:141]}"(pf):[x]"s"(x),[w]"s"(ws+0x42f000),[h]"s"(wphi),"{v32}"(o0),"{v33}"(0),"{v34}"(o2),[t]"{v0}"(t),[b]"s"(b):"memory","s20","s21","s22","scc"); }
  __builtin_amdgcn_sched_barrier(0);
  const int w=rfl(threadIdx.x>>6), rg=4*p+w, l=threadIdx.x&63, g=l>>4;
  const int r=l>>3, pos=l&7; const u32 ch=(u32)(pos^(((r>>1)&3)<<1))*16, row=8*w+r;
  u32x16 g2s, g1s; u32x8 g2i, g1t, in1, dsb; u32x4 in0, in2, ax, s4, go, go2, sws=mksrd(ws,0,WS_END+0x2400000); u32x2 in3, vb, o01; u32 o4;
  const bool q=j<16; const int NG=1024*7168, NQ=1024*6144;
  const int iu=(i+4)&15, i3=(i+((0x84c8>>(i&12))&15))&15;
  { int o0=q?256*i:4096+128*iu, o1=o0-1024, o2=q?256*i+128-2048:128*i, o3=q?256*i+128-3072:4096+128*i-3072, o4=q?128*i:6144+64*i3;
    put4(g1s,0,mksrd(wg,o0,NG-o0)); put4(g1s,1,mksrd(wu,o1,NG-o1)); put4(g1s,2,mksrd(q?wg:wqkv,o2,(q?NG:NQ)-o2)); put4(g1s,3,mksrd(q?wu:wqkv,o3,(q?NG:NQ)-o3));
    s4=mksrd(q?wqkv:((w&1)?wu:wg),o4,(q?NQ:NG)-o4); }
  { const int r2=l>>2,pos2=l&3; go[1]=row*(q?7168:6144)+ch; go[2]=q? row*6144+ch : (u32)(16*(w>>1)+r2)*7168+(u32)(pos2^(((r2>>2)&1)<<1))*16; }
  go[0]=row*7168+ch; go[3]=l*16;
  in0[0]=rfl(q?32*7168:32*6144); in0[1]=rfl(q?32*6144:32*7168); in0[2]=rfl(w*5120); in0[3]=rfl(WS_XN+rg*32*4096);
  for(int k=0;k<4;k++){ g1t[k]=s4[k]; g1t[4+k]=sws[k]; }
  const u32 sph=rfl(WS_END+b*4096), ln1=WS_XN+((4*p+(j>>3))*128+((j>>1)&3))*1024+(8*w+(l>>5))*4096+((2*((l>>3)&1)+((l>>4)&1))*16+8*(j&1)+(l&7))*16, ln3=81920+(l&7)*32+w*8;
  const u32x4 spf= w==3? mksrd(wd,b*28672,3584) : mksrd(w==0?wqkv:w==1?wg:wu,(4*256+b)*(w==0?1536:1792),(w==0?1536:1792));

  asm volatile(
"s_mov_b32 s64, 0\ns_mov_b32 s65, 0\ns_mov_b32 s66, 0\ns_add_u32 m0, s62, 0\ns_nop 0\nbuffer_load_dwordx4 v20, s[36:39], s64 offen offset:0 lds\nbuffer_load_dwordx4 v20, s[40:43], s64 offen offset:1024 lds\ns_add_u32 s64, s64, 229376\n"
"buffer_load_dwordx4 v21, s[44:47], s65 offen offset:2048 lds\nbuffer_load_dwordx4 v21, s[48:51], s65 offen offset:3072 lds\ns_add_u32 s65, s65, s60\ns_add_u32 m0, s62, 4096\ns_nop 0\nbuffer_load_dwordx4 v22, s[52:55], s66 offen offset:0 lds\ns_add_u32 s66, s66, s61\ns_waitcnt vmcnt(19)\n"
"v_lshlrev_b32 v64, 16, v96\nv_and_b32 v65, 0xffff0000, v96\nv_lshlrev_b32 v66, 16, v97\nv_and_b32 v67, 0xffff0000, v97\nv_lshlrev_b32 v68, 16, v98\nv_and_b32 v69, 0xffff0000, v98\nv_lshlrev_b32 v70, 16, v99\nv_and_b32 v71, 0xffff0000, v99\n"
"s_waitcnt vmcnt(18)\nv_lshlrev_b32 v72, 16, v100\nv_and_b32 v73, 0xffff0000, v100\nv_lshlrev_b32 v74, 16, v101\nv_and_b32 v75, 0xffff0000, v101\nv_lshlrev_b32 v76, 16, v102\nv_and_b32 v77, 0xffff0000, v102\ns_add_u32 m0, s62, 20480\n"
"s_nop 0\nbuffer_load_dwordx4 v20, s[36:39], s64 offen offset:0 lds\nv_lshlrev_b32 v78, 16, v103\nv_and_b32 v79, 0xffff0000, v103\ns_waitcnt vmcnt(18)\nv_lshlrev_b32 v80, 16, v104\nv_and_b32 v81, 0xffff0000, v104\nv_lshlrev_b32 v82, 16, v105\n"
"v_and_b32 v83, 0xffff0000, v105\nv_lshlrev_b32 v84, 16, v106\nv_and_b32 v85, 0xffff0000, v106\nv_lshlrev_b32 v86, 16, v107\nv_and_b32 v87, 0xffff0000, v107\ns_waitcnt vmcnt(17)\nv_lshlrev_b32 v88, 16, v108\nv_and_b32 v89, 0xffff0000, v108\n"
"v_lshlrev_b32 v90, 16, v109\nv_and_b32 v91, 0xffff0000, v109\nbuffer_load_dwordx4 v20, s[40:43], s64 offen offset:1024 lds\ns_add_u32 s64, s64, 229376\nv_lshlrev_b32 v92, 16, v110\nv_and_b32 v93, 0xffff0000, v110\nv_lshlrev_b32 v94, 16, v111\nv_and_b32 v95, 0xffff0000, v111\n"
"v_pk_add_f32 v[112:113], v[64:65], v[66:67]\nv_pk_add_f32 v[114:115], v[68:69], v[70:71]\nv_pk_add_f32 v[116:117], v[72:73], v[74:75]\nv_pk_add_f32 v[118:119], v[76:77], v[78:79]\nv_pk_add_f32 v[120:121], v[80:81], v[82:83]\nv_pk_add_f32 v[122:123], v[84:85], v[86:87]\nv_pk_add_f32 v[124:125], v[88:89], v[90:91]\nv_pk_add_f32 v[126:127], v[92:93], v[94:95]\n"
"v_pk_add_f32 v[112:113], v[112:113], v[114:115]\nv_pk_add_f32 v[116:117], v[116:117], v[118:119]\nbuffer_load_dwordx4 v21, s[44:47], s65 offen offset:2048 lds\nv_pk_add_f32 v[120:121], v[120:121], v[122:123]\nv_pk_add_f32 v[124:125], v[124:125], v[126:127]\nv_pk_add_f32 v[112:113], v[112:113], v[116:117]\nv_pk_add_f32 v[120:121], v[120:121], v[124:125]\nv_pk_add_f32 v[112:113], v[112:113], v[120:121]\n"
"v_add_f32 v112, v112, v113\ns_nop 1\nv_add_f32_dpp v112, v112, v112 row_ror:8 row_mask:0xf bank_mask:0xf\nv_mov_b32 v114, v112\ns_nop 1\nv_permlane16_swap_b32 v112, v114\nv_add_f32 v112, v112, v114\nv_mov_b32 v114, v112\n"
"s_nop 1\nv_permlane32_swap_b32 v112, v114\nv_add_f32 v112, v112, v114\nv_mul_f32 v112, 0xbb800000, v112\nv_pk_add_f32 v[64:65], v[64:65], v[112:113] op_sel:[0,0] op_sel_hi:[1,0]\nv_pk_add_f32 v[66:67], v[66:67], v[112:113] op_sel:[0,0] op_sel_hi:[1,0]\nv_pk_add_f32 v[68:69], v[68:69], v[112:113] op_sel:[0,0] op_sel_hi:[1,0]\nv_pk_add_f32 v[70:71], v[70:71], v[112:113] op_sel:[0,0] op_sel_hi:[1,0]\n"
"v_pk_add_f32 v[72:73], v[72:73], v[112:113] op_sel:[0,0] op_sel_hi:[1,0]\nv_pk_add_f32 v[74:75], v[74:75], v[112:113] op_sel:[0,0] op_sel_hi:[1,0]\nv_pk_add_f32 v[76:77], v[76:77], v[112:113] op_sel:[0,0] op_sel_hi:[1,0]\nv_pk_add_f32 v[78:79], v[78:79], v[112:113] op_sel:[0,0] op_sel_hi:[1,0]\nv_pk_add_f32 v[80:81], v[80:81], v[112:113] op_sel:[0,0] op_sel_hi:[1,0]\nv_pk_add_f32 v[82:83], v[82:83], v[112:113] op_sel:[0,0] op_sel_hi:[1,0]\nv_pk_add_f32 v[84:85], v[84:85], v[112:113] op_sel:[0,0] op_sel_hi:[1,0]\nv_pk_add_f32 v[86:87], v[86:87], v[112:113] op_sel:[0,0] op_sel_hi:[1,0]\n"
"v_pk_add_f32 v[88:89], v[88:89], v[112:113] op_sel:[0,0] op_sel_hi:[1,0]\nv_pk_add_f32 v[90:91], v[90:91], v[112:113] op_sel:[0,0] op_sel_hi:[1,0]\nv_pk_add_f32 v[92:93], v[92:93], v[112:113] op_sel:[0,0] op_sel_hi:[1,0]\nv_pk_add_f32 v[94:95], v[94:95], v[112:113] op_sel:[0,0] op_sel_hi:[1,0]\nv_pk_mul_f32 v[116:117], v[64:65], v[64:65]\nv_pk_mul_f32 v[118:119], v[66:67], v[66:67]\nv_pk_fma_f32 v[116:117], v[68:69], v[68:69], v[116:117]\nv_pk_fma_f32 v[118:119], v[70:71], v[70:71], v[118:119]\n"
"v_pk_fma_f32 v[116:117], v[72:73], v[72:73], v[116:117]\nv_pk_fma_f32 v[118:119], v[74:75], v[74:75], v[118:119]\nv_pk_fma_f32 v[116:117], v[76:77], v[76:77], v[116:117]\nv_pk_fma_f32 v[118:119], v[78:79], v[78:79], v[118:119]\nv_pk_fma_f32 v[116:117], v[80:81], v[80:81], v[116:117]\nv_pk_fma_f32 v[118:119], v[82:83], v[82:83], v[118:119]\nv_pk_fma_f32 v[116:117], v[84:85], v[84:85], v[116:117]\nv_pk_fma_f32 v[118:119], v[86:87], v[86:87], v[118:119]\n"
"v_pk_fma_f32 v[116:117], v[88:89], v[88:89], v[116:117]\nv_pk_fma_f32 v[118:119], v[90:91], v[90:91], v[118:119]\nv_pk_fma_f32 v[116:117], v[92:93], v[92:93], v[116:117]\nv_pk_fma_f32 v[118:119], v[94:95], v[94:95], v[118:119]\nv_pk_add_f32 v[116:117], v[116:117], v[118:119]\nv_add_f32 v113, v116, v117\ns_nop 1\nv_add_f32_dpp v113, v113, v113 row_ror:8 row_mask:0xf bank_mask:0xf\n"
"v_mov_b32 v114, v113\ns_nop 1\nv_permlane16_swap_b32 v113, v114\nv_add_f32 v113, v113, v114\nv_mov_b32 v114, v113\ns_nop 1\nv_permlane32_swap_b32 v113, v114\nv_add_f32 v113, v113, v114\n"
"ds_write_b64 v35, v[112:113]\nv_and_b32 v114, 0xffffffe0, v35\ns_waitcnt lgkmcnt(0)\ns_barrier\nds_read_b128 v[120:123], v114 offset:0\nds_read_b128 v[124:127], v114 offset:16\ns_lshr_b32 s87, s62, 10\ns_mul_i32 s87, s87, 205\n"
"s_and_b32 s87, s87, 0xc00\ns_add_u32 s87, s87, s33\ns_mov_b32 s88, 0\ns_mov_b32 s89, 8192\ns_mov_b32 s90, 16384\ns_mov_b32 s91, 24576\ns_waitcnt lgkmcnt(1)\nv_pk_add_f32 v[116:117], v[120:121], v[122:123]\n"
"s_waitcnt lgkmcnt(0)\nv_pk_add_f32 v[118:119], v[124:125], v[126:127]\nv_pk_add_f32 v[116:117], v[116:117], v[118:119]\nv_mul_f32 v116, 0x3e800000, v116\nv_sub_f32 v120, v120, v116\nv_sub_f32 v122, v122, v116\nv_sub_f32 v124, v124, v116\nv_sub_f32 v126, v126, v116\n"
"v_mul_f32 v118, v120, v120\nv_fmac_f32 v118, v122, v122\nv_fmac_f32 v118, v124, v124\nv_fmac_f32 v118, v126, v126\nv_mul_f32 v117, 0x3a800000, v117\nv_add_f32 v117, 0x3727c5ac, v117\nv_fmac_f32 v117, 0x3e800000, v118\nv_rsq_f32 v117, v117\n"
"v_sub_f32 v116, v116, v112\nv_mul_f32 v116, v116, v117\nv_pk_fma_f32 v[64:65], v[64:65], v[116:117], v[116:117] op_sel:[0,1,0] op_sel_hi:[1,1,0]\nv_cvt_pk_bf16_f32 v96, v64, v65\nv_pk_fma_f32 v[66:67], v[66:67], v[116:117], v[116:117] op_sel:[0,1,0] op_sel_hi:[1,1,0]\nv_cvt_pk_bf16_f32 v97, v66, v67\nv_pk_fma_f32 v[68:69], v[68:69], v[116:117], v[116:117] op_sel:[0,1,0] op_sel_hi:[1,1,0]\nv_cvt_pk_bf16_f32 v98, v68, v69\n"
"v_pk_fma_f32 v[70:71], v[70:71], v[116:117], v[116:117] op_sel:[0,1,0] op_sel_hi:[1,1,0]\nv_cvt_pk_bf16_f32 v99, v70, v71\nv_permlane16_swap_b32 v96, v98\nv_pk_fma_f32 v[72:73], v[72:73], v[116:117], v[116:117] op_sel:[0,1,0] op_sel_hi:[1,1,0]\nv_permlane16_swap_b32 v97, v99\nv_cvt_pk_bf16_f32 v100, v72, v73\nv_pk_fma_f32 v[74:75], v[74:75], v[116:117], v[116:117] op_sel:[0,1,0] op_sel_hi:[1,1,0]\nv_cvt_pk_bf16_f32 v101, v74, v75\n"
"v_pk_fma_f32 v[76:77], v[76:77], v[116:117], v[116:117] op_sel:[0,1,0] op_sel_hi:[1,1,0]\nv_cvt_pk_bf16_f32 v102, v76, v77\nv_pk_fma_f32 v[78:79], v[78:79], v[116:117], v[116:117] op_sel:[0,1,0] op_sel_hi:[1,1,0]\nv_cvt_pk_bf16_f32 v103, v78, v79\nv_permlane16_swap_b32 v100, v102\nv_pk_fma_f32 v[80:81], v[80:81], v[116:117], v[116:117] op_sel:[0,1,0] op_sel_hi:[1,1,0]\nv_permlane16_swap_b32 v101, v103\nv_cvt_pk_bf16_f32 v104, v80, v81\n"
"v_pk_fma_f32 v[82:83], v[82:83], v[116:117], v[116:117] op_sel:[0,1,0] op_sel_hi:[1,1,0]\nv_cvt_pk_bf16_f32 v105, v82, v83\nv_pk_fma_f32 v[84:85], v[84:85], v[116:117], v[116:117] op_sel:[0,1,0] op_sel_hi:[1,1,0]\nv_cvt_pk_bf16_f32 v106, v84, v85\nv_pk_fma_f32 v[86:87], v[86:87], v[116:117], v[116:117] op_sel:[0,1,0] op_sel_hi:[1,1,0]\nv_cvt_pk_bf16_f32 v107, v86, v87\nv_permlane16_swap_b32 v104, v106\nv_pk_fma_f32 v[88:89], v[88:89], v[116:117], v[116:117] op_sel:[0,1,0] op_sel_hi:[1,1,0]\n"
"v_permlane16_swap_b32 v105, v107\nv_cvt_pk_bf16_f32 v108, v88, v89\nv_pk_fma_f32 v[90:91], v[90:91], v[116:117], v[116:117] op_sel:[0,1,0] op_sel_hi:[1,1,0]\nv_cvt_pk_bf16_f32 v109, v90, v91\nv_pk_fma_f32 v[92:93], v[92:93], v[116:117], v[116:117] op_sel:[0,1,0] op_sel_hi:[1,1,0]\nv_cvt_pk_bf16_f32 v110, v92, v93\nv_pk_fma_f32 v[94:95], v[94:95], v[116:117], v[116:117] op_sel:[0,1,0] op_sel_hi:[1,1,0]\nv_cvt_pk_bf16_f32 v111, v94, v95\n"
"v_permlane16_swap_b32 v108, v110\ns_nop 0\nv_permlane16_swap_b32 v109, v111\ns_waitcnt vmcnt(16)\nv_lshl_or_b32 v128, v129, 16, v128\ns_waitcnt vmcnt(14)\nv_lshl_or_b32 v129, v131, 16, v130\ns_waitcnt vmcnt(12)\n"
"v_lshl_or_b32 v130, v133, 16, v132\ns_waitcnt vmcnt(10)\nv_lshl_or_b32 v131, v135, 16, v134\nbuffer_store_dwordx4 v[128:131], v23, s[56:59], s87 offen sc1\ns_cmp_lg_u32 s62, 0\ns_cbranch_scc1 47f\nbuffer_store_dwordx4 v[96:99], v33, s[56:59], s88 offen sc1\nbuffer_store_dwordx4 v[100:103], v33, s[56:59], s89 offen sc1\n"
"buffer_store_dwordx4 v[104:107], v33, s[56:59], s90 offen sc1\nbuffer_store_dwordx4 v[108:111], v33, s[56:59], s91 offen sc1\n47:\n"
  :"+{v[96:111]}"(xr),"+{v[128:135]}"(ph),"={s[64:65]}"(o01),"={s66}"(o4) : "{s[36:51]}"(g1s),"{s[52:59]}"(g1t),"{s[60:63]}"(in0),"{v[20:23]}"(go),"{v33}"(ln1),"{v35}"(ln3),"{s33}"(sph),"{s[0:3]}"(spf) : "v64","v65","v66","v67","v68","v69","v70","v71","v72","v73","v74","v75","v76","v77","v78","v79","v80","v81","v82","v83","v84","v85","v86","v87","v88","v89","v90","v91","v92","v93","v94","v95","v112","v113","v114","v115","v116","v117","v118","v119","v120","v121","v122","v123","v124","v125","v126","v127","s86","s87","s88","s89","s90","s91","s92","s93","s94","s95","scc","vcc","memory");
  __builtin_amdgcn_sched_barrier(0);
  const int c=xcd>>2, rt=(xcd&3)*4+(idx>>3), ct=idx&7, rg2=2*rt+(w>>1), cc=c;


  put4(g2s,0,mksrd(wd,256*ct,3584*2048-256*ct)); put4(g2s,1,mksrd(wo,256*ct,1024*2048-256*ct)); put4(g2s,2,mksrd(x,0,2048*2048)); put4(g2s,3,mksrd(out,0,2048*2048));
  g2i[0]=rfl(WS_H+rg2*112*4096+(w&1)*2048); g2i[1]=rfl(WS_O+rg2*32*4096+(w&1)*2048); g2i[2]=rfl(WS_DP+(b*4+w)*8192); g2i[3]=rfl(WS_DP+((b^4)*4+w)*8192);
  g2i[4]=rfl((128*rt+32*w)*2048+(128*ct+64*cc)*2); g2i[5]=sph; g2i[6]=1; g2i[7]=rfl((idx*4+w)*1024);
  for(int k=0;k<4;k++) dsb[k]=dsb_full(l,k);
  dsb[4]=dsb_half(l,0); dsb[5]=dsb_half(l,1); dsb[6]=0; dsb[7]=0;
  vb[0]=row*2048+ch+128*cc; vb[1]=row*2048+ch+128*(1-cc);
  in1[4]=rfl(WS_FLAG+p*256+j*4); in1[5]=rfl(WS_FLAG+(rt>>1)*256); in1[6]=rfl(WS_FLAG+0x1000+i*128); in1[7]=rfl(WS_FLAG+0x2000+b*128);
  in3[0]=rfl(WS_FLAG+0x1000+i*128+4*p); in3[1]=rfl(j<16);
  { const int B=2*p+(w>>1); in2[0]=rfl(WS_KVP+i*131072+(4*p+w)*512); in2[1]=rfl(WS_KV2+(i*4+(B>>2))*4096+(B&1)*1024+(w&1)*512+((B>>1)&1)*8);
    in2[2]=rfl(WS_Z+i*4096+p*64); in2[3]=rfl(WS_KV2+(i*4+(p>>1))*4096+2048+(p&1)*8); }
  ax[0]=(l&15)==0? (l>>4)*16 : BIG; ax[1]=(l&15)*2048+(16*(g&1)+8*(g>>1))*2; ax[2]=threadIdx.x==0? 0 : BIG; ax[3]=0;
  go2[0]=LREG+w*8192+(l&15)*32+(l>>4)*8; go2[1]=LREG+(w>>1)*32768+(w&1)*1024+l*8; go2[2]=LREG+65536+w*2048+l*16; go2[3]=LREG+65536+(w>>1)*16384+l*16;
  int ia=i; asm volatile("":"+s"(ia));
  in1[0]=rfl(WS_H+(rg*112+(q?4*ia:64+2*((ia+4)&15)))*4096); in1[1]=rfl(q? WS_O+(rg*32+2*ia)*4096 : WS_H+(rg*112+96+((ia+((0x84c8>>(ia&12))&15))&15))*4096);
  in1[2]=rfl(q? WS_KV2+i*16384 : WS_KVP+((i*8+p)*4+w)*4096); in1[3]=rfl(WS_Z+((i*8+p)*4+w)*128);
  asm volatile(
".macro lb v,r,s,o\nbuffer_load_dwordx4 v[\\v], s[\\r:\\r+3], s[\\s] offen offset:\\o lds\n.endm\n.macro sm o\ns_add_u32 m0, s62, \\o\n.endm\n.macro lc d,v,s,o\nbuffer_load_dwordx4 a[\\d:\\d+3], v[\\v], s[56:59], s[\\s] offen offset:\\o sc1\n"
".endm\n.macro dt d,a,o\nds_read_b64_tr_b16 v[\\d:\\d+1], v[\\a] offset:\\o\n.endm\n.macro wb n,k\ns_waitcnt vmcnt(\\n) lgkmcnt(\\k)\n.endm\n.macro mvva d,a,b\n"
"v_mfma_f32_16x16x32_bf16 v[\\d:\\d+3], v[\\a:\\a+3], a[\\b:\\b+3], v[\\d:\\d+3]\n.endm\n.macro wl n\ns_waitcnt lgkmcnt(\\n)\n.endm\n.macro mava d,a,b\nv_mfma_f32_16x16x32_bf16 a[\\d:\\d+3], v[\\a:\\a+3], a[\\b:\\b+3], a[\\d:\\d+3]\n.endm\n"
".macro ve d\nv_exp_f32 v[\\d], v[\\d]\n.endm\n.macro vr d\nv_rcp_f32 v[\\d], v[\\d]\n.endm\n.macro ar d,a\nv_accvgpr_read_b32 v[\\d], a[\\a]\n"
".endm\n.macro cv d,a,b\nv_cvt_pk_bf16_f32 v[\\d], v[\\a], v[\\b]\n.endm\n.macro zvav d,a,b\nv_mfma_f32_16x16x32_bf16 v[\\d:\\d+3], a[\\a:\\a+3], v[\\b:\\b+3], 0\n.endm\n.macro zvva d,a,b\n"
"v_mfma_f32_16x16x32_bf16 v[\\d:\\d+3], v[\\a:\\a+3], a[\\b:\\b+3], 0\n.endm\n.macro mvav d,a,b\nv_mfma_f32_16x16x32_bf16 v[\\d:\\d+3], a[\\a:\\a+3], v[\\b:\\b+3], v[\\d:\\d+3]\n.endm\n.macro zvvv d,a,b\nv_mfma_f32_16x16x32_bf16 v[\\d:\\d+3], v[\\a:\\a+3], v[\\b:\\b+3], 0\n.endm\n"
".macro mvvv d,a,b\nv_mfma_f32_16x16x32_bf16 v[\\d:\\d+3], v[\\a:\\a+3], v[\\b:\\b+3], v[\\d:\\d+3]\n.endm\ns_cmp_eq_u32 s71, 0\ns_cbranch_scc0 20f\ns_branch 22f\n20:\nLQ0:\n"
"s_lshr_b32 s87, s62, 10\ns_mul_i32 s87, s87, 205\ns_mov_b32 s88, 0\ns_mov_b32 s89, 8192\ns_mov_b32 s90, 16384\ns_mov_b32 s91, 24576\nv_readfirstlane_b32 s34, v136\ns_and_b32 s92, s76, 0x700\n"
"s_lshl_b32 s92, s92, 1\ns_add_u32 s92, s92, 0x42a000\ns_add_u32 s34, s34, 0x9e3779b1\ns_and_b32 s93, s76, 0x7c\ns_add_u32 s93, s93, s92\ns_lshr_b32 s86, s87, 3\ns_and_b32 s86, s86, 0x180\ns_add_u32 s93, s93, s86\n"
"lb 21,48,65,3072\ns_add_u32 s65, s65, s60\nv_accvgpr_write_b32 a224, v96\nv_accvgpr_write_b32 a225, v97\nv_accvgpr_write_b32 a226, v98\nv_accvgpr_write_b32 a227, v99\nv_accvgpr_write_b32 a228, v100\nv_accvgpr_write_b32 a229, v101\n"
"v_accvgpr_write_b32 a230, v102\nv_accvgpr_write_b32 a231, v103\nsm 24576\ns_nop 0\nlb 22,52,66,0\ns_add_u32 s66, s66, s61\nv_accvgpr_write_b32 a232, v104\nv_accvgpr_write_b32 a233, v105\n"
"v_accvgpr_write_b32 a234, v106\nv_accvgpr_write_b32 a235, v107\nv_accvgpr_write_b32 a236, v108\nv_accvgpr_write_b32 a237, v109\nv_accvgpr_write_b32 a238, v110\nv_accvgpr_write_b32 a239, v111\nv_accvgpr_write_b32 a0, 0\nv_accvgpr_write_b32 a1, 0\n"
"v_accvgpr_write_b32 a2, 0\nv_accvgpr_write_b32 a3, 0\nv_accvgpr_write_b32 a4, 0\nv_accvgpr_write_b32 a5, 0\nv_accvgpr_write_b32 a6, 0\nv_accvgpr_write_b32 a7, 0\nv_accvgpr_write_b32 a8, 0\nv_accvgpr_write_b32 a9, 0\n"
"v_accvgpr_write_b32 a10, 0\nv_accvgpr_write_b32 a11, 0\nv_accvgpr_write_b32 a12, 0\nv_accvgpr_write_b32 a13, 0\nv_accvgpr_write_b32 a14, 0\nv_accvgpr_write_b32 a15, 0\ns_cmp_lg_u32 s62, 0\ns_cbranch_scc1 48f\n"
"s_waitcnt vmcnt(2)\nv_mov_b32 v57, s34\nbuffer_store_dword v57, v30, s[56:59], s93 offen sc1\ns_mov_b32 s93, 0x70000000\n48:\nv_add_u32 v6, 65536, v0\nv_add_u32 v7, 65536, v1\nv_add_u32 v8, 65536, v2\n"
"v_add_u32 v9, 65536, v3\nv_add_u32 v10, 65536, v4\nv_add_u32 v11, 65536, v5\nv_add_u32 v12, 131072, v0\nv_add_u32 v13, 131072, v1\nv_add_u32 v14, 131072, v2\nv_add_u32 v15, 131072, v3\nv_add_u32 v16, 131072, v4\n"
"v_add_u32 v17, 131072, v5\ns_mov_b32 s67, s63\ns_mov_b32 s68, 3\ns_mov_b32 s80, 0x3fb8aa3b\ns_mov_b32 s81, 0x3fb8aa3b\ns_mov_b32 s82, 0xbfb8aa3b\ns_mov_b32 s83, 0xbfb8aa3b\ns_mov_b32 s84, 1.0\n"
"s_mov_b32 s85, 1.0\ns_cmp_eq_u32 s62, 15360\ns_cbranch_scc1 41f\nsm 40960\ns_nop 0\nlb 20,36,64,0\nlb 20,40,64,1024\ns_add_u32 s64, s64, 229376\n"
"lb 21,44,65,2048\nlb 21,48,65,3072\ns_add_u32 s65, s65, s60\nsm 45056\ns_nop 0\nlb 22,52,66,0\ns_add_u32 s66, s66, s61\nsm 61440\n"
"s_nop 0\nlb 20,36,64,0\nlb 20,40,64,1024\ns_add_u32 s64, s64, 229376\nlb 21,44,65,2048\nlb 21,48,65,3072\ns_add_u32 s65, s65, s60\nsm 65536\n"
"s_nop 0\nlb 22,52,66,0\ns_add_u32 s66, s66, s61\n41:\nv_accvgpr_write_b32 a16, 0\nv_accvgpr_write_b32 a17, 0\nv_accvgpr_write_b32 a18, 0\nv_accvgpr_write_b32 a19, 0\n"
"v_accvgpr_write_b32 a20, 0\nv_accvgpr_write_b32 a21, 0\nv_accvgpr_write_b32 a22, 0\nv_accvgpr_write_b32 a23, 0\nv_accvgpr_write_b32 a24, 0\nv_accvgpr_write_b32 a25, 0\nv_accvgpr_write_b32 a26, 0\nv_accvgpr_write_b32 a27, 0\n"
"v_accvgpr_write_b32 a28, 0\nv_accvgpr_write_b32 a29, 0\nv_accvgpr_write_b32 a30, 0\nv_accvgpr_write_b32 a31, 0\nv_accvgpr_write_b32 a32, 0\nv_accvgpr_write_b32 a33, 0\nv_accvgpr_write_b32 a34, 0\nv_accvgpr_write_b32 a35, 0\n"
"v_accvgpr_write_b32 a36, 0\nv_accvgpr_write_b32 a37, 0\nv_accvgpr_write_b32 a38, 0\nv_accvgpr_write_b32 a39, 0\nv_accvgpr_write_b32 a40, 0\nv_accvgpr_write_b32 a41, 0\nv_accvgpr_write_b32 a42, 0\nv_accvgpr_write_b32 a43, 0\n"
"v_accvgpr_write_b32 a44, 0\nv_accvgpr_write_b32 a45, 0\nv_accvgpr_write_b32 a46, 0\nv_accvgpr_write_b32 a47, 0\nv_accvgpr_write_b32 a48, 0\nv_accvgpr_write_b32 a49, 0\nv_accvgpr_write_b32 a50, 0\nv_accvgpr_write_b32 a51, 0\n"
"v_accvgpr_write_b32 a52, 0\nv_accvgpr_write_b32 a53, 0\nv_accvgpr_write_b32 a54, 0\nv_accvgpr_write_b32 a55, 0\nv_accvgpr_write_b32 a56, 0\nv_accvgpr_write_b32 a57, 0\nv_accvgpr_write_b32 a58, 0\nv_accvgpr_write_b32 a59, 0\n"
"v_accvgpr_write_b32 a60, 0\nv_accvgpr_write_b32 a61, 0\nv_accvgpr_write_b32 a62, 0\nv_accvgpr_write_b32 a63, 0\nv_accvgpr_write_b32 a64, 0\nv_accvgpr_write_b32 a65, 0\nv_accvgpr_write_b32 a66, 0\nv_accvgpr_write_b32 a67, 0\n"
"v_accvgpr_write_b32 a68, 0\nv_accvgpr_write_b32 a69, 0\nv_accvgpr_write_b32 a70, 0\nv_accvgpr_write_b32 a71, 0\nv_accvgpr_write_b32 a72, 0\nv_accvgpr_write_b32 a73, 0\nv_accvgpr_write_b32 a74, 0\nv_accvgpr_write_b32 a75, 0\n"
"v_accvgpr_write_b32 a76, 0\nv_accvgpr_write_b32 a77, 0\nv_accvgpr_write_b32 a78, 0\nv_accvgpr_write_b32 a79, 0\nv_accvgpr_write_b32 a80, 0\nv_accvgpr_write_b32 a81, 0\nv_accvgpr_write_b32 a82, 0\nv_accvgpr_write_b32 a83, 0\n"
"v_accvgpr_write_b32 a84, 0\nv_accvgpr_write_b32 a85, 0\nv_accvgpr_write_b32 a86, 0\nv_accvgpr_write_b32 a87, 0\nv_accvgpr_write_b32 a88, 0\nv_accvgpr_write_b32 a89, 0\nv_accvgpr_write_b32 a90, 0\nv_accvgpr_write_b32 a91, 0\n"
"v_accvgpr_write_b32 a92, 0\nv_accvgpr_write_b32 a93, 0\nv_accvgpr_write_b32 a94, 0\nv_accvgpr_write_b32 a95, 0\nv_accvgpr_write_b32 a96, 0\nv_accvgpr_write_b32 a97, 0\nv_accvgpr_write_b32 a98, 0\nv_accvgpr_write_b32 a99, 0\n"
"v_accvgpr_write_b32 a100, 0\nv_accvgpr_write_b32 a101, 0\nv_accvgpr_write_b32 a102, 0\nv_accvgpr_write_b32 a103, 0\nv_accvgpr_write_b32 a104, 0\nv_accvgpr_write_b32 a105, 0\nv_accvgpr_write_b32 a106, 0\nv_accvgpr_write_b32 a107, 0\n"
"v_accvgpr_write_b32 a108, 0\nv_accvgpr_write_b32 a109, 0\nv_accvgpr_write_b32 a110, 0\nv_accvgpr_write_b32 a111, 0\nv_accvgpr_write_b32 a112, 0\nv_accvgpr_write_b32 a113, 0\nv_accvgpr_write_b32 a114, 0\nv_accvgpr_write_b32 a115, 0\n"
"v_accvgpr_write_b32 a116, 0\nv_accvgpr_write_b32 a117, 0\nv_accvgpr_write_b32 a118, 0\nv_accvgpr_write_b32 a119, 0\nv_accvgpr_write_b32 a120, 0\nv_accvgpr_write_b32 a121, 0\nv_accvgpr_write_b32 a122, 0\nv_accvgpr_write_b32 a123, 0\n"
"v_accvgpr_write_b32 a124, 0\nv_accvgpr_write_b32 a125, 0\nv_accvgpr_write_b32 a126, 0\nv_accvgpr_write_b32 a127, 0\nv_mov_b32 v64, 0\nv_mov_b32 v65, 0\nv_mov_b32 v66, 0\nv_mov_b32 v67, 0\n"
"v_mov_b32 v68, 0\nv_mov_b32 v69, 0\nv_mov_b32 v70, 0\nv_mov_b32 v71, 0\nv_mov_b32 v72, 0\nv_mov_b32 v73, 0\nv_mov_b32 v74, 0\nv_mov_b32 v75, 0\n"
"v_mov_b32 v76, 0\nv_mov_b32 v77, 0\nv_mov_b32 v78, 0\nv_mov_b32 v79, 0\nv_mov_b32 v80, 0\nv_mov_b32 v81, 0\nv_mov_b32 v82, 0\nv_mov_b32 v83, 0\n"
"v_mov_b32 v84, 0\nv_mov_b32 v85, 0\nv_mov_b32 v86, 0\nv_mov_b32 v87, 0\nv_mov_b32 v88, 0\nv_mov_b32 v89, 0\nv_mov_b32 v90, 0\nv_mov_b32 v91, 0\n"
"v_mov_b32 v92, 0\nv_mov_b32 v93, 0\nv_mov_b32 v94, 0\nv_mov_b32 v95, 0\nv_mov_b32 v96, 0\nv_mov_b32 v97, 0\nv_mov_b32 v98, 0\nv_mov_b32 v99, 0\n"
"v_mov_b32 v100, 0\nv_mov_b32 v101, 0\nv_mov_b32 v102, 0\nv_mov_b32 v103, 0\nv_mov_b32 v104, 0\nv_mov_b32 v105, 0\nv_mov_b32 v106, 0\nv_mov_b32 v107, 0\n"
"v_mov_b32 v108, 0\nv_mov_b32 v109, 0\nv_mov_b32 v110, 0\nv_mov_b32 v111, 0\nv_mov_b32 v112, 0\nv_mov_b32 v113, 0\nv_mov_b32 v114, 0\nv_mov_b32 v115, 0\n"
"v_mov_b32 v116, 0\nv_mov_b32 v117, 0\nv_mov_b32 v118, 0\nv_mov_b32 v119, 0\nv_mov_b32 v120, 0\nv_mov_b32 v121, 0\nv_mov_b32 v122, 0\nv_mov_b32 v123, 0\n"
"v_mov_b32 v124, 0\nv_mov_b32 v125, 0\nv_mov_b32 v126, 0\nv_mov_b32 v127, 0\nv_mov_b32 v128, 0\nv_mov_b32 v129, 0\nv_mov_b32 v130, 0\nv_mov_b32 v131, 0\n"
"v_mov_b32 v132, 0\nv_mov_b32 v133, 0\nv_mov_b32 v134, 0\nv_mov_b32 v135, 0\nv_mov_b32 v136, 0\nv_mov_b32 v137, 0\nv_mov_b32 v138, 0\nv_mov_b32 v139, 0\n"
"s_waitcnt vmcnt(12)\nv_mov_b32 v140, 0\ns_waitcnt vmcnt(11)\nv_mov_b32 v141, 0\nv_mov_b32 v142, 0\nv_mov_b32 v143, 0\nv_mov_b32 v144, 0\nv_mov_b32 v145, 0\n"
"v_mov_b32 v146, 0\nv_mov_b32 v147, 0\nv_mov_b32 v148, 0\nv_mov_b32 v149, 0\nv_mov_b32 v150, 0\nv_mov_b32 v151, 0\nv_mov_b32 v152, 0\nv_mov_b32 v153, 0\n"
"v_mov_b32 v154, 0\nv_mov_b32 v155, 0\nv_mov_b32 v156, 0\nv_mov_b32 v157, 0\nv_mov_b32 v158, 0\nv_mov_b32 v159, 0\nv_mov_b32 v160, 0\nv_mov_b32 v161, 0\n"
"v_mov_b32 v162, 0\nv_mov_b32 v163, 0\nv_mov_b32 v164, 0\nv_mov_b32 v165, 0\nv_mov_b32 v166, 0\nv_mov_b32 v167, 0\nv_mov_b32 v168, 0\nv_mov_b32 v169, 0\n"
"v_mov_b32 v170, 0\nv_mov_b32 v171, 0\nv_mov_b32 v172, 0\nv_mov_b32 v173, 0\nv_mov_b32 v174, 0\nv_mov_b32 v175, 0\nv_mov_b32 v176, 0\nv_mov_b32 v177, 0\n"
"v_mov_b32 v178, 0\nv_mov_b32 v179, 0\nv_mov_b32 v180, 0\nv_mov_b32 v181, 0\nv_mov_b32 v182, 0\nv_mov_b32 v183, 0\nv_mov_b32 v184, 0\nv_mov_b32 v185, 0\n"
"v_mov_b32 v186, 0\nv_mov_b32 v187, 0\nv_mov_b32 v188, 0\nv_mov_b32 v189, 0\nv_mov_b32 v190, 0\nv_mov_b32 v191, 0\nv_mov_b32 v192, 0\nv_mov_b32 v193, 0\n"
"v_mov_b32 v194, 0\nv_mov_b32 v195, 0\nv_mov_b32 v196, 0\nv_mov_b32 v197, 0\nv_mov_b32 v198, 0\nv_mov_b32 v199, 0\nv_mov_b32 v200, 0\nv_mov_b32 v201, 0\n"
"v_mov_b32 v202, 0\nv_mov_b32 v203, 0\nv_mov_b32 v204, 0\nv_mov_b32 v205, 0\nv_mov_b32 v206, 0\nv_mov_b32 v207, 0\nv_mov_b32 v208, 0\nv_mov_b32 v209, 0\n"
"v_mov_b32 v210, 0\nv_mov_b32 v211, 0\nv_mov_b32 v212, 0\nv_mov_b32 v213, 0\nv_mov_b32 v214, 0\nv_mov_b32 v215, 0\nv_mov_b32 v216, 0\nv_mov_b32 v217, 0\n"
"v_mov_b32 v218, 0\nv_mov_b32 v219, 0\nv_mov_b32 v220, 0\nv_mov_b32 v221, 0\nv_mov_b32 v222, 0\nv_mov_b32 v223, 0\nv_mov_b32 v224, 0\nv_mov_b32 v225, 0\n"
"v_mov_b32 v226, 0\nv_mov_b32 v227, 0\nv_mov_b32 v228, 0\nv_mov_b32 v229, 0\nv_mov_b32 v230, 0\nv_mov_b32 v231, 0\nv_mov_b32 v232, 0\nv_mov_b32 v233, 0\n"
"v_mov_b32 v234, 0\nv_mov_b32 v235, 0\nv_mov_b32 v236, 0\nv_mov_b32 v237, 0\nv_mov_b32 v238, 0\nv_mov_b32 v239, 0\nv_mov_b32 v240, 0\nv_mov_b32 v241, 0\n"
"v_mov_b32 v242, 0\nv_mov_b32 v243, 0\nv_mov_b32 v244, 0\nv_mov_b32 v245, 0\nv_mov_b32 v246, 0\nv_mov_b32 v247, 0\nv_mov_b32 v248, 0\nv_mov_b32 v249, 0\n"
"v_mov_b32 v250, 0\nv_mov_b32 v251, 0\nv_mov_b32 v252, 0\nv_mov_b32 v253, 0\nv_mov_b32 v254, 0\nv_mov_b32 v255, 0\ns_cmp_lg_u32 s62, 15360\ns_cbranch_scc1 49f\n"
"v_lshrrev_b32 v56, 2, v23\nv_and_b32 v56, 124, v56\nbuffer_load_dword v31, v56, s[56:59], s92 offen offset:0 sc1\ns_waitcnt vmcnt(0)\nv_cmp_ne_u32 vcc, s34, v31\ns_cbranch_vccz 51f\ns_mov_b32 s69, 0x80000\n50:\n"
"s_sleep 2\nbuffer_load_dword v31, v56, s[56:59], s92 offen offset:0 sc1\ns_waitcnt vmcnt(0)\nv_cmp_ne_u32 vcc, s34, v31\ns_cbranch_vccz 51f\ns_sub_u32 s69, s69, 1\ns_cbranch_scc0 50b\n51:\n"
"49:\ns_waitcnt vmcnt(10)\ns_barrier\nlc 128,23,67,0\nlc 132,23,67,1024\nlc 136,23,67,2048\nlc 140,23,67,3072\ns_add_u32 s67, s67, 4096\n"
"lc 144,23,67,0\nlc 148,23,67,1024\nlc 152,23,67,2048\nlc 156,23,67,3072\ns_add_u32 s67, s67, 4096\ns_cmp_lg_u32 s62, 15360\ns_cbranch_scc1 40f\nsm 40960\n"
"s_nop 0\nlb 20,36,64,0\nlb 20,40,64,1024\ns_add_u32 s64, s64, 229376\nlb 21,44,65,2048\nlb 21,48,65,3072\ns_add_u32 s65, s65, s60\nsm 45056\n"
"s_nop 0\nlb 22,52,66,0\ns_add_u32 s66, s66, s61\nsm 61440\ns_nop 0\nlb 20,36,64,0\nlb 20,40,64,1024\ns_add_u32 s64, s64, 229376\n"
"lb 21,44,65,2048\nlb 21,48,65,3072\ns_add_u32 s65, s65, s60\nsm 65536\ns_nop 0\nlb 22,52,66,0\ns_add_u32 s66, s66, s61\n40:\n"
"v_mov_b32 v31, v33\ns_cmp_eq_u32 s62, 0\ns_cbranch_scc1 46f\nbuffer_store_dwordx4 a[224:227], v31, s[56:59], s88 offen sc1\nbuffer_store_dwordx4 a[228:231], v31, s[56:59], s89 offen sc1\nbuffer_store_dwordx4 a[232:235], v31, s[56:59], s90 offen sc1\nbuffer_store_dwordx4 a[236:239], v31, s[56:59], s91 offen sc1\n46:\n"
"lc 160,23,67,0\nlc 164,23,67,1024\nlc 168,23,67,2048\nlc 172,23,67,3072\ns_add_u32 s67, s67, 4096\nlc 176,23,67,0\nlc 180,23,67,1024\nlc 184,23,67,2048\n"
"lc 188,23,67,3072\ns_add_u32 s67, s67, 4096\ns_sub_u32 s94, s30, 27287552\ns_lshr_b32 s94, s94, 2\ns_and_b32 s95, s32, 0x700\ns_sub_u32 s86, s8, s95\ns_subb_u32 s95, s9, 0\ns_add_u32 s94, s86, s94\n"
"s_addc_u32 s95, s95, 0\ns_lshl_b32 s86, s2, 8\ns_cmp_eq_u32 s62, 15360\ns_cselect_b32 s86, s2, s86\ns_cselect_b32 s87, 2, 99\ndt 32,0,0\ndt 34,0,10240\ndt 36,0,20480\n"
"dt 38,0,30720\ndt 40,1,0\ndt 42,1,10240\ndt 44,1,20480\ndt 46,1,30720\ndt 48,2,0\ndt 50,2,10240\ndt 52,2,20480\n"
"dt 54,2,30720\ns_cmp_eq_u32 s62, 0\ns_cbranch_scc1 45f\ns_cmp_eq_u32 s62, 15360\ns_waitcnt vmcnt(22)\ns_cbranch_scc1 44f\ns_waitcnt vmcnt(12)\ns_branch 44f\n"
"45:\ns_waitcnt vmcnt(8)\n44:\n1:\nwb 19,8\nmvva 64,32,128\ndt 56,3,0\nmvva 64,36,144\n"
"dt 58,3,10240\nmvva 68,36,148\ndt 60,3,20480\nmvva 68,32,132\ndt 62,3,30720\nmvva 72,32,136\nmvva 72,36,152\nmvva 76,36,156\n"
"mvva 76,32,140\nwl 8\nmvva 92,40,140\ndt 32,0,1024\nmvva 92,44,156\ndt 34,0,11264\nmvva 88,44,152\ndt 36,0,21504\n"
"mvva 88,40,136\ndt 38,0,31744\nmvva 84,40,132\nsm 81920\nmvva 84,44,148\nlb 20,36,64,0\nmvva 80,44,144\nmvva 80,40,128\n"
"wl 8\nmvva 96,48,128\ndt 40,1,1024\nmvva 96,52,144\ndt 42,1,11264\nmvva 100,52,148\ndt 44,1,21504\nmvva 100,48,132\n"
"dt 46,1,31744\nmvva 104,48,136\nmvva 104,52,152\nlb 20,40,64,1024\nmvva 108,52,156\ns_add_u32 s64, s64, 229376\nmvva 108,48,140\nwl 8\n"
"mvva 124,56,140\ndt 48,2,1024\nmvva 124,60,156\ndt 50,2,11264\nmvva 120,60,152\ndt 52,2,21504\nmvva 120,56,136\ndt 54,2,31744\n"
"mvva 116,56,132\nmvva 116,60,148\nlb 21,44,65,2048\nmvva 112,60,144\nmvva 112,56,128\nwl 8\nmava 0,32,128\ndt 56,3,1024\n"
"mava 0,36,144\ndt 58,3,11264\nmava 4,36,148\ndt 60,3,21504\nmava 4,32,132\ndt 62,3,31744\nmava 8,32,136\nmava 8,36,152\n"
"lb 21,48,65,3072\nmava 12,36,156\ns_add_u32 s65, s65, s60\nmava 12,32,140\nwl 8\nmava 28,40,140\ndt 32,0,2048\nmava 28,44,156\n"
"dt 34,0,12288\nmava 24,44,152\ndt 36,0,22528\nmava 24,40,136\ndt 38,0,32768\nmava 20,40,132\nsm 86016\nmava 20,44,148\n"
"lb 22,52,66,0\nmava 16,44,144\nv_lshlrev_b32 v31, 2, v23\ns_add_u32 s66, s66, s61\nmava 16,40,128\nbuffer_load_dword a224, v31, s[0:3], 0 offen\ns_add_u32 s0, s0, s86\ns_addc_u32 s1, s1, 0\n"
"wl 8\nmava 32,48,128\ndt 40,1,2048\nmava 32,52,144\ndt 42,1,12288\nmava 36,52,148\ndt 44,1,22528\nmava 36,48,132\n"
"dt 46,1,32768\nmava 40,48,136\nsm 102400\nmava 40,52,152\nlb 20,36,64,0\nmava 44,52,156\nmava 44,48,140\nwl 8\n"
"mava 60,56,140\ndt 48,2,2048\nmava 60,60,156\ndt 50,2,12288\nmava 56,60,152\ndt 52,2,22528\nmava 56,56,136\ndt 54,2,32768\n"
"mava 52,56,132\nmava 52,60,148\nlb 20,40,64,1024\nmava 48,60,144\ns_add_u32 s64, s64, 229376\nmava 48,56,128\nwl 8\nmvva 128,32,128\n"
"dt 56,3,2048\nmvva 128,36,144\ndt 58,3,12288\nmvva 132,36,148\ndt 60,3,22528\nmvva 132,32,132\ndt 62,3,32768\nmvva 136,32,136\n"
"mvva 136,36,152\nlb 21,44,65,2048\nmvva 140,36,156\nmvva 140,32,140\nwl 8\nmvva 156,40,140\ndt 32,0,3072\nmvva 156,44,156\n"
"dt 34,0,13312\nmvva 152,44,152\ndt 36,0,23552\nmvva 152,40,136\ndt 38,0,33792\nmvva 148,40,132\nmvva 148,44,148\nlb 21,48,65,3072\n"
"mvva 144,44,144\ns_add_u32 s65, s65, s60\nmvva 144,40,128\nwl 8\nmvva 160,48,128\ndt 40,1,3072\nmvva 160,52,144\ndt 42,1,13312\n"
"mvva 164,52,148\ndt 44,1,23552\nmvva 164,48,132\ndt 46,1,33792\nmvva 168,48,136\nsm 106496\nmvva 168,52,152\nlb 22,52,66,0\n"
"mvva 172,52,156\ns_add_u32 s66, s66, s61\nmvva 172,48,140\nwl 8\nmvva 188,56,140\ndt 48,2,3072\nmvva 188,60,156\ndt 50,2,13312\n"
"mvva 184,60,152\ndt 52,2,23552\nmvva 184,56,136\ndt 54,2,33792\nmvva 180,56,132\nmvva 180,60,148\nlc 192,23,67,0\nmvva 176,60,144\n"
"mvva 176,56,128\nwl 8\nmava 64,32,128\ndt 56,3,3072\nmava 64,36,144\ndt 58,3,13312\nmava 68,36,148\ndt 60,3,23552\n"
"mava 68,32,132\ndt 62,3,33792\nmava 72,32,136\nmava 72,36,152\nlc 196,23,67,1024\nmava 76,36,156\nmava 76,32,140\nwl 8\n"
"mava 92,40,140\ndt 32,0,4096\nmava 92,44,156\ndt 34,0,14336\nmava 88,44,152\ndt 36,0,24576\nmava 88,40,136\ndt 38,0,34816\n"
"mava 84,40,132\nmava 84,44,148\nlc 200,23,67,2048\nmava 80,44,144\nmava 80,40,128\nwl 8\nmava 96,48,128\ndt 40,1,4096\n"
"mava 96,52,144\ndt 42,1,14336\nmava 100,52,148\ndt 44,1,24576\nmava 100,48,132\ndt 46,1,34816\nmava 104,48,136\nmava 104,52,152\n"
"lc 204,23,67,3072\nmava 108,52,156\ns_add_u32 s67, s67, 4096\nmava 108,48,140\nwl 8\nmava 124,56,140\ndt 48,2,4096\nmava 124,60,156\n"
"dt 50,2,14336\nmava 120,60,152\ndt 52,2,24576\nmava 120,56,136\ndt 54,2,34816\nmava 116,56,132\nmava 116,60,148\nlc 208,23,67,0\n"
"mava 112,60,144\nmava 112,56,128\nwl 8\nmvva 192,32,128\ndt 56,3,4096\nmvva 192,36,144\ndt 58,3,14336\nmvva 196,36,148\n"
"dt 60,3,24576\nmvva 196,32,132\ndt 62,3,34816\nmvva 200,32,136\nmvva 200,36,152\nlc 212,23,67,1024\nmvva 204,36,156\ns_waitcnt vmcnt(25)\n"
"s_barrier\nmvva 204,32,140\nwl 8\ns_mov_b64 exec, 1\nv_mov_b32 v31, s34\nbuffer_store_dword v31, off, s[56:59], s93 sc1\ns_mov_b64 exec, -1\ns_mov_b32 s93, 0x70000000\n"
"s_add_u32 s92, s92, 128\nmvva 220,40,140\ndt 32,0,40960\nmvva 220,44,156\ndt 34,0,51200\nmvva 216,44,152\ndt 36,0,61440\nmvva 216,40,136\n"
"dt 38,6,6144\nmvva 212,40,132\nmvva 212,44,148\nlc 216,23,67,2048\nmvva 208,44,144\nmvva 208,40,128\nwl 8\nmvva 224,48,128\n"
"dt 40,1,40960\nmvva 224,52,144\ndt 42,1,51200\nmvva 228,52,148\ndt 44,1,61440\nmvva 228,48,132\ndt 46,7,6144\nmvva 232,48,136\n"
"mvva 232,52,152\nlc 220,23,67,3072\nmvva 236,52,156\ns_add_u32 s67, s67, 4096\nmvva 236,48,140\nwl 8\nmvva 252,56,140\ndt 48,2,40960\n"
"mvva 252,60,156\ndt 50,2,51200\nmvva 248,60,152\ndt 52,2,61440\nmvva 248,56,136\ndt 54,8,6144\nmvva 244,56,132\nmvva 244,60,148\n"
"mvva 240,60,144\nmvva 240,56,128\nwb 20,8\nmvva 64,32,160\ndt 56,3,40960\nmvva 64,36,176\ndt 58,3,51200\nmvva 68,36,180\n"
"dt 60,3,61440\nmvva 68,32,164\ndt 62,9,6144\nmvva 72,32,168\nmvva 72,36,184\nmvva 76,36,188\nmvva 76,32,172\nwl 8\n"
"mvva 92,40,172\ndt 32,0,41984\nmvva 92,44,188\ndt 34,0,52224\nmvva 88,44,184\ndt 36,0,62464\nmvva 88,40,168\ndt 38,6,7168\n"
"mvva 84,40,164\nsm 122880\nmvva 84,44,180\nlb 20,36,64,0\nmvva 80,44,176\nmvva 80,40,160\nwl 8\nmvva 96,48,160\n"
"dt 40,1,41984\nmvva 96,52,176\ndt 42,1,52224\nmvva 100,52,180\ndt 44,1,62464\nmvva 100,48,164\ndt 46,7,7168\nmvva 104,48,168\n"
"mvva 104,52,184\nlb 20,40,64,1024\nmvva 108,52,188\ns_add_u32 s64, s64, 229376\nmvva 108,48,172\nwl 8\nmvva 124,56,172\ndt 48,2,41984\n"
"mvva 124,60,188\ndt 50,2,52224\nmvva 120,60,184\ndt 52,2,62464\nmvva 120,56,168\ndt 54,8,7168\nmvva 116,56,164\nmvva 116,60,180\n"
"lb 21,44,65,2048\nmvva 112,60,176\nmvva 112,56,160\nwl 8\nmava 0,32,160\ndt 56,3,41984\nmava 0,36,176\ndt 58,3,52224\n"
"mava 4,36,180\ndt 60,3,62464\nmava 4,32,164\ndt 62,9,7168\nmava 8,32,168\nmava 8,36,184\nlb 21,48,65,3072\nmava 12,36,188\n"
"s_add_u32 s65, s65, s60\nmava 12,32,172\nwl 8\nmava 28,40,172\ndt 32,0,43008\nmava 28,44,188\ndt 34,0,53248\nmava 24,44,184\n"
"dt 36,0,63488\nmava 24,40,168\ndt 38,6,8192\nmava 20,40,164\nsm 126976\nmava 20,44,180\nlb 22,52,66,0\nmava 16,44,176\n"
"v_lshlrev_b32 v31, 2, v23\ns_add_u32 s66, s66, s61\nmava 16,40,160\nbuffer_load_dword a128, v31, s[0:3], 0 offen\ns_add_u32 s0, s0, s86\ns_addc_u32 s1, s1, 0\nwl 8\nmava 32,48,160\n"
"dt 40,1,43008\nmava 32,52,176\ndt 42,1,53248\nmava 36,52,180\ndt 44,1,63488\nmava 36,48,164\ndt 46,7,8192\nmava 40,48,168\n"
"sm 143360\nmava 40,52,184\nlb 20,36,64,0\nmava 44,52,188\nmava 44,48,172\nwl 8\nmava 60,56,172\ndt 48,2,43008\n"
"mava 60,60,188\ndt 50,2,53248\nmava 56,60,184\ndt 52,2,63488\nmava 56,56,168\ndt 54,8,8192\nmava 52,56,164\nmava 52,60,180\n"
"lb 20,40,64,1024\nmava 48,60,176\ns_add_u32 s64, s64, 229376\nmava 48,56,160\nwl 8\nmvva 128,32,160\ndt 56,3,43008\nmvva 128,36,176\n"
"dt 58,3,53248\nmvva 132,36,180\ndt 60,3,63488\nmvva 132,32,164\ndt 62,9,8192\nmvva 136,32,168\nmvva 136,36,184\nlb 21,44,65,2048\n"
"mvva 140,36,188\nmvva 140,32,172\nwl 8\nmvva 156,40,172\ndt 32,0,44032\nmvva 156,44,188\ndt 34,0,54272\nmvva 152,44,184\n"
"dt 36,0,64512\nmvva 152,40,168\ndt 38,6,9216\nmvva 148,40,164\nmvva 148,44,180\nlb 21,48,65,3072\nmvva 144,44,176\ns_add_u32 s65, s65, s60\n"
"mvva 144,40,160\nwl 8\nmvva 160,48,160\ndt 40,1,44032\nmvva 160,52,176\ndt 42,1,54272\nmvva 164,52,180\ndt 44,1,64512\n"
"mvva 164,48,164\ndt 46,7,9216\nmvva 168,48,168\nsm 147456\nmvva 168,52,184\nlb 22,52,66,0\nmvva 172,52,188\ns_add_u32 s66, s66, s61\n"
"mvva 172,48,172\nwl 8\nv_lshrrev_b32 v31, 2, v23\nv_and_b32 v31, 124, v31\nbuffer_load_dword v31, v31, s[56:59], s92 offen offset:0 sc1\nmvva 188,56,172\ndt 48,2,44032\nmvva 188,60,188\n"
"dt 50,2,54272\nmvva 184,60,184\ndt 52,2,64512\nmvva 184,56,168\ndt 54,8,9216\nmvva 180,56,164\nmvva 180,60,180\nlc 224,23,67,0\n"
"mvva 176,60,176\nmvva 176,56,160\nwl 8\nmava 64,32,160\ndt 56,3,44032\nmava 64,36,176\ndt 58,3,54272\nmava 68,36,180\n"
"dt 60,3,64512\nmava 68,32,164\ndt 62,9,9216\nmava 72,32,168\nmava 72,36,184\nlc 228,23,67,1024\nmava 76,36,188\nmava 76,32,172\n"
"wl 8\nmava 92,40,172\ndt 32,0,45056\nmava 92,44,188\ndt 34,0,55296\nmava 88,44,184\ndt 36,6,0\nmava 88,40,168\n"
"dt 38,6,10240\nmava 84,40,164\nmava 84,44,180\nlc 232,23,67,2048\nmava 80,44,176\nmava 80,40,160\nwl 8\nmava 96,48,160\n"
"dt 40,1,45056\nmava 96,52,176\ndt 42,1,55296\nmava 100,52,180\ndt 44,7,0\nmava 100,48,164\ndt 46,7,10240\nmava 104,48,168\n"
"mava 104,52,184\nlc 236,23,67,3072\nmava 108,52,188\ns_add_u32 s67, s67, 4096\nmava 108,48,172\nwl 8\nmava 124,56,172\ndt 48,2,45056\n"
"mava 124,60,188\ndt 50,2,55296\nmava 120,60,184\ndt 52,8,0\nmava 120,56,168\ndt 54,8,10240\nmava 116,56,164\nmava 116,60,180\n"
"lc 240,23,67,0\nmava 112,60,176\nmava 112,56,160\nwl 8\nmvva 192,32,160\ndt 56,3,45056\nmvva 192,36,176\ndt 58,3,55296\n"
"mvva 196,36,180\ndt 60,9,0\nmvva 196,32,164\ndt 62,9,10240\nmvva 200,32,168\nmvva 200,36,184\nlc 244,23,67,1024\nmvva 204,36,188\n"
"s_waitcnt vmcnt(27)\ns_barrier\nmvva 204,32,172\nwl 8\nmvva 220,40,172\ndt 32,6,16384\nmvva 220,44,188\ndt 34,6,26624\n"
"mvva 216,44,184\ndt 36,6,36864\nmvva 216,40,168\ndt 38,6,47104\nmvva 212,40,164\nmvva 212,44,180\nlc 248,23,67,2048\nmvva 208,44,176\n"
"mvva 208,40,160\nwl 8\nmvva 224,48,160\ndt 40,7,16384\nmvva 224,52,176\ndt 42,7,26624\nmvva 228,52,180\ndt 44,7,36864\n"
"mvva 228,48,164\ndt 46,7,47104\nmvva 232,48,168\nmvva 232,52,184\nlc 252,23,67,3072\nmvva 236,52,188\ns_add_u32 s67, s67, 4096\nmvva 236,48,172\n"
"wl 8\nmvva 252,56,172\ndt 48,8,16384\nmvva 252,60,188\ndt 50,8,26624\nmvva 248,60,184\ndt 52,8,36864\nmvva 248,56,168\n"
"dt 54,8,47104\nmvva 244,56,164\nmvva 244,60,180\nmvva 240,60,176\nmvva 240,56,160\nwb 20,8\nmvva 64,32,192\ndt 56,9,16384\n"
"mvva 64,36,208\ndt 58,9,26624\nmvva 68,36,212\ndt 60,9,36864\nmvva 68,32,196\ndt 62,9,47104\nmvva 72,32,200\nmvva 72,36,216\n"
"mvva 76,36,220\nmvva 76,32,204\nwl 8\nmvva 92,40,204\ndt 32,6,17408\nmvva 92,44,220\ndt 34,6,27648\nmvva 88,44,216\n"
"dt 36,6,37888\nmvva 88,40,200\ndt 38,6,48128\nmvva 84,40,196\nsm 0\nmvva 84,44,212\nlb 20,36,64,0\nmvva 80,44,208\n"
"mvva 80,40,192\nwl 8\nmvva 96,48,192\ndt 40,7,17408\nmvva 96,52,208\ndt 42,7,27648\nmvva 100,52,212\ndt 44,7,37888\n"
"mvva 100,48,196\ndt 46,7,48128\nmvva 104,48,200\nmvva 104,52,216\nlb 20,40,64,1024\nmvva 108,52,220\ns_add_u32 s64, s64, 229376\nmvva 108,48,204\n"
"wl 8\nmvva 124,56,204\ndt 48,8,17408\nmvva 124,60,220\ndt 50,8,27648\nmvva 120,60,216\ndt 52,8,37888\nmvva 120,56,200\n"
"dt 54,8,48128\nmvva 116,56,196\nmvva 116,60,212\nlb 21,44,65,2048\nmvva 112,60,208\nmvva 112,56,192\nwl 8\nmava 0,32,192\n"
"dt 56,9,17408\nmava 0,36,208\ndt 58,9,27648\nmava 4,36,212\ndt 60,9,37888\nmava 4,32,196\ndt 62,9,48128\nmava 8,32,200\n"
"mava 8,36,216\nlb 21,48,65,3072\nmava 12,36,220\ns_add_u32 s65, s65, s60\nmava 12,32,204\nwl 8\nmava 28,40,204\ndt 32,6,18432\n"
"mava 28,44,220\ndt 34,6,28672\nmava 24,44,216\ndt 36,6,38912\nmava 24,40,200\ndt 38,6,49152\nmava 20,40,196\nsm 4096\n"
"mava 20,44,212\nlb 22,52,66,0\nmava 16,44,208\ns_add_u32 s66, s66, s61\nmava 16,40,192\nwl 8\nmava 32,48,192\ndt 40,7,18432\n"
"mava 32,52,208\ndt 42,7,28672\nmava 36,52,212\ndt 44,7,38912\nmava 36,48,196\ndt 46,7,49152\nmava 40,48,200\nsm 20480\n"
"mava 40,52,216\nlb 20,36,64,0\nmava 44,52,220\nmava 44,48,204\nwl 8\nmava 60,56,204\ndt 48,8,18432\nmava 60,60,220\n"
"dt 50,8,28672\nmava 56,60,216\ndt 52,8,38912\nmava 56,56,200\ndt 54,8,49152\nmava 52,56,196\nmava 52,60,212\nlb 20,40,64,1024\n"
"mava 48,60,208\ns_add_u32 s64, s64, 229376\nmava 48,56,192\nwl 8\nmvva 128,32,192\ndt 56,9,18432\nmvva 128,36,208\ndt 58,9,28672\n"
"mvva 132,36,212\ndt 60,9,38912\nmvva 132,32,196\ndt 62,9,49152\nmvva 136,32,200\nmvva 136,36,216\nlb 21,44,65,2048\nmvva 140,36,220\n"
"mvva 140,32,204\nwl 8\nmvva 156,40,204\ndt 32,6,19456\nmvva 156,44,220\ndt 34,6,29696\nmvva 152,44,216\ndt 36,6,39936\n"
"mvva 152,40,200\ndt 38,6,50176\nmvva 148,40,196\nmvva 148,44,212\nlb 21,48,65,3072\nmvva 144,44,208\ns_add_u32 s65, s65, s60\nmvva 144,40,192\n"
"wl 8\ns_waitcnt vmcnt(17)\nv_cmp_ne_u32 vcc, s34, v31\ns_cbranch_vccz 43f\ns_mov_b32 s69, 0x80000\n42:\ns_sleep 1\nv_lshrrev_b32 v31, 2, v23\n"
"v_and_b32 v31, 124, v31\nbuffer_load_dword v31, v31, s[56:59], s92 offen offset:0 sc1\ns_waitcnt vmcnt(0)\nv_cmp_ne_u32 vcc, s34, v31\ns_cbranch_vccz 43f\ns_sub_u32 s69, s69, 1\ns_cbranch_scc0 42b\n43:\n"
"mvva 160,48,192\ndt 40,7,19456\nmvva 160,52,208\ndt 42,7,29696\nmvva 164,52,212\ndt 44,7,39936\nmvva 164,48,196\ndt 46,7,50176\n"
"mvva 168,48,200\nsm 24576\nmvva 168,52,216\nlb 22,52,66,0\nmvva 172,52,220\ns_add_u32 s66, s66, s61\nmvva 172,48,204\nwl 8\n"
"mvva 188,56,204\ndt 48,8,19456\nmvva 188,60,220\ndt 50,8,29696\nmvva 184,60,216\ndt 52,8,39936\nmvva 184,56,200\ndt 54,8,50176\n"
"mvva 180,56,196\nmvva 180,60,212\nlc 128,23,67,0\nmvva 176,60,208\nmvva 176,56,192\nwl 8\nmava 64,32,192\ndt 56,9,19456\n"
"mava 64,36,208\ndt 58,9,29696\nmava 68,36,212\ndt 60,9,39936\nmava 68,32,196\ndt 62,9,50176\nmava 72,32,200\nmava 72,36,216\n"
"lc 132,23,67,1024\nmava 76,36,220\nmava 76,32,204\nwl 8\nmava 92,40,204\ndt 32,6,20480\nmava 92,44,220\ndt 34,6,30720\n"
"mava 88,44,216\ndt 36,6,40960\nmava 88,40,200\ndt 38,6,51200\nmava 84,40,196\nmava 84,44,212\nlc 136,23,67,2048\nmava 80,44,208\n"
"mava 80,40,192\nwl 8\nmava 96,48,192\ndt 40,7,20480\nmava 96,52,208\ndt 42,7,30720\nmava 100,52,212\ndt 44,7,40960\n"
"mava 100,48,196\ndt 46,7,51200\nmava 104,48,200\nmava 104,52,216\nlc 140,23,67,3072\nmava 108,52,220\ns_add_u32 s67, s67, 4096\nmava 108,48,204\n"
"wl 8\nmava 124,56,204\ndt 48,8,20480\nmava 124,60,220\ndt 50,8,30720\nmava 120,60,216\ndt 52,8,40960\nmava 120,56,200\n"
"dt 54,8,51200\nmava 116,56,196\nmava 116,60,212\nlc 144,23,67,0\nmava 112,60,208\nv_lshlrev_b32 v31, 2, v23\nmava 112,56,192\nbuffer_load_dword a160, v31, s[0:3], 0 offen\n"
"s_add_u32 s0, s0, s86\ns_addc_u32 s1, s1, 0\nwl 8\nmvva 192,32,192\ndt 56,9,20480\nmvva 192,36,208\ndt 58,9,30720\nmvva 196,36,212\n"
"dt 60,9,40960\nmvva 196,32,196\ndt 62,9,51200\nmvva 200,32,200\nmvva 200,36,216\nlc 148,23,67,1024\nmvva 204,36,220\ns_barrier\n"
"mvva 204,32,204\nwl 8\nmvva 220,40,204\ndt 32,6,57344\nmvva 220,44,220\ndt 34,12,2048\nmvva 216,44,216\ndt 36,12,12288\n"
"mvva 216,40,200\ndt 38,12,22528\nmvva 212,40,196\nmvva 212,44,212\nlc 152,23,67,2048\nmvva 208,44,208\nmvva 208,40,192\nwl 8\n"
"mvva 224,48,192\ndt 40,7,57344\nmvva 224,52,208\ndt 42,13,2048\nmvva 228,52,212\ndt 44,13,12288\nmvva 228,48,196\ndt 46,13,22528\n"
"mvva 232,48,200\nmvva 232,52,216\nlc 156,23,67,3072\nmvva 236,52,220\ns_add_u32 s67, s67, 4096\nmvva 236,48,204\nwl 8\nmvva 252,56,204\n"
"dt 48,8,57344\nmvva 252,60,220\ndt 50,14,2048\nmvva 248,60,216\ndt 52,14,12288\nmvva 248,56,200\ndt 54,14,22528\nmvva 244,56,196\n"
"mvva 244,60,212\nmvva 240,60,208\nmvva 240,56,192\nwb 19,8\nmvva 64,32,224\ndt 56,9,57344\nmvva 64,36,240\ndt 58,15,2048\n"
"mvva 68,36,244\ndt 60,15,12288\nmvva 68,32,228\ndt 62,15,22528\nmvva 72,32,232\nmvva 72,36,248\nmvva 76,36,252\nmvva 76,32,236\n"
"wl 8\nmvva 92,40,236\ndt 32,6,58368\nmvva 92,44,252\ndt 34,12,3072\nmvva 88,44,248\ndt 36,12,13312\nmvva 88,40,232\n"
"dt 38,12,23552\nmvva 84,40,228\nsm 40960\nmvva 84,44,244\nlb 20,36,64,0\nmvva 80,44,240\nmvva 80,40,224\nwl 8\n"
"mvva 96,48,224\ndt 40,7,58368\nmvva 96,52,240\ndt 42,13,3072\nmvva 100,52,244\ndt 44,13,13312\nmvva 100,48,228\ndt 46,13,23552\n"
"mvva 104,48,232\nmvva 104,52,248\nlb 20,40,64,1024\nmvva 108,52,252\ns_add_u32 s64, s64, 229376\nmvva 108,48,236\nwl 8\nmvva 124,56,236\n"
"dt 48,8,58368\nmvva 124,60,252\ndt 50,14,3072\nmvva 120,60,248\ndt 52,14,13312\nmvva 120,56,232\ndt 54,14,23552\nmvva 116,56,228\n"
"mvva 116,60,244\nlb 21,44,65,2048\nmvva 112,60,240\nmvva 112,56,224\nwl 8\nmava 0,32,224\ndt 56,9,58368\nmava 0,36,240\n"
"dt 58,15,3072\nmava 4,36,244\ndt 60,15,13312\nmava 4,32,228\ndt 62,15,23552\nmava 8,32,232\nmava 8,36,248\nlb 21,48,65,3072\n"
"mava 12,36,252\ns_add_u32 s65, s65, s60\nmava 12,32,236\nwl 8\nmava 28,40,236\ndt 32,6,59392\nmava 28,44,252\ndt 34,12,4096\n"
"mava 24,44,248\ndt 36,12,14336\nmava 24,40,232\ndt 38,12,24576\nmava 20,40,228\nsm 45056\nmava 20,44,244\nlb 22,52,66,0\n"
"mava 16,44,240\nv_lshlrev_b32 v31, 2, v23\ns_add_u32 s66, s66, s61\nmava 16,40,224\nbuffer_load_dword a192, v31, s[0:3], 0 offen\ns_add_u32 s0, s0, s86\ns_addc_u32 s1, s1, 0\ns_cmp_eq_u32 s68, s87\n"
"s_cselect_b64 s[0:1], s[94:95], s[0:1]\ns_cselect_b32 s2, 2048, s2\ns_cselect_b32 s86, 2048, s86\nwl 8\nmava 32,48,224\ndt 40,7,59392\nmava 32,52,240\ndt 42,13,4096\n"
"mava 36,52,244\ndt 44,13,14336\nmava 36,48,228\ndt 46,13,24576\nmava 40,48,232\nsm 61440\nmava 40,52,248\nlb 20,36,64,0\n"
"mava 44,52,252\nmava 44,48,236\nwl 8\nmava 60,56,236\ndt 48,8,59392\nmava 60,60,252\ndt 50,14,4096\nmava 56,60,248\n"
"dt 52,14,14336\nmava 56,56,232\ndt 54,14,24576\nmava 52,56,228\nmava 52,60,244\nlb 20,40,64,1024\nmava 48,60,240\ns_add_u32 s64, s64, 229376\n"
"mava 48,56,224\nwl 8\nmvva 128,32,224\ndt 56,9,59392\nmvva 128,36,240\ndt 58,15,4096\nmvva 132,36,244\ndt 60,15,14336\n"
"mvva 132,32,228\ndt 62,15,24576\nmvva 136,32,232\nmvva 136,36,248\nlb 21,44,65,2048\nmvva 140,36,252\nmvva 140,32,236\nwl 8\n"
"mvva 156,40,236\ndt 32,6,60416\nmvva 156,44,252\ndt 34,12,5120\nmvva 152,44,248\ndt 36,12,15360\nmvva 152,40,232\ndt 38,12,25600\n"
"mvva 148,40,228\nmvva 148,44,244\nlb 21,48,65,3072\nmvva 144,44,240\ns_add_u32 s65, s65, s60\nmvva 144,40,224\nwl 8\nmvva 160,48,224\n"
"dt 40,7,60416\nmvva 160,52,240\ndt 42,13,5120\nmvva 164,52,244\ndt 44,13,15360\nmvva 164,48,228\ndt 46,13,25600\nmvva 168,48,232\n"
"sm 65536\nmvva 168,52,248\nlb 22,52,66,0\nmvva 172,52,252\ns_add_u32 s66, s66, s61\nmvva 172,48,236\nwl 8\nmvva 188,56,236\n"
"dt 48,8,60416\nmvva 188,60,252\ndt 50,14,5120\nmvva 184,60,248\ndt 52,14,15360\nmvva 184,56,232\ndt 54,14,25600\nmvva 180,56,228\n"
"mvva 180,60,244\nlc 160,23,67,0\nmvva 176,60,240\nmvva 176,56,224\nwl 8\nmava 64,32,224\ndt 56,9,60416\nmava 64,36,240\n"
"dt 58,15,5120\nmava 68,36,244\ndt 60,15,15360\nmava 68,32,228\ndt 62,15,25600\nmava 72,32,232\nmava 72,36,248\nlc 164,23,67,1024\n"
"mava 76,36,252\nmava 76,32,236\nwl 8\nmava 92,40,236\ndt 32,6,61440\nmava 92,44,252\ndt 34,12,6144\nmava 88,44,248\n"
"dt 36,12,16384\nmava 88,40,232\ndt 38,12,26624\nmava 84,40,228\nmava 84,44,244\nlc 168,23,67,2048\nmava 80,44,240\nmava 80,40,224\n"
"wl 8\nmava 96,48,224\ndt 40,7,61440\nmava 96,52,240\ndt 42,13,6144\nmava 100,52,244\ndt 44,13,16384\nmava 100,48,228\n"
"dt 46,13,26624\nmava 104,48,232\nmava 104,52,248\nlc 172,23,67,3072\nmava 108,52,252\ns_add_u32 s67, s67, 4096\nmava 108,48,236\nwl 8\n"
"mava 124,56,236\ndt 48,8,61440\nmava 124,60,252\ndt 50,14,6144\nmava 120,60,248\ndt 52,14,16384\nmava 120,56,232\ndt 54,14,26624\n"
"mava 116,56,228\nmava 116,60,244\nlc 176,23,67,0\nmava 112,60,240\nmava 112,56,224\nwl 8\nmvva 192,32,224\ndt 56,9,61440\n"
"mvva 192,36,240\ndt 58,15,6144\nmvva 196,36,244\ndt 60,15,16384\nmvva 196,32,228\ndt 62,15,26624\nmvva 200,32,232\nmvva 200,36,248\n"
"lc 180,23,67,1024\nmvva 204,36,252\ns_waitcnt vmcnt(26)\ns_barrier\nmvva 204,32,236\nwl 8\nmvva 220,40,236\ndt 32,0,0\n"
"mvva 220,44,252\ndt 34,0,10240\nmvva 216,44,248\ndt 36,0,20480\nmvva 216,40,232\ndt 38,0,30720\nmvva 212,40,228\nmvva 212,44,244\n"
"lc 184,23,67,2048\nmvva 208,44,240\nmvva 208,40,224\nwl 8\nmvva 224,48,224\ndt 40,1,0\nmvva 224,52,240\ndt 42,1,10240\n"
"mvva 228,52,244\ndt 44,1,20480\nmvva 228,48,228\ndt 46,1,30720\nmvva 232,48,232\nmvva 232,52,248\nlc 188,23,67,3072\nmvva 236,52,252\n"
"s_add_u32 s67, s67, 4096\nmvva 236,48,236\nwl 8\nmvva 252,56,236\ndt 48,2,0\nmvva 252,60,252\ndt 50,2,10240\nmvva 248,60,248\n"
"dt 52,2,20480\nmvva 248,56,232\ndt 54,2,30720\nmvva 244,56,228\nmvva 244,60,244\nmvva 240,60,240\nmvva 240,56,224\ns_sub_u32 s68, s68, 1\n"
"s_cmp_lg_u32 s68, 0\ns_cbranch_scc1 1b\nwb 19,8\nmvva 64,32,128\ns_getpc_b64 s[88:89]\ndt 56,3,0\nmvva 64,36,144\n31:\n"
"dt 58,3,10240\nmvva 68,36,148\ns_and_b32 s89, s89, 0xffff\ndt 60,3,20480\nmvva 68,32,132\ns_mov_b32 s90, 39f-31b\ndt 62,3,30720\nmvva 72,32,136\n"
"s_mov_b32 s91, 0x20000\nmvva 72,36,152\nbuffer_load_dword v31, v23, s[88:91], s35 offen\nmvva 76,36,156\nmvva 76,32,140\nwl 8\nmvva 92,40,140\ndt 32,0,1024\n"
"mvva 92,44,156\ndt 34,0,11264\nmvva 88,44,152\ndt 36,0,21504\nmvva 88,40,136\ndt 38,0,31744\nmvva 84,40,132\nsm 81920\n"
"mvva 84,44,148\nlb 20,36,64,0\nmvva 80,44,144\nmvva 80,40,128\nwl 8\nmvva 96,48,128\ndt 40,1,1024\nmvva 96,52,144\n"
"dt 42,1,11264\nmvva 100,52,148\ndt 44,1,21504\nmvva 100,48,132\ndt 46,1,31744\nmvva 104,48,136\nmvva 104,52,152\nlb 20,40,64,1024\n"
"mvva 108,52,156\ns_add_u32 s64, s64, 229376\nmvva 108,48,140\nwl 8\nmvva 124,56,140\ndt 48,2,1024\nmvva 124,60,156\ndt 50,2,11264\n"
"mvva 120,60,152\ndt 52,2,21504\nmvva 120,56,136\ndt 54,2,31744\nmvva 116,56,132\nmvva 116,60,148\nlb 21,44,65,2048\nmvva 112,60,144\n"
"mvva 112,56,128\nwl 8\nmava 0,32,128\ndt 56,3,1024\nmava 0,36,144\ndt 58,3,11264\nmava 4,36,148\ndt 60,3,21504\n"
"mava 4,32,132\ndt 62,3,31744\nmava 8,32,136\nmava 8,36,152\nlb 21,48,65,3072\nmava 12,36,156\ns_add_u32 s65, s65, s60\nmava 12,32,140\n"
"wl 8\nmava 28,40,140\ndt 32,0,2048\nmava 28,44,156\ndt 34,0,12288\nmava 24,44,152\ndt 36,0,22528\nmava 24,40,136\n"
"dt 38,0,32768\nmava 20,40,132\nsm 86016\nmava 20,44,148\nlb 22,52,66,0\nmava 16,44,144\ns_add_u32 s66, s66, s61\nmava 16,40,128\n"
"wl 8\nmava 32,48,128\ndt 40,1,2048\nmava 32,52,144\ndt 42,1,12288\nmava 36,52,148\ndt 44,1,22528\nmava 36,48,132\n"
"dt 46,1,32768\nmava 40,48,136\nsm 102400\nmava 40,52,152\nlb 20,36,64,0\nmava 44,52,156\nmava 44,48,140\nwl 8\n"
"mava 60,56,140\ndt 48,2,2048\nmava 60,60,156\ndt 50,2,12288\nmava 56,60,152\ndt 52,2,22528\nmava 56,56,136\ndt 54,2,32768\n"
"mava 52,56,132\nmava 52,60,148\nlb 20,40,64,1024\nmava 48,60,144\ns_add_u32 s64, s64, 229376\nmava 48,56,128\nwl 8\nmvva 128,32,128\n"
"dt 56,3,2048\nmvva 128,36,144\ndt 58,3,12288\nmvva 132,36,148\ndt 60,3,22528\nmvva 132,32,132\ndt 62,3,32768\nmvva 136,32,136\n"
"mvva 136,36,152\nlb 21,44,65,2048\nmvva 140,36,156\nmvva 140,32,140\nwl 8\nmvva 156,40,140\ndt 32,0,3072\nmvva 156,44,156\n"
"dt 34,0,13312\nmvva 152,44,152\ndt 36,0,23552\nmvva 152,40,136\ndt 38,0,33792\nmvva 148,40,132\nmvva 148,44,148\nlb 21,48,65,3072\n"
"mvva 144,44,144\ns_add_u32 s65, s65, s60\nmvva 144,40,128\nwl 8\nmvva 160,48,128\ndt 40,1,3072\nmvva 160,52,144\ndt 42,1,13312\n"
"mvva 164,52,148\ndt 44,1,23552\nmvva 164,48,132\ndt 46,1,33792\nmvva 168,48,136\nsm 106496\nmvva 168,52,152\nlb 22,52,66,0\n"
"mvva 172,52,156\ns_add_u32 s66, s66, s61\nmvva 172,48,140\nwl 8\nmvva 188,56,140\ndt 48,2,3072\nmvva 188,60,156\ndt 50,2,13312\n"
"mvva 184,60,152\ndt 52,2,23552\nmvva 184,56,136\ndt 54,2,33792\nmvva 180,56,132\nmvva 180,60,148\nlc 192,23,67,0\nmvva 176,60,144\n"
"mvva 176,56,128\nwl 8\nmava 64,32,128\ndt 56,3,3072\nmava 64,36,144\ndt 58,3,13312\nmava 68,36,148\ndt 60,3,23552\n"
"mava 68,32,132\ndt 62,3,33792\nmava 72,32,136\nmava 72,36,152\nlc 196,23,67,1024\nmava 76,36,156\nmava 76,32,140\nwl 8\n"
"mava 92,40,140\ndt 32,0,4096\nmava 92,44,156\ndt 34,0,14336\nmava 88,44,152\ndt 36,0,24576\nmava 88,40,136\ndt 38,0,34816\n"
"mava 84,40,132\nmava 84,44,148\nlc 200,23,67,2048\nmava 80,44,144\nmava 80,40,128\nwl 8\nmava 96,48,128\ndt 40,1,4096\n"
"mava 96,52,144\ndt 42,1,14336\nmava 100,52,148\ndt 44,1,24576\nmava 100,48,132\ndt 46,1,34816\nmava 104,48,136\nmava 104,52,152\n"
"lc 204,23,67,3072\nmava 108,52,156\ns_add_u32 s67, s67, 4096\nmava 108,48,140\nwl 8\nmava 124,56,140\ndt 48,2,4096\nmava 124,60,156\n"
"dt 50,2,14336\nmava 120,60,152\ndt 52,2,24576\nmava 120,56,136\ndt 54,2,34816\nmava 116,56,132\nmava 116,60,148\nlc 208,23,67,0\n"
"mava 112,60,144\nmava 112,56,128\nwl 8\nmvva 192,32,128\ndt 56,3,4096\nmvva 192,36,144\ndt 58,3,14336\nmvva 196,36,148\n"
"dt 60,3,24576\nmvva 196,32,132\ndt 62,3,34816\nmvva 200,32,136\nmvva 200,36,152\nlc 212,23,67,1024\nmvva 204,36,156\ns_waitcnt vmcnt(25)\n"
"s_barrier\nmvva 204,32,140\nwl 8\nmvva 220,40,140\ndt 32,0,40960\nmvva 220,44,156\ndt 34,0,51200\nmvva 216,44,152\n"
"dt 36,0,61440\nmvva 216,40,136\ndt 38,6,6144\nmvva 212,40,132\nmvva 212,44,148\nlc 216,23,67,2048\nmvva 208,44,144\nmvva 208,40,128\n"
"wl 8\nmvva 224,48,128\ndt 40,1,40960\nmvva 224,52,144\ndt 42,1,51200\nmvva 228,52,148\ndt 44,1,61440\nmvva 228,48,132\n"
"dt 46,7,6144\nmvva 232,48,136\nmvva 232,52,152\nlc 220,23,67,3072\nmvva 236,52,156\ns_add_u32 s67, s67, 4096\nmvva 236,48,140\nwl 8\n"
"mvva 252,56,140\ndt 48,2,40960\nmvva 252,60,156\ndt 50,2,51200\nmvva 248,60,152\ndt 52,2,61440\nmvva 248,56,136\ndt 54,8,6144\n"
"mvva 244,56,132\nmvva 244,60,148\nmvva 240,60,144\nmvva 240,56,128\nwb 19,8\nmvva 64,32,160\ndt 56,3,40960\nmvva 64,36,176\n"
"dt 58,3,51200\nmvva 68,36,180\ndt 60,3,61440\nmvva 68,32,164\ndt 62,9,6144\nmvva 72,32,168\nmvva 72,36,184\nmvva 76,36,188\n"
"mvva 76,32,172\nwl 8\nmvva 92,40,172\ndt 32,0,41984\nmvva 92,44,188\ndt 34,0,52224\nmvva 88,44,184\ndt 36,0,62464\n"
"mvva 88,40,168\ndt 38,6,7168\nmvva 84,40,164\nsm 122880\nmvva 84,44,180\nlb 20,36,64,0\nmvva 80,44,176\nmvva 80,40,160\n"
"wl 8\nmvva 96,48,160\ndt 40,1,41984\nmvva 96,52,176\ndt 42,1,52224\nmvva 100,52,180\ndt 44,1,62464\nmvva 100,48,164\n"
"dt 46,7,7168\nmvva 104,48,168\nmvva 104,52,184\nlb 20,40,64,1024\nmvva 108,52,188\ns_movk_i32 s95, 1920\ns_waitcnt vmcnt(20)\nv_lshrrev_b32 v31, 7, v23\n"
"s_add_u32 s64, s64, 229376\nmvva 108,48,172\nv_mad_u32_u24 v31, v31, s95, v23\nwl 8\nmvva 124,56,172\ns_and_b32 s94, s32, 0x700\ns_sub_u32 s88, s8, s94\ns_subb_u32 s89, s9, 0\n"
"dt 48,2,41984\nmvva 124,60,188\ns_mov_b32 s90, 2097152\ns_mov_b32 s91, s11\ndt 50,2,52224\nmvva 120,60,184\ns_and_b32 s94, s76, 0x7c\ns_lshl_b32 s94, s94, 14\n"
"dt 52,2,62464\nmvva 120,56,168\ns_and_b32 s95, s35, 0xc00\ns_lshl_b32 s95, s95, 4\ns_add_u32 s94, s94, s95\ndt 54,8,7168\nmvva 116,56,164\nmvva 116,60,180\n"
"lb 21,44,65,2048\nmvva 112,60,176\nmvva 112,56,160\nbuffer_load_dwordx4 a[136:139], v31, s[88:91], s94 offen offset:384\nwl 8\nmava 0,32,160\ndt 56,3,41984\nmava 0,36,176\n"
"dt 58,3,52224\nmava 4,36,180\ndt 60,3,62464\nmava 4,32,164\ndt 62,9,7168\nmava 8,32,168\nmava 8,36,184\nlb 21,48,65,3072\n"
"mava 12,36,188\ns_add_u32 s65, s65, s60\nmava 12,32,172\nwl 8\nmava 28,40,172\ndt 32,0,43008\nmava 28,44,188\ndt 34,0,53248\n"
"mava 24,44,184\ndt 36,0,63488\nmava 24,40,168\ndt 38,6,8192\nmava 20,40,164\nsm 126976\nmava 20,44,180\nlb 22,52,66,0\n"
"mava 16,44,176\ns_add_u32 s66, s66, s61\nmava 16,40,160\nbuffer_load_dwordx4 a[140:143], v31, s[88:91], s94 offen offset:1408\nwl 8\nmava 32,48,160\ndt 40,1,43008\nmava 32,52,176\n"
"dt 42,1,53248\nmava 36,52,180\ndt 44,1,63488\nmava 36,48,164\ndt 46,7,8192\nmava 40,48,168\nsm 143360\nmava 40,52,184\n"
"lb 20,36,64,0\nmava 44,52,188\nmava 44,48,172\nwl 8\nmava 60,56,172\ndt 48,2,43008\nmava 60,60,188\ndt 50,2,53248\n"
"mava 56,60,184\ndt 52,2,63488\nmava 56,56,168\ndt 54,8,8192\nmava 52,56,164\nmava 52,60,180\nlb 20,40,64,1024\nmava 48,60,176\n"
"s_add_u32 s64, s64, 229376\nmava 48,56,160\nwl 8\nmvva 128,32,160\ndt 56,3,43008\nmvva 128,36,176\ndt 58,3,53248\nmvva 132,36,180\n"
"dt 60,3,63488\nmvva 132,32,164\ndt 62,9,8192\nmvva 136,32,168\nmvva 136,36,184\nlb 21,44,65,2048\nmvva 140,36,188\nmvva 140,32,172\n"
"wl 8\nmvva 156,40,172\ndt 32,0,44032\nmvva 156,44,188\ndt 34,0,54272\nmvva 152,44,184\ndt 36,0,64512\nmvva 152,40,168\n"
"dt 38,6,9216\nmvva 148,40,164\nmvva 148,44,180\nlb 21,48,65,3072\nmvva 144,44,176\ns_add_u32 s65, s65, s60\nmvva 144,40,160\nwl 8\n"
"mvva 160,48,160\ndt 40,1,44032\nmvva 160,52,176\ndt 42,1,54272\nmvva 164,52,180\ndt 44,1,64512\nmvva 164,48,164\ndt 46,7,9216\n"
"mvva 168,48,168\nsm 147456\nmvva 168,52,184\nlb 22,52,66,0\nmvva 172,52,188\ns_add_u32 s66, s66, s61\nmvva 172,48,172\nwl 8\n"
"mvva 188,56,172\ndt 48,2,44032\nmvva 188,60,188\ndt 50,2,54272\nmvva 184,60,184\ndt 52,2,64512\nmvva 184,56,168\ndt 54,8,9216\n"
"mvva 180,56,164\nmvva 180,60,180\nlc 224,23,67,0\nmvva 176,60,176\nmvva 176,56,160\nwl 8\nmava 64,32,160\ndt 56,3,44032\n"
"mava 64,36,176\ndt 58,3,54272\nmava 68,36,180\ndt 60,3,64512\nmava 68,32,164\ndt 62,9,9216\nmava 72,32,168\nmava 72,36,184\n"
"lc 228,23,67,1024\nmava 76,36,188\nmava 76,32,172\nwl 8\nmava 92,40,172\ndt 32,0,45056\nmava 92,44,188\ndt 34,0,55296\n"
"mava 88,44,184\ndt 36,6,0\nmava 88,40,168\ndt 38,6,10240\nmava 84,40,164\nmava 84,44,180\nlc 232,23,67,2048\nmava 80,44,176\n"
"mava 80,40,160\nwl 8\nmava 96,48,160\ndt 40,1,45056\nmava 96,52,176\ndt 42,1,55296\nmava 100,52,180\ndt 44,7,0\n"
"mava 100,48,164\ndt 46,7,10240\nmava 104,48,168\nmava 104,52,184\nlc 236,23,67,3072\nmava 108,52,188\ns_add_u32 s67, s67, 4096\nmava 108,48,172\n"
"wl 8\nmava 124,56,172\ndt 48,2,45056\nmava 124,60,188\ndt 50,2,55296\nmava 120,60,184\ndt 52,8,0\nmava 120,56,168\n"
"dt 54,8,10240\nmava 116,56,164\nmava 116,60,180\nlc 240,23,67,0\nmava 112,60,176\nmava 112,56,160\nwl 8\nmvva 192,32,160\n"
"dt 56,3,45056\nmvva 192,36,176\ndt 58,3,55296\nmvva 196,36,180\ndt 60,9,0\nmvva 196,32,164\ndt 62,9,10240\nmvva 200,32,168\n"
"mvva 200,36,184\nlc 244,23,67,1024\nmvva 204,36,188\ns_waitcnt vmcnt(26)\ns_barrier\nmvva 204,32,172\nwl 8\nmvva 220,40,172\n"
"dt 32,6,16384\nmvva 220,44,188\ndt 34,6,26624\nmvva 216,44,184\ndt 36,6,36864\nmvva 216,40,168\ndt 38,6,47104\nmvva 212,40,164\n"
"mvva 212,44,180\nlc 248,23,67,2048\nmvva 208,44,176\nmvva 208,40,160\nwl 8\nmvva 224,48,160\ndt 40,7,16384\nmvva 224,52,176\n"
"dt 42,7,26624\nmvva 228,52,180\ndt 44,7,36864\nmvva 228,48,164\ndt 46,7,47104\nmvva 232,48,168\nmvva 232,52,184\nlc 252,23,67,3072\n"
"mvva 236,52,188\ns_add_u32 s67, s67, 4096\nmvva 236,48,172\nwl 8\nmvva 252,56,172\ndt 48,8,16384\nmvva 252,60,188\ndt 50,8,26624\n"
"mvva 248,60,184\ndt 52,8,36864\nmvva 248,56,168\ndt 54,8,47104\nmvva 244,56,164\nmvva 244,60,180\nmvva 240,60,176\nmvva 240,56,160\n"
"wb 20,8\nmvva 64,32,192\ndt 56,9,16384\nmvva 64,36,208\ndt 58,9,26624\nmvva 68,36,212\ndt 60,9,36864\nmvva 68,32,196\n"
"dt 62,9,47104\nmvva 72,32,200\nmvva 72,36,216\nmvva 76,36,220\nmvva 76,32,204\nwl 8\nmvva 92,40,204\ndt 32,6,17408\n"
"mvva 92,44,220\ndt 34,6,27648\nmvva 88,44,216\ndt 36,6,37888\nmvva 88,40,200\ndt 38,6,48128\nmvva 84,40,196\nmvva 84,44,212\n"
"s_lshr_b32 s86, s35, 14\ns_mul_i32 s86, s86, 262144\ns_and_b32 s87, s35, 16383\ns_lshl_b32 s87, s87, 2\ns_add_u32 s86, s86, s87\nlc 176,23,33,0\nmvva 80,44,208\ns_and_b32 s87, s32, 0x700\n"
"s_sub_u32 s87, 0, s87\ns_add_u32 s86, s86, s87\ns_max_i32 s86, s86, 0\nv_lshlrev_b32 v31, 2, v23\nmvva 80,40,192\nbuffer_load_dword a128, v31, s[4:7], s86 offen\nwl 8\nmvva 96,48,192\n"
"dt 40,7,17408\nmvva 96,52,208\ndt 42,7,27648\nmvva 100,52,212\ndt 44,7,37888\nmvva 100,48,196\ndt 46,7,48128\nmvva 104,48,200\n"
"mvva 104,52,216\nlc 180,23,33,1024\nmvva 108,52,220\nmvva 108,48,204\nwl 8\nmvva 124,56,204\ndt 48,8,17408\nmvva 124,60,220\n"
"dt 50,8,27648\nmvva 120,60,216\ndt 52,8,37888\nmvva 120,56,200\ndt 54,8,48128\nmvva 116,56,196\nmvva 116,60,212\nlc 184,23,33,2048\n"
"mvva 112,60,208\nmvva 112,56,192\nwl 8\nmava 0,32,192\ndt 56,9,17408\nmava 0,36,208\ndt 58,9,27648\nmava 4,36,212\n"
"dt 60,9,37888\nmava 4,32,196\ndt 62,9,48128\nmava 8,32,200\nmava 8,36,216\nlc 188,23,33,3072\nmava 12,36,220\nmava 12,32,204\n"
"wl 8\nmava 28,40,204\ndt 32,6,18432\nmava 28,44,220\ndt 34,6,28672\nmava 24,44,216\ndt 36,6,38912\nmava 24,40,200\n"
"s_mov_b32 s65, 0\ndt 38,6,49152\nmava 20,40,196\nsm 0\nmava 20,44,212\nlb 18,4,65,0\nmava 16,44,208\nmava 16,40,192\n"
"wl 8\nmava 32,48,192\ndt 40,7,18432\nmava 32,52,208\ndt 42,7,28672\nmava 36,52,212\ndt 44,7,38912\nmava 36,48,196\n"
"dt 46,7,49152\nmava 40,48,200\nsm 1024\nmava 40,52,216\nlb 19,4,65,0\nmava 44,52,220\nmava 44,48,204\nwl 8\n"
"mava 60,56,204\ndt 48,8,18432\nmava 60,60,220\ndt 50,8,28672\nmava 56,60,216\ndt 52,8,38912\nmava 56,56,200\ns_mov_b32 s64, 262144\n"
"dt 54,8,49152\nmava 52,56,196\nsm 2048\nmava 52,60,212\nlb 18,4,64,0\nmava 48,60,208\nmava 48,56,192\nwl 8\n"
"mvva 128,32,192\ndt 56,9,18432\nmvva 128,36,208\ndt 58,9,28672\nmvva 132,36,212\ndt 60,9,38912\nmvva 132,32,196\ndt 62,9,49152\n"
"mvva 136,32,200\nsm 3072\nmvva 136,36,216\nlb 19,4,64,0\nmvva 140,36,220\nmvva 140,32,204\nwl 8\nmvva 156,40,204\n"
"dt 32,6,19456\nmvva 156,44,220\ndt 34,6,29696\nmvva 152,44,216\ndt 36,6,39936\nmvva 152,40,200\ns_mov_b32 s65, 524288\ndt 38,6,50176\n"
"mvva 148,40,196\nsm 4096\nmvva 148,44,212\nlb 18,4,65,0\nmvva 144,44,208\nmvva 144,40,192\nwl 8\nmvva 160,48,192\n"
"dt 40,7,19456\nmvva 160,52,208\ndt 42,7,29696\nmvva 164,52,212\ndt 44,7,39936\nmvva 164,48,196\ndt 46,7,50176\nmvva 168,48,200\n"
"sm 20480\nmvva 168,52,216\nlb 19,4,65,0\nmvva 172,52,220\nmvva 172,48,204\nwl 8\nmvva 188,56,204\ndt 48,8,19456\n"
"mvva 188,60,220\ndt 50,8,29696\nmvva 184,60,216\ndt 52,8,39936\nmvva 184,56,200\ns_mov_b32 s64, 786432\ndt 54,8,50176\nmvva 180,56,196\n"
"sm 21504\nmvva 180,60,212\ns_lshr_b32 s86, s35, 14\ns_mul_i32 s86, s86, 262144\ns_and_b32 s87, s35, 16383\ns_lshl_b32 s87, s87, 2\ns_add_u32 s86, s86, s87\nlb 18,4,64,0\n"
"mvva 176,60,208\ns_and_b32 s87, s32, 0x700\ns_sub_u32 s87, 2097152, s87\ns_add_u32 s86, s86, s87\ns_max_i32 s86, s86, 0\nv_lshlrev_b32 v31, 2, v23\nmvva 176,56,192\nbuffer_load_dword a129, v31, s[4:7], s86 offen\n"
"wl 8\nmava 64,32,192\ndt 56,9,19456\nmava 64,36,208\ndt 58,9,29696\nmava 68,36,212\ndt 60,9,39936\nmava 68,32,196\n"
"dt 62,9,50176\nmava 72,32,200\nsm 22528\nmava 72,36,216\nlb 19,4,64,0\nmava 76,36,220\nmava 76,32,204\nwl 8\n"
"mava 92,40,204\ndt 32,6,20480\nmava 92,44,220\ndt 34,6,30720\nmava 88,44,216\ndt 36,6,40960\nmava 88,40,200\ns_mov_b32 s65, 1048576\n"
"dt 38,6,51200\nmava 84,40,196\nsm 23552\nmava 84,44,212\nlb 18,4,65,0\nmava 80,44,208\nmava 80,40,192\nwl 8\n"
"mava 96,48,192\ndt 40,7,20480\nmava 96,52,208\ndt 42,7,30720\nmava 100,52,212\ndt 44,7,40960\nmava 100,48,196\ndt 46,7,51200\n"
"mava 104,48,200\nsm 24576\nmava 104,52,216\nlb 19,4,65,0\nmava 108,52,220\nmava 108,48,204\nwl 8\nmava 124,56,204\n"
"dt 48,8,20480\nmava 124,60,220\ndt 50,8,30720\nmava 120,60,216\ndt 52,8,40960\nmava 120,56,200\ndt 54,8,51200\nmava 116,56,196\n"
"mava 116,60,212\nmava 112,60,208\nmava 112,56,192\nwl 8\nmvva 192,32,192\ndt 56,9,20480\nmvva 192,36,208\ndt 58,9,30720\n"
"mvva 196,36,212\ndt 60,9,40960\nmvva 196,32,196\ndt 62,9,51200\nmvva 200,32,200\nmvva 200,36,216\nmvva 204,36,220\ns_waitcnt vmcnt(24)\n"
"s_barrier\nmvva 204,32,204\nwl 8\nmvva 220,40,204\ndt 32,6,57344\nmvva 220,44,220\ndt 34,12,2048\nmvva 216,44,216\n"
"dt 36,12,12288\nmvva 216,40,200\ndt 38,12,22528\nmvva 212,40,196\nmvva 212,44,212\nmvva 208,44,208\nmvva 208,40,192\nwl 8\n"
"mvva 224,48,192\ndt 40,7,57344\nmvva 224,52,208\ndt 42,13,2048\nmvva 228,52,212\ndt 44,13,12288\nmvva 228,48,196\ndt 46,13,22528\n"
"mvva 232,48,200\nmvva 232,52,216\nmvva 236,52,220\nmvva 236,48,204\nwl 8\nmvva 252,56,204\ndt 48,8,57344\nmvva 252,60,220\n"
"dt 50,14,2048\nmvva 248,60,216\ndt 52,14,12288\nmvva 248,56,200\ndt 54,14,22528\nmvva 244,56,196\nmvva 244,60,212\nmvva 240,60,208\n"
"mvva 240,56,192\nwb 16,8\nmvva 64,32,224\ndt 56,9,57344\nmvva 64,36,240\ndt 58,15,2048\nmvva 68,36,244\ndt 60,15,12288\n"
"mvva 68,32,228\ndt 62,15,22528\nmvva 72,32,232\nmvva 72,36,248\nmvva 76,36,252\nmvva 76,32,236\nwl 8\nmvva 92,40,236\n"
"dt 32,6,58368\nmvva 92,44,252\ndt 34,12,3072\nmvva 88,44,248\ndt 36,12,13312\nmvva 88,40,232\ns_mov_b32 s64, 1310720\ndt 38,12,23552\n"
"mvva 84,40,228\nsm 40960\nmvva 84,44,244\ns_lshr_b32 s86, s35, 14\ns_mul_i32 s86, s86, 262144\ns_and_b32 s87, s35, 16383\ns_lshl_b32 s87, s87, 2\ns_add_u32 s86, s86, s87\n"
"lb 18,4,64,0\nmvva 80,44,240\ns_and_b32 s87, s32, 0x700\ns_sub_u32 s87, 65536, s87\ns_add_u32 s86, s86, s87\ns_max_i32 s86, s86, 0\nv_lshlrev_b32 v31, 2, v23\nmvva 80,40,224\n"
"buffer_load_dword a130, v31, s[4:7], s86 offen\nwl 8\nmvva 96,48,224\ndt 40,7,58368\nmvva 96,52,240\ndt 42,13,3072\nmvva 100,52,244\ndt 44,13,13312\n"
"mvva 100,48,228\ndt 46,13,23552\nmvva 104,48,232\nsm 41984\nmvva 104,52,248\nlb 19,4,64,0\nmvva 108,52,252\nmvva 108,48,236\n"
"wl 8\nmvva 124,56,236\ndt 48,8,58368\nmvva 124,60,252\ndt 50,14,3072\nmvva 120,60,248\ndt 52,14,13312\nmvva 120,56,232\n"
"s_mov_b32 s65, 1572864\ndt 54,14,23552\nmvva 116,56,228\nsm 43008\nmvva 116,60,244\nlb 18,4,65,0\nmvva 112,60,240\nmvva 112,56,224\n"
"wl 8\nmava 0,32,224\ndt 56,9,58368\nmava 0,36,240\ndt 58,15,3072\nmava 4,36,244\ndt 60,15,13312\nmava 4,32,228\n"
"dt 62,15,23552\nmava 8,32,232\nsm 44032\nmava 8,36,248\nlb 19,4,65,0\nmava 12,36,252\nmava 12,32,236\nwl 8\n"
"mava 28,40,236\ndt 32,6,59392\nmava 28,44,252\ndt 34,12,4096\nmava 24,44,248\ndt 36,12,14336\nmava 24,40,232\ns_mov_b32 s64, 1835008\n"
"dt 38,12,24576\nmava 20,40,228\nsm 45056\nmava 20,44,244\nlb 18,4,64,0\nmava 16,44,240\nmava 16,40,224\nwl 8\n"
"mava 32,48,224\ndt 40,7,59392\nmava 32,52,240\ndt 42,13,4096\nmava 36,52,244\ndt 44,13,14336\nmava 36,48,228\ndt 46,13,24576\n"
"mava 40,48,232\nsm 61440\nmava 40,52,248\nlb 19,4,64,0\nmava 44,52,252\nmava 44,48,236\nwl 8\nmava 60,56,236\n"
"dt 48,8,59392\nmava 60,60,252\ndt 50,14,4096\nmava 56,60,248\ndt 52,14,14336\nmava 56,56,232\ns_mov_b32 s65, 2097152\ndt 54,14,24576\n"
"mava 52,56,228\nsm 62464\nmava 52,60,244\nlb 18,4,65,0\nmava 48,60,240\nmava 48,56,224\nwl 8\nmvva 128,32,224\n"
"dt 56,9,59392\nmvva 128,36,240\ns_and_b32 s94, s76, 0x7c\ns_lshl_b32 s94, s94, 14\ndt 58,15,4096\nmvva 132,36,244\ns_and_b32 s95, s76, 0x700\ns_lshl_b32 s95, s95, 5\n"
"s_add_u32 s94, s94, s95\ndt 60,15,14336\nmvva 132,32,228\ns_and_b32 s95, s35, 0xc00\ns_add_u32 s94, s94, s95\ns_add_u32 s94, s94, 37748736\ndt 62,15,24576\nmvva 136,32,232\n"
"sm 63488\nmvva 136,36,248\nlb 19,4,65,0\nmvva 140,36,252\nmvva 140,32,236\nbuffer_store_dwordx4 a[136:139], v23, s[56:59], s94 offen sc1\nwl 8\nmvva 156,40,236\n"
"dt 32,6,60416\nmvva 156,44,252\ndt 34,12,5120\nmvva 152,44,248\ndt 36,12,15360\nmvva 152,40,232\ns_mov_b32 s64, 2359296\ndt 38,12,25600\n"
"mvva 148,40,228\nsm 64512\nmvva 148,44,244\nlb 18,4,64,0\nmvva 144,44,240\nmvva 144,40,224\nwl 8\nmvva 160,48,224\n"
"dt 40,7,60416\nmvva 160,52,240\ndt 42,13,5120\nmvva 164,52,244\ndt 44,13,15360\nmvva 164,48,228\ns_add_u32 s94, s94, 4096\ndt 46,13,25600\n"
"mvva 168,48,232\nsm 65536\nmvva 168,52,248\nlb 19,4,64,0\nmvva 172,52,252\nmvva 172,48,236\nbuffer_store_dwordx4 a[140:143], v23, s[56:59], s94 offen sc1\nwl 8\n"
"mvva 188,56,236\ndt 48,8,60416\nmvva 188,60,252\ndt 50,14,5120\nmvva 184,60,248\ndt 52,14,15360\nmvva 184,56,232\ndt 54,14,25600\n"
"mvva 180,56,228\nmvva 180,60,244\ns_lshr_b32 s86, s35, 14\ns_mul_i32 s86, s86, 262144\ns_and_b32 s87, s35, 16383\ns_lshl_b32 s87, s87, 2\ns_add_u32 s86, s86, s87\nmvva 176,60,240\n"
"s_and_b32 s87, s32, 0x700\ns_sub_u32 s87, 2162688, s87\ns_add_u32 s86, s86, s87\ns_max_i32 s86, s86, 0\nv_lshlrev_b32 v31, 2, v23\nmvva 176,56,224\nbuffer_load_dword a131, v31, s[4:7], s86 offen\nwl 8\n"
"mava 64,32,224\ndt 56,9,60416\nmava 64,36,240\ndt 58,15,5120\nmava 68,36,244\ndt 60,15,15360\nmava 68,32,228\ndt 62,15,25600\n"
"mava 72,32,232\nmava 72,36,248\nmava 76,36,252\nmava 76,32,236\nwl 8\nmava 92,40,236\ndt 32,6,61440\nmava 92,44,252\n"
"dt 34,12,6144\nmava 88,44,248\ndt 36,12,16384\nmava 88,40,232\ndt 38,12,26624\nmava 84,40,228\nmava 84,44,244\nmava 80,44,240\n"
"mava 80,40,224\nwl 8\nmava 96,48,224\ndt 40,7,61440\nmava 96,52,240\ndt 42,13,6144\nmava 100,52,244\ndt 44,13,16384\n"
"mava 100,48,228\ndt 46,13,26624\nmava 104,48,232\nmava 104,52,248\nmava 108,52,252\nmava 108,48,236\nwl 8\nmava 124,56,236\n"
"dt 48,8,61440\nmava 124,60,252\ndt 50,14,6144\nmava 120,60,248\ndt 52,14,16384\nmava 120,56,232\ndt 54,14,26624\nmava 116,56,228\n"
"mava 116,60,244\nmava 112,60,240\nmava 112,56,224\nwl 8\nmvva 192,32,224\ndt 56,9,61440\nmvva 192,36,240\ndt 58,15,6144\n"
"mvva 196,36,244\ndt 60,15,16384\nmvva 196,32,228\ndt 62,15,26624\nmvva 200,32,232\nmvva 200,36,248\nmvva 204,36,252\nmvva 204,32,236\n"
"wl 8\nmvva 220,40,236\nmvva 220,44,252\nmvva 216,44,248\nmvva 216,40,232\nmvva 212,40,228\nmvva 212,44,244\nmvva 208,44,240\n"
"mvva 208,40,224\nwl 4\nmvva 224,48,224\nmvva 224,52,240\nmvva 228,52,244\nmvva 228,48,228\nmvva 232,48,232\nmvva 232,52,248\n"
"mvva 236,52,252\nmvva 236,48,236\nwl 0\nmvva 252,56,236\nmvva 252,60,252\nmvva 248,60,248\nmvva 248,56,232\nmvva 244,56,228\n"
"mvva 244,60,244\nmvva 240,60,240\nmvva 240,56,224\nv_lshrrev_b32 v21, 7, v23\nv_cmp_eq_u32 vcc, 3, v21\nv_add_u32 v21, 37748864, v23\ns_nop 0\nv_cndmask_b32 v21, v23, v21, vcc\n"
"s_barrier\ns_add_u32 s86, s72, 4096\ns_add_u32 s87, s72, 8192\ns_add_u32 s88, s72, 12288\nv_mov_b32 v31, 0x3f803f80\nv_accvgpr_write_b32 a240, v31\nv_accvgpr_write_b32 a241, v31\nv_accvgpr_write_b32 a242, v31\n"
"v_accvgpr_write_b32 a243, v31\ns_mov_b32 s65, 2621440\nsm 81920\nv_pk_mul_f32 v[64:65], v[64:65], s[82:83]\nlb 18,4,65,0\nv_pk_mul_f32 v[66:67], v[66:67], s[82:83]\nv_pk_mul_f32 v[80:81], v[80:81], s[82:83]\nv_pk_mul_f32 v[82:83], v[82:83], s[82:83]\n"
"ve 64\nve 65\nve 66\nve 67\nve 80\nve 81\nve 82\nve 83\n"
"v_pk_add_f32 v[64:65], v[64:65], s[84:85]\nv_pk_add_f32 v[66:67], v[66:67], s[84:85]\nv_pk_add_f32 v[80:81], v[80:81], s[84:85]\nv_pk_add_f32 v[82:83], v[82:83], s[84:85]\nvr 64\nvr 65\nvr 66\nvr 67\n"
"vr 80\nvr 81\nvr 82\nvr 83\nar 32,0\nar 33,1\nar 34,2\nar 35,3\n"
"ar 36,16\nar 37,17\nar 38,18\nar 39,19\nsm 82944\nv_pk_mul_f32 v[64:65], v[64:65], v[32:33]\nlb 19,4,65,0\nv_pk_mul_f32 v[66:67], v[66:67], v[34:35]\n"
"v_pk_mul_f32 v[80:81], v[80:81], v[36:37]\nv_pk_mul_f32 v[82:83], v[82:83], v[38:39]\ncv 64,64,65\ncv 65,66,67\ncv 66,80,81\ncv 67,82,83\nbuffer_store_dwordx4 v[64:67], v21, s[56:59], s72 offen offset:0 sc1\nv_pk_mul_f32 v[68:69], v[68:69], s[82:83]\n"
"v_pk_mul_f32 v[70:71], v[70:71], s[82:83]\nv_pk_mul_f32 v[84:85], v[84:85], s[82:83]\nv_pk_mul_f32 v[86:87], v[86:87], s[82:83]\nve 68\nve 69\nve 70\nve 71\nve 84\n"
"ve 85\nve 86\nve 87\nv_pk_add_f32 v[68:69], v[68:69], s[84:85]\nv_pk_add_f32 v[70:71], v[70:71], s[84:85]\nv_pk_add_f32 v[84:85], v[84:85], s[84:85]\nv_pk_add_f32 v[86:87], v[86:87], s[84:85]\nvr 68\n"
"vr 69\nvr 70\nvr 71\nvr 84\nvr 85\nvr 86\nvr 87\ns_mov_b32 s64, 2883584\n"
"sm 83968\nar 40,4\nlb 18,4,64,0\nar 41,5\nar 42,6\nar 43,7\nar 44,20\nar 45,21\n"
"ar 46,22\nar 47,23\nv_pk_mul_f32 v[68:69], v[68:69], v[40:41]\nv_pk_mul_f32 v[70:71], v[70:71], v[42:43]\nv_pk_mul_f32 v[84:85], v[84:85], v[44:45]\nv_pk_mul_f32 v[86:87], v[86:87], v[46:47]\ncv 68,68,69\ncv 69,70,71\n"
"cv 70,84,85\ncv 71,86,87\nbuffer_store_dwordx4 v[68:71], v21, s[56:59], s72 offen offset:1024 sc1\nv_pk_mul_f32 v[72:73], v[72:73], s[82:83]\nv_pk_mul_f32 v[74:75], v[74:75], s[82:83]\nv_pk_mul_f32 v[88:89], v[88:89], s[82:83]\nv_pk_mul_f32 v[90:91], v[90:91], s[82:83]\nve 72\n"
"ve 73\nve 74\nve 75\nve 88\nve 89\nve 90\nve 91\nv_pk_add_f32 v[72:73], v[72:73], s[84:85]\n"
"v_pk_add_f32 v[74:75], v[74:75], s[84:85]\nv_pk_add_f32 v[88:89], v[88:89], s[84:85]\nv_pk_add_f32 v[90:91], v[90:91], s[84:85]\nsm 84992\nvr 72\nlb 19,4,64,0\nvr 73\nvr 74\n"
"vr 75\nvr 88\nvr 89\nvr 90\nvr 91\nar 48,8\nar 49,9\nar 50,10\n"
"ar 51,11\nar 52,24\nar 53,25\nar 54,26\nar 55,27\nv_pk_mul_f32 v[72:73], v[72:73], v[48:49]\nv_pk_mul_f32 v[74:75], v[74:75], v[50:51]\nv_pk_mul_f32 v[88:89], v[88:89], v[52:53]\n"
"v_pk_mul_f32 v[90:91], v[90:91], v[54:55]\ncv 72,72,73\ncv 73,74,75\ncv 74,88,89\ncv 75,90,91\nbuffer_store_dwordx4 v[72:75], v21, s[56:59], s72 offen offset:2048 sc1\nv_pk_mul_f32 v[76:77], v[76:77], s[82:83]\nv_pk_mul_f32 v[78:79], v[78:79], s[82:83]\n"
"v_pk_mul_f32 v[92:93], v[92:93], s[82:83]\nv_pk_mul_f32 v[94:95], v[94:95], s[82:83]\nve 76\nve 77\nve 78\nve 79\ns_mov_b32 s65, 3145728\nsm 86016\n"
"ve 92\nlb 18,4,65,0\nve 93\nve 94\nve 95\nv_pk_add_f32 v[76:77], v[76:77], s[84:85]\nv_pk_add_f32 v[78:79], v[78:79], s[84:85]\nv_pk_add_f32 v[92:93], v[92:93], s[84:85]\n"
"v_pk_add_f32 v[94:95], v[94:95], s[84:85]\nvr 76\nvr 77\nvr 78\nvr 79\nvr 92\nvr 93\nvr 94\n"
"vr 95\nar 56,12\nar 57,13\nar 58,14\nar 59,15\nar 60,28\nar 61,29\nar 62,30\n"
"ar 63,31\nv_pk_mul_f32 v[76:77], v[76:77], v[56:57]\nv_pk_mul_f32 v[78:79], v[78:79], v[58:59]\nv_pk_mul_f32 v[92:93], v[92:93], v[60:61]\nv_pk_mul_f32 v[94:95], v[94:95], v[62:63]\ncv 76,76,77\ncv 77,78,79\ncv 78,92,93\n"
"cv 79,94,95\nbuffer_store_dwordx4 v[76:79], v21, s[56:59], s72 offen offset:3072 sc1\nsm 102400\nv_pk_mul_f32 v[96:97], v[96:97], s[82:83]\nlb 19,4,65,0\nv_pk_mul_f32 v[98:99], v[98:99], s[82:83]\nv_pk_mul_f32 v[112:113], v[112:113], s[82:83]\nv_pk_mul_f32 v[114:115], v[114:115], s[82:83]\n"
"ve 96\nve 97\nve 98\nve 99\nve 112\nve 113\nve 114\nve 115\n"
"v_pk_add_f32 v[96:97], v[96:97], s[84:85]\nv_pk_add_f32 v[98:99], v[98:99], s[84:85]\nv_pk_add_f32 v[112:113], v[112:113], s[84:85]\nv_pk_add_f32 v[114:115], v[114:115], s[84:85]\nvr 96\nvr 97\nvr 98\nvr 99\n"
"vr 112\nvr 113\nvr 114\nvr 115\nar 32,32\nar 33,33\nar 34,34\nar 35,35\n"
"ar 36,48\nar 37,49\nar 38,50\nar 39,51\ns_mov_b32 s64, 3407872\nsm 103424\nv_pk_mul_f32 v[96:97], v[96:97], v[32:33]\nlb 18,4,64,0\n"
"v_pk_mul_f32 v[98:99], v[98:99], v[34:35]\nv_pk_mul_f32 v[112:113], v[112:113], v[36:37]\nv_pk_mul_f32 v[114:115], v[114:115], v[38:39]\ncv 96,96,97\ncv 97,98,99\ncv 98,112,113\ncv 99,114,115\nbuffer_store_dwordx4 v[96:99], v21, s[56:59], s86 offen offset:0 sc1\n"
"v_pk_mul_f32 v[100:101], v[100:101], s[82:83]\nv_pk_mul_f32 v[102:103], v[102:103], s[82:83]\nv_pk_mul_f32 v[116:117], v[116:117], s[82:83]\nv_pk_mul_f32 v[118:119], v[118:119], s[82:83]\nve 100\nve 101\nve 102\nve 103\n"
"ve 116\nve 117\nve 118\nve 119\nv_pk_add_f32 v[100:101], v[100:101], s[84:85]\nv_pk_add_f32 v[102:103], v[102:103], s[84:85]\nv_pk_add_f32 v[116:117], v[116:117], s[84:85]\nv_pk_add_f32 v[118:119], v[118:119], s[84:85]\n"
"vr 100\nvr 101\nvr 102\nvr 103\nvr 116\nvr 117\nvr 118\nvr 119\n"
"sm 104448\nar 40,36\nlb 19,4,64,0\nar 41,37\nar 42,38\nar 43,39\nar 44,52\nar 45,53\n"
"ar 46,54\nar 47,55\nv_pk_mul_f32 v[100:101], v[100:101], v[40:41]\nv_pk_mul_f32 v[102:103], v[102:103], v[42:43]\nv_pk_mul_f32 v[116:117], v[116:117], v[44:45]\nv_pk_mul_f32 v[118:119], v[118:119], v[46:47]\ncv 100,100,101\ncv 101,102,103\n"
"cv 102,116,117\ncv 103,118,119\nbuffer_store_dwordx4 v[100:103], v21, s[56:59], s86 offen offset:1024 sc1\nv_pk_mul_f32 v[104:105], v[104:105], s[82:83]\nv_pk_mul_f32 v[106:107], v[106:107], s[82:83]\nv_pk_mul_f32 v[120:121], v[120:121], s[82:83]\nv_pk_mul_f32 v[122:123], v[122:123], s[82:83]\nve 104\n"
"ve 105\nve 106\nve 107\nve 120\nve 121\nve 122\nve 123\nv_pk_add_f32 v[104:105], v[104:105], s[84:85]\n"
"v_pk_add_f32 v[106:107], v[106:107], s[84:85]\nv_pk_add_f32 v[120:121], v[120:121], s[84:85]\nv_pk_add_f32 v[122:123], v[122:123], s[84:85]\ns_mov_b32 s65, 3670016\nsm 105472\nvr 104\nlb 18,4,65,0\nvr 105\n"
"vr 106\nvr 107\nvr 120\nvr 121\nvr 122\nvr 123\nar 48,40\nar 49,41\n"
"ar 50,42\nar 51,43\nar 52,56\nar 53,57\nar 54,58\nar 55,59\nv_pk_mul_f32 v[104:105], v[104:105], v[48:49]\nv_pk_mul_f32 v[106:107], v[106:107], v[50:51]\n"
"v_pk_mul_f32 v[120:121], v[120:121], v[52:53]\nv_pk_mul_f32 v[122:123], v[122:123], v[54:55]\ncv 104,104,105\ncv 105,106,107\ncv 106,120,121\ncv 107,122,123\nbuffer_store_dwordx4 v[104:107], v21, s[56:59], s86 offen offset:2048 sc1\nv_pk_mul_f32 v[108:109], v[108:109], s[82:83]\n"
"v_pk_mul_f32 v[110:111], v[110:111], s[82:83]\nv_pk_mul_f32 v[124:125], v[124:125], s[82:83]\nv_pk_mul_f32 v[126:127], v[126:127], s[82:83]\nve 108\nve 109\nve 110\nve 111\nsm 106496\n"
"ve 124\nlb 19,4,65,0\nve 125\nve 126\nve 127\nv_pk_add_f32 v[108:109], v[108:109], s[84:85]\nv_pk_add_f32 v[110:111], v[110:111], s[84:85]\nv_pk_add_f32 v[124:125], v[124:125], s[84:85]\n"
"v_pk_add_f32 v[126:127], v[126:127], s[84:85]\nvr 108\nvr 109\nvr 110\nvr 111\nvr 124\nvr 125\nvr 126\n"
"vr 127\nar 56,44\nar 57,45\nar 58,46\nar 59,47\nar 60,60\nar 61,61\nar 62,62\n"
"ar 63,63\nv_pk_mul_f32 v[108:109], v[108:109], v[56:57]\nv_pk_mul_f32 v[110:111], v[110:111], v[58:59]\nv_pk_mul_f32 v[124:125], v[124:125], v[60:61]\nv_pk_mul_f32 v[126:127], v[126:127], v[62:63]\ncv 108,108,109\ncv 109,110,111\ncv 110,124,125\n"
"cv 111,126,127\nbuffer_store_dwordx4 v[108:111], v21, s[56:59], s86 offen offset:3072 sc1\ns_mov_b32 s64, 3932160\nsm 122880\nv_pk_mul_f32 v[128:129], v[128:129], s[82:83]\nlb 18,4,64,0\nv_pk_mul_f32 v[130:131], v[130:131], s[82:83]\nv_pk_mul_f32 v[144:145], v[144:145], s[82:83]\n"
"v_pk_mul_f32 v[146:147], v[146:147], s[82:83]\nve 128\nve 129\nve 130\nve 131\nve 144\nve 145\nve 146\n"
"ve 147\nv_pk_add_f32 v[128:129], v[128:129], s[84:85]\nv_pk_add_f32 v[130:131], v[130:131], s[84:85]\nv_pk_add_f32 v[144:145], v[144:145], s[84:85]\nv_pk_add_f32 v[146:147], v[146:147], s[84:85]\nvr 128\nvr 129\nvr 130\n"
"vr 131\nvr 144\nvr 145\nvr 146\nvr 147\nar 32,64\nar 33,65\nar 34,66\n"
"ar 35,67\nar 36,80\nar 37,81\nar 38,82\nar 39,83\nsm 123904\nv_pk_mul_f32 v[128:129], v[128:129], v[32:33]\nlb 19,4,64,0\n"
"v_pk_mul_f32 v[130:131], v[130:131], v[34:35]\nv_pk_mul_f32 v[144:145], v[144:145], v[36:37]\nv_pk_mul_f32 v[146:147], v[146:147], v[38:39]\ncv 128,128,129\ncv 129,130,131\ncv 130,144,145\ncv 131,146,147\nbuffer_store_dwordx4 v[128:131], v21, s[56:59], s87 offen offset:0 sc1\n"
"v_pk_mul_f32 v[132:133], v[132:133], s[82:83]\nv_pk_mul_f32 v[134:135], v[134:135], s[82:83]\nv_pk_mul_f32 v[148:149], v[148:149], s[82:83]\nv_pk_mul_f32 v[150:151], v[150:151], s[82:83]\nve 132\nve 133\nve 134\nve 135\n"
"ve 148\nve 149\nve 150\nve 151\nv_pk_add_f32 v[132:133], v[132:133], s[84:85]\nv_pk_add_f32 v[134:135], v[134:135], s[84:85]\nv_pk_add_f32 v[148:149], v[148:149], s[84:85]\nv_pk_add_f32 v[150:151], v[150:151], s[84:85]\n"
"vr 132\nvr 133\nvr 134\nvr 135\nvr 148\nvr 149\nvr 150\nvr 151\n"
"s_mov_b32 s65, 65536\nsm 124928\nar 40,68\nlb 18,4,65,0\nar 41,69\nar 42,70\nar 43,71\nar 44,84\n"
"ar 45,85\nar 46,86\nar 47,87\nv_pk_mul_f32 v[132:133], v[132:133], v[40:41]\nv_pk_mul_f32 v[134:135], v[134:135], v[42:43]\nv_pk_mul_f32 v[148:149], v[148:149], v[44:45]\nv_pk_mul_f32 v[150:151], v[150:151], v[46:47]\ncv 132,132,133\n"
"cv 133,134,135\ncv 134,148,149\ncv 135,150,151\nbuffer_store_dwordx4 v[132:135], v21, s[56:59], s87 offen offset:1024 sc1\nv_pk_mul_f32 v[136:137], v[136:137], s[82:83]\nv_pk_mul_f32 v[138:139], v[138:139], s[82:83]\nv_pk_mul_f32 v[152:153], v[152:153], s[82:83]\nv_pk_mul_f32 v[154:155], v[154:155], s[82:83]\n"
"ve 136\nve 137\nve 138\nve 139\nve 152\nve 153\nve 154\nve 155\n"
"v_pk_add_f32 v[136:137], v[136:137], s[84:85]\nv_pk_add_f32 v[138:139], v[138:139], s[84:85]\nv_pk_add_f32 v[152:153], v[152:153], s[84:85]\nv_pk_add_f32 v[154:155], v[154:155], s[84:85]\nsm 125952\nvr 136\nlb 19,4,65,0\nvr 137\n"
"vr 138\nvr 139\nvr 152\nvr 153\nvr 154\nvr 155\nar 48,72\nar 49,73\n"
"ar 50,74\nar 51,75\nar 52,88\nar 53,89\nar 54,90\nar 55,91\nv_pk_mul_f32 v[136:137], v[136:137], v[48:49]\nv_pk_mul_f32 v[138:139], v[138:139], v[50:51]\n"
"v_pk_mul_f32 v[152:153], v[152:153], v[52:53]\nv_pk_mul_f32 v[154:155], v[154:155], v[54:55]\ncv 136,136,137\ncv 137,138,139\ncv 138,152,153\ncv 139,154,155\nbuffer_store_dwordx4 v[136:139], v21, s[56:59], s87 offen offset:2048 sc1\nv_pk_mul_f32 v[140:141], v[140:141], s[82:83]\n"
"v_pk_mul_f32 v[142:143], v[142:143], s[82:83]\nv_pk_mul_f32 v[156:157], v[156:157], s[82:83]\nv_pk_mul_f32 v[158:159], v[158:159], s[82:83]\nve 140\nve 141\nve 142\nve 143\ns_mov_b32 s64, 327680\n"
"sm 126976\nve 156\nlb 18,4,64,0\nve 157\nve 158\nve 159\nv_pk_add_f32 v[140:141], v[140:141], s[84:85]\nv_pk_add_f32 v[142:143], v[142:143], s[84:85]\n"
"v_pk_add_f32 v[156:157], v[156:157], s[84:85]\nv_pk_add_f32 v[158:159], v[158:159], s[84:85]\nvr 140\nvr 141\nvr 142\nvr 143\nvr 156\nvr 157\n"
"vr 158\nvr 159\nar 56,76\nar 57,77\nar 58,78\nar 59,79\nar 60,92\nar 61,93\n"
"ar 62,94\nar 63,95\nv_pk_mul_f32 v[140:141], v[140:141], v[56:57]\nv_pk_mul_f32 v[142:143], v[142:143], v[58:59]\nv_pk_mul_f32 v[156:157], v[156:157], v[60:61]\nv_pk_mul_f32 v[158:159], v[158:159], v[62:63]\ncv 140,140,141\ncv 141,142,143\n"
"cv 142,156,157\ncv 143,158,159\nbuffer_store_dwordx4 v[140:143], v21, s[56:59], s87 offen offset:3072 sc1\nsm 143360\nv_pk_mul_f32 v[160:161], v[160:161], s[82:83]\nlb 19,4,64,0\nv_pk_mul_f32 v[162:163], v[162:163], s[82:83]\nv_pk_mul_f32 v[176:177], v[176:177], s[82:83]\n"
"v_pk_mul_f32 v[178:179], v[178:179], s[82:83]\nve 160\nve 161\nve 162\nve 163\nve 176\nve 177\nve 178\n"
"ve 179\nv_pk_add_f32 v[160:161], v[160:161], s[84:85]\nv_pk_add_f32 v[162:163], v[162:163], s[84:85]\nv_pk_add_f32 v[176:177], v[176:177], s[84:85]\nv_pk_add_f32 v[178:179], v[178:179], s[84:85]\nvr 160\nvr 161\nvr 162\n"
"vr 163\nvr 176\nvr 177\nvr 178\nvr 179\nar 32,96\nar 33,97\nar 34,98\n"
"ar 35,99\nar 36,112\nar 37,113\nar 38,114\nar 39,115\ns_mov_b32 s65, 589824\nsm 144384\nv_pk_mul_f32 v[160:161], v[160:161], v[32:33]\n"
"lb 18,4,65,0\nv_pk_mul_f32 v[162:163], v[162:163], v[34:35]\nv_pk_mul_f32 v[176:177], v[176:177], v[36:37]\nv_pk_mul_f32 v[178:179], v[178:179], v[38:39]\ncv 160,160,161\ncv 161,162,163\ncv 162,176,177\ncv 163,178,179\n"
"buffer_store_dwordx4 v[160:163], v21, s[56:59], s88 offen offset:0 sc1\nv_pk_mul_f32 v[164:165], v[164:165], s[82:83]\nv_pk_mul_f32 v[166:167], v[166:167], s[82:83]\nv_pk_mul_f32 v[180:181], v[180:181], s[82:83]\nv_pk_mul_f32 v[182:183], v[182:183], s[82:83]\nve 164\nve 165\nve 166\n"
"ve 167\nve 180\nve 181\nve 182\nve 183\nv_pk_add_f32 v[164:165], v[164:165], s[84:85]\nv_pk_add_f32 v[166:167], v[166:167], s[84:85]\nv_pk_add_f32 v[180:181], v[180:181], s[84:85]\n"
"v_pk_add_f32 v[182:183], v[182:183], s[84:85]\nvr 164\nvr 165\nvr 166\nvr 167\nvr 180\nvr 181\nvr 182\n"
"vr 183\nsm 145408\nar 40,100\nlb 19,4,65,0\nar 41,101\nar 42,102\nar 43,103\nar 44,116\n"
"ar 45,117\nar 46,118\nar 47,119\nv_pk_mul_f32 v[164:165], v[164:165], v[40:41]\nv_pk_mul_f32 v[166:167], v[166:167], v[42:43]\nv_pk_mul_f32 v[180:181], v[180:181], v[44:45]\nv_pk_mul_f32 v[182:183], v[182:183], v[46:47]\ncv 164,164,165\n"
"cv 165,166,167\ncv 166,180,181\ncv 167,182,183\ns_waitcnt vmcnt(56)\nbuffer_store_dwordx4 v[164:167], v21, s[56:59], s88 offen offset:1024 sc1\nv_pk_mul_f32 v[168:169], v[168:169], s[82:83]\nv_pk_mul_f32 v[170:171], v[170:171], s[82:83]\nv_pk_mul_f32 v[184:185], v[184:185], s[82:83]\n"
"v_pk_mul_f32 v[186:187], v[186:187], s[82:83]\nve 168\nve 169\nve 170\nve 171\nve 184\nve 185\nve 186\n"
"ve 187\nv_pk_add_f32 v[168:169], v[168:169], s[84:85]\nv_pk_add_f32 v[170:171], v[170:171], s[84:85]\nv_pk_add_f32 v[184:185], v[184:185], s[84:85]\nv_pk_add_f32 v[186:187], v[186:187], s[84:85]\ns_mov_b32 s64, 851968\nsm 146432\nvr 168\n"
"lb 18,4,64,0\nvr 169\nvr 170\nvr 171\nvr 184\nvr 185\nvr 186\nvr 187\n"
"ar 48,104\nar 49,105\nar 50,106\nar 51,107\nar 52,120\nar 53,121\nar 54,122\nar 55,123\n"
"v_pk_mul_f32 v[168:169], v[168:169], v[48:49]\nv_pk_mul_f32 v[170:171], v[170:171], v[50:51]\nv_pk_mul_f32 v[184:185], v[184:185], v[52:53]\nv_pk_mul_f32 v[186:187], v[186:187], v[54:55]\ncv 168,168,169\ncv 169,170,171\ncv 170,184,185\ncv 171,186,187\n"
"buffer_store_dwordx4 v[168:171], v21, s[56:59], s88 offen offset:2048 sc1\nv_pk_mul_f32 v[172:173], v[172:173], s[82:83]\nv_pk_mul_f32 v[174:175], v[174:175], s[82:83]\nv_pk_mul_f32 v[188:189], v[188:189], s[82:83]\nv_pk_mul_f32 v[190:191], v[190:191], s[82:83]\nve 172\nve 173\nve 174\n"
"ve 175\nsm 147456\nve 188\nlb 19,4,64,0\nve 189\nve 190\nve 191\nv_pk_add_f32 v[172:173], v[172:173], s[84:85]\n"
"v_pk_add_f32 v[174:175], v[174:175], s[84:85]\nv_pk_add_f32 v[188:189], v[188:189], s[84:85]\nv_pk_add_f32 v[190:191], v[190:191], s[84:85]\nvr 172\nvr 173\nvr 174\nvr 175\nvr 188\n"
"vr 189\nvr 190\nvr 191\nar 56,108\nar 57,109\nar 58,110\nar 59,111\nar 60,124\n"
"ar 61,125\nar 62,126\nar 63,127\nv_pk_mul_f32 v[172:173], v[172:173], v[56:57]\nv_pk_mul_f32 v[174:175], v[174:175], v[58:59]\nv_pk_mul_f32 v[188:189], v[188:189], v[60:61]\nv_pk_mul_f32 v[190:191], v[190:191], v[62:63]\ncv 172,172,173\n"
"cv 173,174,175\ncv 174,188,189\ncv 175,190,191\nbuffer_store_dwordx4 v[172:175], v21, s[56:59], s88 offen offset:3072 sc1\ncv 128,192,193\ncv 129,194,195\ncv 130,208,209\ncv 131,210,211\n"
"cv 144,196,197\ncv 145,198,199\nzvav 192,176,128\ncv 146,212,213\ncv 147,214,215\nzvav 208,180,128\ns_nop 0\nzvav 196,176,144\n"
"zvav 212,180,144\ns_nop 1\nv_max3_f32 v136, v192, v193, v194\nv_max3_f32 v137, -v192, -v193, -v194\ns_nop 0\nv_max3_f32 v136, v136, v195, v208\nv_max3_f32 v136, v136, v209, v210\nv_max_f32 v136, v136, v211\n"
"v_max3_f32 v137, v137, -v195, -v208\nv_max3_f32 v137, v137, -v209, -v210\nv_max_f32 v137, v137, -v211\nv_max3_f32 v152, v196, v197, v198\nv_max3_f32 v152, v152, v199, v212\nv_permlane32_swap_b32 v136, v137\nv_max_f32 v136, v136, v137\nv_mov_b32 v137, v136\n"
"v_max3_f32 v152, v152, v213, v214\nv_max_f32 v152, v152, v215\nv_permlane16_swap_b32 v136, v137\nv_max_f32 v136, v136, v137\nv_mul_f32 v137, s82, v136\nv_mul_f32 v136, s82, v136\nv_max3_f32 v153, -v196, -v197, -v198\nv_max3_f32 v153, v153, -v199, -v212\n"
"v_permlane32_swap_b32 v136, v137\nv_pk_fma_f32 v[128:129], v[192:193], s[82:83], v[136:137] op_sel:[0,0,1] op_sel_hi:[1,1,1]\nv_pk_fma_f32 v[130:131], v[194:195], s[82:83], v[136:137] op_sel:[0,0,1] op_sel_hi:[1,1,1]\nv_pk_fma_f32 v[132:133], v[208:209], s[82:83], v[136:137] op_sel:[0,0,1] op_sel_hi:[1,1,1]\nv_pk_fma_f32 v[134:135], v[210:211], s[82:83], v[136:137] op_sel:[0,0,1] op_sel_hi:[1,1,1]\nv_pk_fma_f32 v[192:193], v[192:193], s[80:81], v[136:137] op_sel:[0,0,0] op_sel_hi:[1,1,0]\nv_pk_fma_f32 v[194:195], v[194:195], s[80:81], v[136:137] op_sel:[0,0,0] op_sel_hi:[1,1,0]\nv_pk_fma_f32 v[208:209], v[208:209], s[80:81], v[136:137] op_sel:[0,0,0] op_sel_hi:[1,1,0]\n"
"v_pk_fma_f32 v[210:211], v[210:211], s[80:81], v[136:137] op_sel:[0,0,0] op_sel_hi:[1,1,0]\nve 192\nve 193\nve 194\nve 195\nve 208\nve 209\nve 210\n"
"ve 211\nve 128\nve 129\nve 130\nve 131\nve 132\nve 133\nve 134\n"
"ve 135\ncv 192,192,193\ncv 193,194,195\ncv 194,208,209\ncv 195,210,211\ncv 208,128,129\ncv 209,130,131\ncv 210,132,133\n"
"cv 211,134,135\nv_max3_f32 v153, v153, -v213, -v214\nv_max_f32 v153, v153, -v215\ns_nop 1\nv_permlane32_swap_b32 v152, v153\nv_max_f32 v152, v152, v153\nv_mov_b32 v153, v152\ns_nop 1\n"
"v_permlane16_swap_b32 v152, v153\nv_max_f32 v152, v152, v153\nv_mul_f32 v153, s82, v152\nv_mul_f32 v152, s82, v152\ns_nop 1\nv_permlane32_swap_b32 v152, v153\nv_pk_fma_f32 v[144:145], v[196:197], s[82:83], v[152:153] op_sel:[0,0,1] op_sel_hi:[1,1,1]\nv_pk_fma_f32 v[146:147], v[198:199], s[82:83], v[152:153] op_sel:[0,0,1] op_sel_hi:[1,1,1]\n"
"v_pk_fma_f32 v[148:149], v[212:213], s[82:83], v[152:153] op_sel:[0,0,1] op_sel_hi:[1,1,1]\nv_pk_fma_f32 v[150:151], v[214:215], s[82:83], v[152:153] op_sel:[0,0,1] op_sel_hi:[1,1,1]\nv_pk_fma_f32 v[196:197], v[196:197], s[80:81], v[152:153] op_sel:[0,0,0] op_sel_hi:[1,1,0]\nv_pk_fma_f32 v[198:199], v[198:199], s[80:81], v[152:153] op_sel:[0,0,0] op_sel_hi:[1,1,0]\nv_pk_fma_f32 v[212:213], v[212:213], s[80:81], v[152:153] op_sel:[0,0,0] op_sel_hi:[1,1,0]\nv_pk_fma_f32 v[214:215], v[214:215], s[80:81], v[152:153] op_sel:[0,0,0] op_sel_hi:[1,1,0]\nve 196\nve 197\n"
"ve 198\nve 199\nve 212\nve 213\nve 214\nve 215\nve 144\nve 145\n"
"ve 146\nve 147\nve 148\nve 149\nve 150\nve 151\ncv 196,196,197\ncv 197,198,199\n"
"cv 198,212,213\ncv 199,214,215\ncv 212,144,145\ncv 213,146,147\ncv 214,148,149\ncv 215,150,151\ns_waitcnt vmcnt(0)\ns_barrier\n"
"v_mov_b32 v22, s34\nbuffer_store_dword v22, v30, s[56:59], s76 offen offset:0 sc1\ns_add_u32 s95, s32, 32768\nbuffer_load_dwordx4 a[172:175], v29, s[12:15], s32 offen offset:0\ncv 160,200,201\ncv 161,202,203\ncv 162,216,217\ncv 163,218,219\n"
"cv 176,204,205\ncv 177,206,207\nzvav 200,176,160\ncv 178,220,221\ncv 179,222,223\nzvav 216,180,160\nbuffer_load_dwordx4 a[244:247], v29, s[12:15], s32 offen offset:64\nzvav 204,176,176\n"
"cv 128,224,225\ncv 129,226,227\nzvav 220,180,176\nv_max3_f32 v168, v200, v201, v202\nv_max3_f32 v169, -v200, -v201, -v202\ncv 130,240,241\nv_max3_f32 v168, v168, v203, v216\nv_max3_f32 v168, v168, v217, v218\n"
"v_max_f32 v168, v168, v219\nv_max3_f32 v169, v169, -v203, -v216\nv_max3_f32 v169, v169, -v217, -v218\nv_max_f32 v169, v169, -v219\nv_max3_f32 v184, v204, v205, v206\nv_max3_f32 v184, v184, v207, v220\nv_permlane32_swap_b32 v168, v169\nv_max_f32 v168, v168, v169\n"
"v_mov_b32 v169, v168\nv_max3_f32 v184, v184, v221, v222\nv_max_f32 v184, v184, v223\nv_permlane16_swap_b32 v168, v169\nv_max_f32 v168, v168, v169\nv_mul_f32 v169, s82, v168\nv_mul_f32 v168, s82, v168\nv_max3_f32 v185, -v204, -v205, -v206\n"
"v_max3_f32 v185, v185, -v207, -v220\nv_permlane32_swap_b32 v168, v169\nv_pk_fma_f32 v[160:161], v[200:201], s[82:83], v[168:169] op_sel:[0,0,1] op_sel_hi:[1,1,1]\nv_pk_fma_f32 v[162:163], v[202:203], s[82:83], v[168:169] op_sel:[0,0,1] op_sel_hi:[1,1,1]\nv_pk_fma_f32 v[164:165], v[216:217], s[82:83], v[168:169] op_sel:[0,0,1] op_sel_hi:[1,1,1]\nv_pk_fma_f32 v[166:167], v[218:219], s[82:83], v[168:169] op_sel:[0,0,1] op_sel_hi:[1,1,1]\nv_pk_fma_f32 v[200:201], v[200:201], s[80:81], v[168:169] op_sel:[0,0,0] op_sel_hi:[1,1,0]\nv_pk_fma_f32 v[202:203], v[202:203], s[80:81], v[168:169] op_sel:[0,0,0] op_sel_hi:[1,1,0]\n"
"v_pk_fma_f32 v[216:217], v[216:217], s[80:81], v[168:169] op_sel:[0,0,0] op_sel_hi:[1,1,0]\nv_pk_fma_f32 v[218:219], v[218:219], s[80:81], v[168:169] op_sel:[0,0,0] op_sel_hi:[1,1,0]\nve 200\nve 201\nve 202\nve 203\nve 216\nve 217\n"
"ve 218\nve 219\nve 160\nve 161\nve 162\nve 163\nve 164\nve 165\n"
"ve 166\nve 167\ncv 200,200,201\ncv 201,202,203\ncv 202,216,217\ncv 203,218,219\ncv 216,160,161\ncv 217,162,163\n"
"cv 218,164,165\ncv 219,166,167\nv_max3_f32 v185, v185, -v221, -v222\nv_max_f32 v185, v185, -v223\nbuffer_load_dwordx4 a[248:251], v29, s[12:15], s95 offen offset:0\ncv 131,242,243\nv_permlane32_swap_b32 v184, v185\nv_max_f32 v184, v184, v185\n"
"v_mov_b32 v185, v184\nzvav 224,184,128\ncv 144,228,229\nv_permlane16_swap_b32 v184, v185\nv_max_f32 v184, v184, v185\nv_mul_f32 v185, s82, v184\nv_mul_f32 v184, s82, v184\nzvav 240,188,128\n"
"buffer_load_dwordx4 a[252:255], v29, s[12:15], s95 offen offset:64\nv_permlane32_swap_b32 v184, v185\nv_pk_fma_f32 v[176:177], v[204:205], s[82:83], v[184:185] op_sel:[0,0,1] op_sel_hi:[1,1,1]\nv_pk_fma_f32 v[178:179], v[206:207], s[82:83], v[184:185] op_sel:[0,0,1] op_sel_hi:[1,1,1]\nv_pk_fma_f32 v[180:181], v[220:221], s[82:83], v[184:185] op_sel:[0,0,1] op_sel_hi:[1,1,1]\nv_pk_fma_f32 v[182:183], v[222:223], s[82:83], v[184:185] op_sel:[0,0,1] op_sel_hi:[1,1,1]\nv_pk_fma_f32 v[204:205], v[204:205], s[80:81], v[184:185] op_sel:[0,0,0] op_sel_hi:[1,1,0]\nv_pk_fma_f32 v[206:207], v[206:207], s[80:81], v[184:185] op_sel:[0,0,0] op_sel_hi:[1,1,0]\n"
"v_pk_fma_f32 v[220:221], v[220:221], s[80:81], v[184:185] op_sel:[0,0,0] op_sel_hi:[1,1,0]\nv_pk_fma_f32 v[222:223], v[222:223], s[80:81], v[184:185] op_sel:[0,0,0] op_sel_hi:[1,1,0]\nve 204\nve 205\nve 206\nve 207\nve 220\nve 221\n"
"ve 222\nve 223\nve 176\nve 177\nve 178\nve 179\nve 180\nve 181\n"
"ve 182\nve 183\ncv 204,204,205\ncv 205,206,207\ncv 206,220,221\ncv 207,222,223\ncv 220,176,177\ncv 221,178,179\n"
"cv 222,180,181\ncv 223,182,183\nv_max3_f32 v136, v224, v225, v226\nv_max3_f32 v136, v136, v227, v240\nv_max3_f32 v136, v136, v241, v242\nv_max_f32 v136, v136, v243\nv_max3_f32 v137, -v224, -v225, -v226\nv_max3_f32 v137, v137, -v227, -v240\n"
"v_max3_f32 v137, v137, -v241, -v242\nv_max_f32 v137, v137, -v243\ncv 145,230,231\ncv 146,244,245\nv_permlane32_swap_b32 v136, v137\nv_max_f32 v136, v136, v137\nv_mov_b32 v137, v136\ncv 147,246,247\n"
"s_nop 0\nv_permlane16_swap_b32 v136, v137\nv_max_f32 v136, v136, v137\nv_mul_f32 v137, s82, v136\nv_mul_f32 v136, s82, v136\nzvav 228,184,144\ns_nop 0\nv_permlane32_swap_b32 v136, v137\n"
"zvav 244,188,144\nv_pk_fma_f32 v[128:129], v[224:225], s[82:83], v[136:137] op_sel:[0,0,1] op_sel_hi:[1,1,1]\nv_pk_fma_f32 v[130:131], v[226:227], s[82:83], v[136:137] op_sel:[0,0,1] op_sel_hi:[1,1,1]\nv_pk_fma_f32 v[132:133], v[240:241], s[82:83], v[136:137] op_sel:[0,0,1] op_sel_hi:[1,1,1]\nv_pk_fma_f32 v[134:135], v[242:243], s[82:83], v[136:137] op_sel:[0,0,1] op_sel_hi:[1,1,1]\nv_pk_fma_f32 v[224:225], v[224:225], s[80:81], v[136:137] op_sel:[0,0,0] op_sel_hi:[1,1,0]\nv_pk_fma_f32 v[226:227], v[226:227], s[80:81], v[136:137] op_sel:[0,0,0] op_sel_hi:[1,1,0]\nv_pk_fma_f32 v[240:241], v[240:241], s[80:81], v[136:137] op_sel:[0,0,0] op_sel_hi:[1,1,0]\n"
"v_pk_fma_f32 v[242:243], v[242:243], s[80:81], v[136:137] op_sel:[0,0,0] op_sel_hi:[1,1,0]\nve 224\nve 225\nve 226\nve 227\nve 240\nve 241\nve 242\n"
"ve 243\nve 128\nve 129\nve 130\nve 131\nve 132\nve 133\nve 134\n"
"ve 135\ncv 224,224,225\ncv 225,226,227\ncv 226,240,241\ncv 227,242,243\ncv 240,128,129\ncv 241,130,131\ncv 242,132,133\n"
"cv 243,134,135\nv_max3_f32 v152, v228, v229, v230\nv_max3_f32 v152, v152, v231, v244\nv_max3_f32 v152, v152, v245, v246\nv_max_f32 v152, v152, v247\nv_max3_f32 v153, -v228, -v229, -v230\nv_max3_f32 v153, v153, -v231, -v244\nv_max3_f32 v153, v153, -v245, -v246\n"
"v_max_f32 v153, v153, -v247\ns_nop 1\nv_permlane32_swap_b32 v152, v153\nv_max_f32 v152, v152, v153\nv_mov_b32 v153, v152\ns_nop 1\nv_permlane16_swap_b32 v152, v153\nv_max_f32 v152, v152, v153\n"
"v_mul_f32 v153, s82, v152\nv_mul_f32 v152, s82, v152\ns_nop 1\nv_permlane32_swap_b32 v152, v153\nv_pk_fma_f32 v[144:145], v[228:229], s[82:83], v[152:153] op_sel:[0,0,1] op_sel_hi:[1,1,1]\nv_pk_fma_f32 v[146:147], v[230:231], s[82:83], v[152:153] op_sel:[0,0,1] op_sel_hi:[1,1,1]\nv_pk_fma_f32 v[148:149], v[244:245], s[82:83], v[152:153] op_sel:[0,0,1] op_sel_hi:[1,1,1]\nv_pk_fma_f32 v[150:151], v[246:247], s[82:83], v[152:153] op_sel:[0,0,1] op_sel_hi:[1,1,1]\n"
"v_pk_fma_f32 v[228:229], v[228:229], s[80:81], v[152:153] op_sel:[0,0,0] op_sel_hi:[1,1,0]\nv_pk_fma_f32 v[230:231], v[230:231], s[80:81], v[152:153] op_sel:[0,0,0] op_sel_hi:[1,1,0]\nv_pk_fma_f32 v[244:245], v[244:245], s[80:81], v[152:153] op_sel:[0,0,0] op_sel_hi:[1,1,0]\nv_pk_fma_f32 v[246:247], v[246:247], s[80:81], v[152:153] op_sel:[0,0,0] op_sel_hi:[1,1,0]\nve 228\nve 229\nve 230\nve 231\n"
"ve 244\nve 245\nve 246\nve 247\nve 144\nve 145\nve 146\nve 147\n"
"ve 148\nve 149\nve 150\nve 151\ncv 228,228,229\ncv 229,230,231\ncv 230,244,245\ncv 231,246,247\n"
"cv 244,144,145\ncv 245,146,147\ncv 246,148,149\ncv 247,150,151\nv_lshrrev_b32 v20, 2, v23\nv_and_b32 v20, 60, v20\nbuffer_load_dword v31, v20, s[56:59], s77 offen offset:0 sc1\ncv 160,232,233\n"
"cv 161,234,235\ncv 162,248,249\ncv 163,250,251\ns_nop 1\nzvav 232,184,160\nzvav 248,188,160\ns_nop 6\nv_max3_f32 v168, v232, v233, v234\n"
"v_max3_f32 v168, v168, v235, v248\nv_max3_f32 v168, v168, v249, v250\nv_max_f32 v168, v168, v251\nv_max3_f32 v169, -v232, -v233, -v234\nv_max3_f32 v169, v169, -v235, -v248\nv_max3_f32 v169, v169, -v249, -v250\nv_max_f32 v169, v169, -v251\ns_nop 1\n"
"v_permlane32_swap_b32 v168, v169\nv_max_f32 v168, v168, v169\nv_mov_b32 v169, v168\ns_nop 1\nv_permlane16_swap_b32 v168, v169\nv_max_f32 v168, v168, v169\nv_mul_f32 v169, s82, v168\nv_mul_f32 v168, s82, v168\n"
"s_nop 1\nv_permlane32_swap_b32 v168, v169\nv_pk_fma_f32 v[160:161], v[232:233], s[82:83], v[168:169] op_sel:[0,0,1] op_sel_hi:[1,1,1]\nv_pk_fma_f32 v[162:163], v[234:235], s[82:83], v[168:169] op_sel:[0,0,1] op_sel_hi:[1,1,1]\nv_pk_fma_f32 v[164:165], v[248:249], s[82:83], v[168:169] op_sel:[0,0,1] op_sel_hi:[1,1,1]\nv_pk_fma_f32 v[166:167], v[250:251], s[82:83], v[168:169] op_sel:[0,0,1] op_sel_hi:[1,1,1]\nv_pk_fma_f32 v[232:233], v[232:233], s[80:81], v[168:169] op_sel:[0,0,0] op_sel_hi:[1,1,0]\nv_pk_fma_f32 v[234:235], v[234:235], s[80:81], v[168:169] op_sel:[0,0,0] op_sel_hi:[1,1,0]\n"
"v_pk_fma_f32 v[248:249], v[248:249], s[80:81], v[168:169] op_sel:[0,0,0] op_sel_hi:[1,1,0]\nv_pk_fma_f32 v[250:251], v[250:251], s[80:81], v[168:169] op_sel:[0,0,0] op_sel_hi:[1,1,0]\nve 232\nve 233\nve 234\nve 235\nve 248\nve 249\n"
"ve 250\nve 251\nve 160\nve 161\nve 162\nve 163\nve 164\nve 165\n"
"ve 166\nve 167\ncv 232,232,233\ncv 233,234,235\ncv 234,248,249\ncv 235,250,251\ncv 248,160,161\ncv 249,162,163\n"
"cv 250,164,165\ncv 251,166,167\ns_waitcnt vmcnt(0)\nv_cmp_ne_u32 vcc, s34, v31\ns_cbranch_vccz 53f\ns_mov_b32 s69, 0x80000\n52:\ns_sleep 1\n"
"buffer_load_dword v31, v20, s[56:59], s77 offen offset:0 sc1\ns_waitcnt vmcnt(0)\nv_cmp_ne_u32 vcc, s34, v31\ns_cbranch_vccz 53f\ns_sub_u32 s69, s69, 1\ns_cbranch_scc0 52b\n53:\ns_add_u32 s66, s28, 0\n"
"lc 0,21,66,0\ncv 176,236,237\ncv 177,238,239\ncv 178,252,253\ncv 179,254,255\ns_add_u32 s67, s28, 16384\ns_nop 0\nzvav 236,184,176\n"
"zvav 252,188,176\nlc 4,21,66,1024\ns_add_u32 s66, s28, 32768\ns_nop 4\nv_max3_f32 v184, v236, v237, v238\nv_max3_f32 v184, v184, v239, v252\nv_max3_f32 v184, v184, v253, v254\nv_max_f32 v184, v184, v255\n"
"v_max3_f32 v185, -v236, -v237, -v238\nv_max3_f32 v185, v185, -v239, -v252\nv_max3_f32 v185, v185, -v253, -v254\nlc 8,21,67,0\nv_max_f32 v185, v185, -v255\ns_nop 1\nv_permlane32_swap_b32 v184, v185\nv_max_f32 v184, v184, v185\n"
"v_mov_b32 v185, v184\ns_nop 1\nv_permlane16_swap_b32 v184, v185\nv_max_f32 v184, v184, v185\nlc 12,21,67,1024\nv_mul_f32 v185, s82, v184\nv_mul_f32 v184, s82, v184\ns_add_u32 s67, s28, 49152\n"
"s_nop 0\nv_permlane32_swap_b32 v184, v185\nv_pk_fma_f32 v[176:177], v[236:237], s[82:83], v[184:185] op_sel:[0,0,1] op_sel_hi:[1,1,1]\nv_pk_fma_f32 v[178:179], v[238:239], s[82:83], v[184:185] op_sel:[0,0,1] op_sel_hi:[1,1,1]\nv_pk_fma_f32 v[180:181], v[252:253], s[82:83], v[184:185] op_sel:[0,0,1] op_sel_hi:[1,1,1]\nv_pk_fma_f32 v[182:183], v[254:255], s[82:83], v[184:185] op_sel:[0,0,1] op_sel_hi:[1,1,1]\nv_pk_fma_f32 v[236:237], v[236:237], s[80:81], v[184:185] op_sel:[0,0,0] op_sel_hi:[1,1,0]\nv_pk_fma_f32 v[238:239], v[238:239], s[80:81], v[184:185] op_sel:[0,0,0] op_sel_hi:[1,1,0]\n"
"v_pk_fma_f32 v[252:253], v[252:253], s[80:81], v[184:185] op_sel:[0,0,0] op_sel_hi:[1,1,0]\nlc 16,21,66,0\nv_pk_fma_f32 v[254:255], v[254:255], s[80:81], v[184:185] op_sel:[0,0,0] op_sel_hi:[1,1,0]\nve 236\nve 237\nve 238\nve 239\nve 252\n"
"ve 253\nlc 20,21,66,1024\nve 254\nve 255\nve 176\nve 177\nve 178\nve 179\n"
"lc 24,21,67,0\nve 180\ns_add_u32 s66, s28, 65536\nve 181\nve 182\nve 183\ncv 236,236,237\ncv 237,238,239\n"
"cv 238,252,253\ncv 239,254,255\nlc 28,21,67,1024\ncv 252,176,177\ncv 253,178,179\ncv 254,180,181\ncv 255,182,183\nlc 32,21,66,0\n"
"lc 36,21,66,1024\ndt 32,0,0\ndt 34,0,10240\ndt 36,0,2048\ndt 38,0,12288\ndt 40,1,0\ndt 42,1,10240\ndt 44,1,2048\n"
"dt 46,1,12288\ndt 48,2,0\ndt 50,2,10240\ndt 52,2,2048\ndt 54,2,12288\nwb 6,8\nzvva 64,32,0\ndt 56,3,0\n"
"dt 58,3,10240\nmvva 64,36,8\ns_add_u32 s67, s28, 81920\ndt 60,3,2048\ndt 62,3,12288\nzvva 68,36,12\nlc 40,21,67,0\nmvva 68,32,4\n"
"wl 8\nlc 44,21,67,1024\nzvva 76,40,4\ndt 32,0,1024\ndt 34,0,11264\nmvva 76,44,12\ndt 36,0,3072\ndt 38,0,13312\n"
"zvva 72,44,8\nmvva 72,40,0\nwl 8\nzvva 80,48,0\ndt 40,1,1024\ndt 42,1,11264\nmvva 80,52,8\ndt 44,1,3072\n"
"dt 46,1,13312\nzvva 84,52,12\ns_add_u32 s66, s28, 98304\nmvva 84,48,4\nwl 8\nlc 48,21,66,0\nzvva 92,56,4\ndt 48,2,1024\n"
"dt 50,2,11264\nmvva 92,60,12\ndt 52,2,3072\ndt 54,2,13312\nzvva 88,60,8\nmvva 88,56,0\nwl 8\nlc 52,21,66,1024\n"
"zvva 96,32,0\ndt 56,3,1024\ndt 58,3,11264\nmvva 96,36,8\ndt 60,3,3072\ndt 62,3,13312\nzvva 100,36,12\ns_add_u32 s67, s28, 114688\n"
"mvva 100,32,4\nwl 8\nlc 56,21,67,0\nzvva 108,40,4\ndt 32,0,4096\ndt 34,0,14336\nmvva 108,44,12\ndt 36,0,21504\n"
"dt 38,0,31744\nzvva 104,44,8\nmvva 104,40,0\nwl 8\nlc 60,21,67,1024\nzvva 112,48,0\ndt 40,1,4096\ndt 42,1,14336\n"
"mvva 112,52,8\ndt 44,1,21504\ndt 46,1,31744\nzvva 116,52,12\ns_add_u32 s66, s28, 131072\nmvva 116,48,4\nwl 8\nlc 64,21,66,0\n"
"zvva 124,56,4\ndt 48,2,4096\ndt 50,2,14336\nmvva 124,60,12\ndt 52,2,21504\ndt 54,2,31744\nzvva 120,60,8\nmvva 120,56,0\n"
"wb 9,8\nlc 68,21,66,1024\nmvva 64,32,16\ndt 56,3,4096\ndt 58,3,14336\nmvva 64,36,24\ndt 60,3,21504\ndt 62,3,31744\n"
"mvva 68,36,28\ns_add_u32 s67, s28, 147456\nmvva 68,32,20\nwl 8\nlc 72,21,67,0\nmvva 76,40,20\ndt 32,0,20480\ndt 34,0,30720\n"
"mvva 76,44,28\ndt 36,0,22528\ndt 38,0,32768\nmvva 72,44,24\nmvva 72,40,16\nwl 8\nlc 76,21,67,1024\nmvva 80,48,16\n"
"dt 40,1,20480\ndt 42,1,30720\nmvva 80,52,24\ndt 44,1,22528\ndt 46,1,32768\nmvva 84,52,28\ns_add_u32 s66, s28, 163840\nmvva 84,48,20\n"
"wl 8\nlc 80,21,66,0\nmvva 92,56,20\ndt 48,2,20480\ndt 50,2,30720\nmvva 92,60,28\ndt 52,2,22528\ndt 54,2,32768\n"
"mvva 88,60,24\nmvva 88,56,16\nwl 8\nlc 84,21,66,1024\ns_barrier\nmvva 96,32,16\ndt 56,3,20480\ndt 58,3,30720\n"
"mvva 96,36,24\ndt 60,3,22528\ndt 62,3,32768\nmvva 100,36,28\ns_add_u32 s67, s28, 180224\nmvva 100,32,20\nwl 8\nlc 88,21,67,0\n"
"mvva 108,40,20\ndt 32,0,23552\ndt 34,0,33792\nmvva 108,44,28\ndt 36,0,40960\ndt 38,0,51200\nmvva 104,44,24\nmvva 104,40,16\n"
"wl 8\nlc 92,21,67,1024\nmvva 112,48,16\ndt 40,1,23552\ndt 42,1,33792\nmvva 112,52,24\ndt 44,1,40960\ndt 46,1,51200\n"
"mvva 116,52,28\ns_add_u32 s66, s28, 196608\nmvva 116,48,20\nwl 8\nlc 96,21,66,0\nmvva 124,56,20\ndt 48,2,23552\ndt 50,2,33792\n"
"mvva 124,60,28\ndt 52,2,40960\ndt 54,2,51200\nmvva 120,60,24\nmvva 120,56,16\nwb 13,8\nlc 100,21,66,1024\nmvva 64,32,32\n"
"dt 56,3,23552\ndt 58,3,33792\nmvva 64,36,40\ndt 60,3,40960\ndt 62,3,51200\nmvva 68,36,44\ns_add_u32 s67, s28, 212992\nmvva 68,32,36\n"
"wl 8\nlc 104,21,67,0\nmvva 76,40,36\ndt 32,0,24576\ndt 34,0,34816\nmvva 76,44,44\ndt 36,0,41984\ndt 38,0,52224\n"
"mvva 72,44,40\nmvva 72,40,32\nwl 8\nlc 108,21,67,1024\nmvva 80,48,32\ndt 40,1,24576\ndt 42,1,34816\nmvva 80,52,40\n"
"dt 44,1,41984\ndt 46,1,52224\nmvva 84,52,44\ns_add_u32 s66, s28, 229376\nmvva 84,48,36\nwl 8\nlc 112,21,66,0\nmvva 92,56,36\n"
"dt 48,2,24576\ndt 50,2,34816\nmvva 92,60,44\ndt 52,2,41984\ndt 54,2,52224\nmvva 88,60,40\nmvva 88,56,32\nwl 8\n"
"lc 116,21,66,1024\nmvva 96,32,32\ndt 56,3,24576\ndt 58,3,34816\nmvva 96,36,40\ndt 60,3,41984\ndt 62,3,52224\nmvva 100,36,44\n"
"mvva 100,32,36\nwl 8\nmvva 108,40,36\ndt 32,0,43008\ndt 34,0,53248\nmvva 108,44,44\ndt 36,0,45056\ndt 38,0,55296\n"
"mvva 104,44,40\ns_add_u32 s67, s28, 245760\nmvva 104,40,32\nwl 8\nlc 120,21,67,0\nmvva 112,48,32\ndt 40,1,43008\ndt 42,1,53248\n"
"mvva 112,52,40\ndt 44,1,45056\ndt 46,1,55296\nmvva 116,52,44\nmvva 116,48,36\nwl 8\nlc 124,21,67,1024\nmvva 124,56,36\n"
"dt 48,2,43008\ndt 50,2,53248\nmvva 124,60,44\ndt 52,2,45056\ndt 54,2,55296\nmvva 120,60,40\ns_add_u32 s66, s28, 4096\nmvva 120,56,32\n"
"wb 16,8\nlc 128,21,66,0\nmvva 64,32,48\ndt 56,3,43008\ndt 58,3,53248\nmvva 64,36,56\ndt 60,3,45056\ndt 62,3,55296\n"
"mvva 68,36,60\nmvva 68,32,52\nwl 8\nlc 132,21,66,1024\nmvva 76,40,52\ndt 32,0,44032\ndt 34,0,54272\nmvva 76,44,60\n"
"dt 36,0,61440\ndt 38,6,6144\nmvva 72,44,56\ns_add_u32 s67, s28, 20480\nmvva 72,40,48\nwl 8\nlc 136,21,67,0\nmvva 80,48,48\n"
"dt 40,1,44032\ndt 42,1,54272\nmvva 80,52,56\ndt 44,1,61440\ndt 46,7,6144\nmvva 84,52,60\nmvva 84,48,52\nwl 8\n"
"lc 140,21,67,1024\nmvva 92,56,52\ndt 48,2,44032\ndt 50,2,54272\nmvva 92,60,60\ndt 52,2,61440\ndt 54,8,6144\nmvva 88,60,56\n"
"s_add_u32 s66, s28, 36864\nmvva 88,56,48\nwl 8\nlc 144,21,66,0\ns_barrier\nmvva 96,32,48\ndt 56,3,44032\ndt 58,3,54272\n"
"mvva 96,36,56\ndt 60,3,61440\ndt 62,9,6144\nmvva 100,36,60\nmvva 100,32,52\nwl 8\nlc 148,21,66,1024\nmvva 108,40,52\n"
"dt 32,0,62464\ndt 34,6,7168\nmvva 108,44,60\ndt 36,0,64512\ndt 38,6,9216\nmvva 104,44,56\ns_add_u32 s67, s28, 53248\nmvva 104,40,48\n"
"wl 8\nlc 152,21,67,0\nmvva 112,48,48\ndt 40,1,62464\ndt 42,7,7168\nmvva 112,52,56\ndt 44,1,64512\ndt 46,7,9216\n"
"mvva 116,52,60\nmvva 116,48,52\nwl 8\nlc 156,21,67,1024\nmvva 124,56,52\ns_mov_b32 s65, 1114112\ndt 48,2,62464\ndt 50,8,7168\n"
"mvva 124,60,60\nsm 0\ndt 52,2,64512\ndt 54,8,9216\nmvva 120,60,56\nlb 18,4,65,0\nmvva 120,56,48\nwb 21,8\n"
"mvva 64,32,64\ndt 56,3,62464\ndt 58,9,7168\nmvva 64,36,72\nsm 1024\ndt 60,3,64512\ndt 62,9,9216\nmvva 68,36,76\n"
"lb 19,4,65,0\nmvva 68,32,68\nwl 8\nmvva 76,40,68\ns_mov_b32 s64, 1376256\ndt 32,0,63488\ndt 34,6,8192\nmvva 76,44,76\n"
"sm 2048\ndt 36,6,0\ndt 38,6,10240\nmvva 72,44,72\nlb 18,4,64,0\nmvva 72,40,64\nwl 8\nmvva 80,48,64\n"
"dt 40,1,63488\ndt 42,7,8192\nmvva 80,52,72\nsm 3072\ndt 44,7,0\ndt 46,7,10240\nmvva 84,52,76\nlb 19,4,64,0\n"
"mvva 84,48,68\nwl 8\nmvva 92,56,68\ns_mov_b32 s65, 1638400\ndt 48,2,63488\ndt 50,8,8192\nmvva 92,60,76\nsm 4096\n"
"dt 52,8,0\ndt 54,8,10240\nmvva 88,60,72\nlb 18,4,65,0\nmvva 88,56,64\nwl 8\nmvva 96,32,64\ndt 56,3,63488\n"
"dt 58,9,8192\nmvva 96,36,72\nsm 20480\ndt 60,9,0\ndt 62,9,10240\nmvva 100,36,76\nlb 19,4,65,0\nmvva 100,32,68\n"
"wl 8\nmvva 108,40,68\ns_mov_b32 s64, 1900544\ndt 32,6,16384\ndt 34,6,26624\nmvva 108,44,76\nsm 21504\ndt 36,6,18432\n"
"dt 38,6,28672\nmvva 104,44,72\nlb 18,4,64,0\nmvva 104,40,64\nwl 8\nmvva 112,48,64\ndt 40,7,16384\ndt 42,7,26624\n"
"mvva 112,52,72\nsm 22528\ndt 44,7,18432\ndt 46,7,28672\nmvva 116,52,76\nlb 19,4,64,0\nmvva 116,48,68\nwl 8\n"
"mvva 124,56,68\ndt 48,8,16384\ndt 50,8,26624\nmvva 124,60,76\ndt 52,8,18432\ndt 54,8,28672\nmvva 120,60,72\ns_add_u32 s66, s28, 69632\n"
"mvva 120,56,64\nwb 24,8\nlc 160,21,66,0\nmvva 64,32,80\ndt 56,9,16384\ndt 58,9,26624\nmvva 64,36,88\ndt 60,9,18432\n"
"dt 62,9,28672\nmvva 68,36,92\nmvva 68,32,84\nwl 8\nmvva 76,40,84\ndt 32,6,17408\ndt 34,6,27648\nmvva 76,44,92\n"
"dt 36,6,19456\ndt 38,6,29696\nmvva 72,44,88\nmvva 72,40,80\nwl 8\nlc 164,21,66,1024\nmvva 80,48,80\ndt 40,7,17408\n"
"dt 42,7,27648\nmvva 80,52,88\ndt 44,7,19456\ndt 46,7,29696\nmvva 84,52,92\ns_add_u32 s67, s28, 86016\nmvva 84,48,84\nwl 8\n"
"lc 168,21,67,0\nmvva 92,56,84\ndt 48,8,17408\ndt 50,8,27648\nmvva 92,60,92\ndt 52,8,19456\ndt 54,8,29696\nmvva 88,60,88\n"
"mvva 88,56,80\nwl 8\nlc 0,21,67,1024\ns_barrier\nmvva 96,32,80\ndt 56,9,17408\ndt 58,9,27648\nmvva 96,36,88\n"
"dt 60,9,19456\ndt 62,9,29696\nmvva 100,36,92\ns_add_u32 s66, s28, 102400\nmvva 100,32,84\nwl 8\nlc 4,21,66,0\nmvva 108,40,84\n"
"dt 32,6,20480\ndt 34,6,30720\nmvva 108,44,92\ndt 36,6,37888\ndt 38,6,48128\nmvva 104,44,88\nmvva 104,40,80\nwl 8\n"
"lc 8,21,66,1024\nmvva 112,48,80\ndt 40,7,20480\ndt 42,7,30720\nmvva 112,52,88\ndt 44,7,37888\ndt 46,7,48128\nmvva 116,52,92\n"
"s_add_u32 s67, s28, 118784\nmvva 116,48,84\nwl 8\nlc 12,21,67,0\nmvva 124,56,84\ndt 48,8,20480\ndt 50,8,30720\nmvva 124,60,92\n"
"dt 52,8,37888\ndt 54,8,48128\nmvva 120,60,88\nmvva 120,56,80\nwb 27,8\nlc 16,21,67,1024\nmvva 64,32,96\ns_mov_b32 s65, 2162688\n"
"dt 56,9,20480\ndt 58,9,30720\nmvva 64,36,104\nsm 23552\ndt 60,9,37888\ndt 62,9,48128\nmvva 68,36,108\nlb 18,4,65,0\n"
"mvva 68,32,100\nwl 8\nmvva 76,40,100\ndt 32,6,36864\ndt 34,6,47104\nmvva 76,44,108\nsm 24576\ndt 36,6,38912\n"
"dt 38,6,49152\nmvva 72,44,104\nlb 19,4,65,0\nmvva 72,40,96\nwl 8\nmvva 80,48,96\ns_mov_b32 s64, 2424832\ndt 40,7,36864\n"
"dt 42,7,47104\nmvva 80,52,104\nsm 40960\ndt 44,7,38912\ndt 46,7,49152\nmvva 84,52,108\nlb 18,4,64,0\nmvva 84,48,100\n"
"wl 8\nmvva 92,56,100\ndt 48,8,36864\ndt 50,8,47104\nmvva 92,60,108\nsm 41984\ndt 52,8,38912\ndt 54,8,49152\n"
"mvva 88,60,104\nlb 19,4,64,0\nmvva 88,56,96\nwl 8\nmvva 96,32,96\ns_mov_b32 s65, 2686976\ndt 56,9,36864\ndt 58,9,47104\n"
"mvva 96,36,104\nsm 43008\ndt 60,9,38912\ndt 62,9,49152\nmvva 100,36,108\nlb 18,4,65,0\nmvva 100,32,100\nwl 8\n"
"mvva 108,40,100\ndt 32,6,39936\ndt 34,6,50176\nmvva 108,44,108\nsm 44032\ndt 36,6,57344\ndt 38,12,2048\nmvva 104,44,104\n"
"lb 19,4,65,0\nmvva 104,40,96\nwl 8\nmvva 112,48,96\ns_mov_b32 s64, 2949120\ndt 40,7,39936\ndt 42,7,50176\nmvva 112,52,104\n"
"sm 45056\ndt 44,7,57344\ndt 46,13,2048\nmvva 116,52,108\nlb 18,4,64,0\nmvva 116,48,100\nwl 8\nmvva 124,56,100\n"
"dt 48,8,39936\ndt 50,8,50176\nmvva 124,60,108\nsm 61440\ndt 52,8,57344\ndt 54,14,2048\nmvva 120,60,104\nlb 19,4,64,0\n"
"mvva 120,56,96\nwb 32,8\nmvva 64,32,112\ndt 56,9,39936\ndt 58,9,50176\nmvva 64,36,120\ndt 60,9,57344\ndt 62,15,2048\n"
"mvva 68,36,124\ns_add_u32 s66, s28, 135168\nmvva 68,32,116\nwl 8\nlc 20,21,66,0\nmvva 76,40,116\ndt 32,6,40960\ndt 34,6,51200\n"
"mvva 76,44,124\ndt 36,6,58368\ndt 38,12,3072\nmvva 72,44,120\nmvva 72,40,112\nwl 8\nlc 24,21,66,1024\nmvva 80,48,112\n"
"dt 40,7,40960\ndt 42,7,51200\nmvva 80,52,120\ndt 44,7,58368\ndt 46,13,3072\nmvva 84,52,124\ns_add_u32 s67, s28, 151552\nmvva 84,48,116\n"
"wl 8\nlc 28,21,67,0\nmvva 92,56,116\ndt 48,8,40960\ndt 50,8,51200\nmvva 92,60,124\ndt 52,8,58368\ndt 54,14,3072\n"
"mvva 88,60,120\nmvva 88,56,112\nwl 8\nlc 32,21,67,1024\ns_barrier\nmvva 96,32,112\ndt 56,9,40960\ndt 58,9,51200\n"
"mvva 96,36,120\ndt 60,9,58368\ndt 62,15,3072\nmvva 100,36,124\nmvva 100,32,116\nwl 8\nmvva 108,40,116\ndt 32,6,59392\n"
"dt 34,12,4096\nmvva 108,44,124\ndt 36,6,61440\ndt 38,12,6144\nmvva 104,44,120\ns_add_u32 s66, s28, 167936\nmvva 104,40,112\nwl 8\n"
"lc 36,21,66,0\nmvva 112,48,112\ndt 40,7,59392\ndt 42,13,4096\nmvva 112,52,120\ndt 44,7,61440\ndt 46,13,6144\nmvva 116,52,124\n"
"mvva 116,48,116\nwl 8\nlc 40,21,66,1024\nmvva 124,56,116\ndt 48,8,59392\ndt 50,14,4096\nmvva 124,60,124\ndt 52,8,61440\n"
"dt 54,14,6144\nmvva 120,60,120\ns_add_u32 s67, s28, 184320\nmvva 120,56,112\nwb 34,8\nlc 44,21,67,0\nmvva 64,32,128\ndt 56,9,59392\n"
"dt 58,15,4096\nmvva 64,36,136\ndt 60,9,61440\ndt 62,15,6144\nmvva 68,36,140\nmvva 68,32,132\nwl 8\nlc 48,21,67,1024\n"
"mvva 76,40,132\ns_mov_b32 s65, 3211264\ndt 32,6,60416\ndt 34,12,5120\nmvva 76,44,140\nsm 62464\ndt 36,12,12288\ndt 38,12,22528\n"
"mvva 72,44,136\nlb 18,4,65,0\nmvva 72,40,128\nwl 8\nmvva 80,48,128\ndt 40,7,60416\ndt 42,13,5120\nmvva 80,52,136\n"
"sm 63488\ndt 44,13,12288\ndt 46,13,22528\nmvva 84,52,140\nlb 19,4,65,0\nmvva 84,48,132\nwl 8\nmvva 92,56,132\n"
"s_mov_b32 s64, 3473408\ndt 48,8,60416\ndt 50,14,5120\nmvva 92,60,140\nsm 64512\ndt 52,14,12288\ndt 54,14,22528\nmvva 88,60,136\n"
"lb 18,4,64,0\nmvva 88,56,128\nwl 8\nmvva 96,32,128\ndt 56,9,60416\ndt 58,15,5120\nmvva 96,36,136\nsm 65536\n"
"dt 60,15,12288\ndt 62,15,22528\nmvva 100,36,140\nlb 19,4,64,0\nmvva 100,32,132\nwl 8\nmvva 108,40,132\ns_mov_b32 s65, 3735552\n"
"dt 32,12,13312\ndt 34,12,23552\nmvva 108,44,140\nsm 81920\ndt 36,12,15360\ndt 38,12,25600\nmvva 104,44,136\nlb 18,4,65,0\n"
"mvva 104,40,128\nwl 8\nmvva 112,48,128\ndt 40,13,13312\ndt 42,13,23552\nmvva 112,52,136\nsm 82944\ndt 44,13,15360\n"
"dt 46,13,25600\nmvva 116,52,140\nlb 19,4,65,0\nmvva 116,48,132\nwl 8\nmvva 124,56,132\ns_mov_b32 s64, 3997696\ndt 48,14,13312\n"
"dt 50,14,23552\nmvva 124,60,140\nsm 83968\ndt 52,14,15360\ndt 54,14,25600\nmvva 120,60,136\nlb 18,4,64,0\nmvva 120,56,128\n"
"wb 39,8\nmvva 64,32,144\ndt 56,15,13312\ndt 58,15,23552\nmvva 64,36,152\nsm 84992\ndt 60,15,15360\ndt 62,15,25600\n"
"mvva 68,36,156\nlb 19,4,64,0\nmvva 68,32,148\nwl 8\nmvva 76,40,148\ndt 32,12,14336\ndt 34,12,24576\nmvva 76,44,156\n"
"dt 36,12,16384\ndt 38,12,26624\nmvva 72,44,152\ns_add_u32 s66, s28, 200704\nmvva 72,40,144\nwl 8\nlc 52,21,66,0\nmvva 80,48,144\n"
"dt 40,13,14336\ndt 42,13,24576\nmvva 80,52,152\ndt 44,13,16384\ndt 46,13,26624\nmvva 84,52,156\nmvva 84,48,148\nwl 8\n"
"lc 56,21,66,1024\nmvva 92,56,148\ndt 48,14,14336\ndt 50,14,24576\nmvva 92,60,156\ndt 52,14,16384\ndt 54,14,26624\nmvva 88,60,152\n"
"s_add_u32 s67, s28, 217088\nmvva 88,56,144\nwb 34,8\nlc 60,21,67,0\ns_barrier\nmvva 96,32,144\ndt 56,15,14336\ndt 58,15,24576\n"
"mvva 96,36,152\ndt 60,15,16384\ndt 62,15,26624\nmvva 100,36,156\nmvva 100,32,148\nwl 8\nlc 64,21,67,1024\nmvva 108,40,148\n"
"dt 32,0,0\ndt 34,0,10240\nmvva 108,44,156\ndt 36,0,2048\ndt 38,0,12288\nmvva 104,44,152\ns_add_u32 s66, s28, 233472\nmvva 104,40,144\n"
"wl 8\nlc 68,21,66,0\nmvva 112,48,144\ndt 40,1,0\ndt 42,1,10240\nmvva 112,52,152\ndt 44,1,2048\ndt 46,1,12288\n"
"mvva 116,52,156\nmvva 116,48,148\nwl 8\nlc 72,21,66,1024\nmvva 124,56,148\ndt 48,2,0\ndt 50,2,10240\nmvva 124,60,156\n"
"dt 52,2,2048\ndt 54,2,12288\nmvva 120,60,152\ns_add_u32 s67, s28, 249856\nmvva 120,56,144\nwb 34,8\nlc 76,21,67,0\nmvva 64,32,160\n"
"dt 56,3,0\ndt 58,3,10240\nmvva 64,36,168\ndt 60,3,2048\ndt 62,3,12288\nmvva 68,36,0\nmvva 68,32,164\nwl 8\n"
"mvva 76,40,164\ndt 32,0,1024\ndt 34,0,11264\nmvva 76,44,0\ndt 36,0,3072\ndt 38,0,13312\nmvva 72,44,168\nmvva 72,40,160\n"
"wl 8\nlc 80,21,67,1024\nmvva 80,48,160\ns_mov_b32 s65, 131072\ndt 40,1,1024\ndt 42,1,11264\nmvva 80,52,168\nsm 86016\n"
"dt 44,1,3072\ndt 46,1,13312\nmvva 84,52,0\nlb 18,4,65,0\nmvva 84,48,164\nwl 8\nmvva 92,56,164\ndt 48,2,1024\n"
"dt 50,2,11264\nmvva 92,60,0\nsm 102400\ndt 52,2,3072\ndt 54,2,13312\nmvva 88,60,168\nlb 19,4,65,0\nmvva 88,56,160\n"
"wl 8\nmvva 96,32,160\ns_mov_b32 s64, 393216\ndt 56,3,1024\ndt 58,3,11264\nmvva 96,36,168\nsm 103424\ndt 60,3,3072\n"
"dt 62,3,13312\nmvva 100,36,0\nlb 18,4,64,0\nmvva 100,32,164\nwl 8\nmvva 108,40,164\ndt 32,0,4096\ndt 34,0,14336\n"
"mvva 108,44,0\nsm 104448\ndt 36,0,21504\ndt 38,0,31744\nmvva 104,44,168\nlb 19,4,64,0\nmvva 104,40,160\nwl 8\n"
"mvva 112,48,160\ns_mov_b32 s65, 655360\ndt 40,1,4096\ndt 42,1,14336\nmvva 112,52,168\nsm 105472\ndt 44,1,21504\ndt 46,1,31744\n"
"mvva 116,52,0\nlb 18,4,65,0\nmvva 116,48,164\nwl 8\nmvva 124,56,164\ndt 48,2,4096\ndt 50,2,14336\nmvva 124,60,0\n"
"sm 106496\ndt 52,2,21504\ndt 54,2,31744\nmvva 120,60,168\nlb 19,4,65,0\nmvva 120,56,160\nwb 38,8\nmvva 64,32,4\n"
"s_mov_b32 s64, 917504\ndt 56,3,4096\ndt 58,3,14336\nmvva 64,36,12\nsm 122880\ndt 60,3,21504\ndt 62,3,31744\nmvva 68,36,16\n"
"lb 18,4,64,0\nmvva 68,32,8\nwl 8\nmvva 76,40,8\ndt 32,0,20480\ndt 34,0,30720\nmvva 76,44,16\nsm 123904\n"
"dt 36,0,22528\ndt 38,0,32768\nmvva 72,44,12\nlb 19,4,64,0\nmvva 72,40,4\nwl 8\nmvva 80,48,4\ndt 40,1,20480\n"
"dt 42,1,30720\nmvva 80,52,12\ndt 44,1,22528\ndt 46,1,32768\nmvva 84,52,16\ns_add_u32 s66, s28, 8192\nmvva 84,48,8\nwl 8\n"
"lc 84,21,66,0\nmvva 92,56,8\ndt 48,2,20480\ndt 50,2,30720\nmvva 92,60,16\ndt 52,2,22528\ndt 54,2,32768\nmvva 88,60,12\n"
"mvva 88,56,4\nwb 33,8\nlc 88,21,66,1024\ns_barrier\nmvva 96,32,4\ndt 56,3,20480\ndt 58,3,30720\nmvva 96,36,12\n"
"dt 60,3,22528\ndt 62,3,32768\nmvva 100,36,16\ns_add_u32 s67, s28, 24576\nmvva 100,32,8\nwl 8\nlc 92,21,67,0\nmvva 108,40,8\n"
"dt 32,0,23552\ndt 34,0,33792\nmvva 108,44,16\ndt 36,0,40960\ndt 38,0,51200\nmvva 104,44,12\nmvva 104,40,4\nwl 8\n"
"lc 96,21,67,1024\nmvva 112,48,4\ndt 40,1,23552\ndt 42,1,33792\nmvva 112,52,12\ndt 44,1,40960\ndt 46,1,51200\nmvva 116,52,16\n"
"s_add_u32 s66, s28, 40960\nmvva 116,48,8\nwl 8\nlc 100,21,66,0\nmvva 124,56,8\ndt 48,2,23552\ndt 50,2,33792\nmvva 124,60,16\n"
"dt 52,2,40960\ndt 54,2,51200\nmvva 120,60,12\nmvva 120,56,4\nwb 33,8\nlc 104,21,66,1024\nmvva 64,32,20\ndt 56,3,23552\n"
"dt 58,3,33792\nmvva 64,36,28\ndt 60,3,40960\ndt 62,3,51200\nmvva 68,36,32\ns_add_u32 s67, s28, 57344\nmvva 68,32,24\nwl 8\n"
"lc 108,21,67,0\nmvva 76,40,24\ndt 32,0,24576\ndt 34,0,34816\nmvva 76,44,32\ndt 36,0,41984\ndt 38,0,52224\nmvva 72,44,28\n"
"mvva 72,40,20\nwl 8\nlc 112,21,67,1024\nmvva 80,48,20\ns_mov_b32 s65, 1179648\ndt 40,1,24576\ndt 42,1,34816\nmvva 80,52,28\n"
"sm 124928\ndt 44,1,41984\ndt 46,1,52224\nmvva 84,52,32\nlb 18,4,65,0\nmvva 84,48,24\nwl 8\nmvva 92,56,24\n"
"dt 48,2,24576\ndt 50,2,34816\nmvva 92,60,32\nsm 125952\ndt 52,2,41984\ndt 54,2,52224\nmvva 88,60,28\nlb 19,4,65,0\n"
"mvva 88,56,20\nwl 8\nmvva 96,32,20\ndt 56,3,24576\ndt 58,3,34816\nmvva 96,36,28\ndt 60,3,41984\ndt 62,3,52224\n"
"mvva 100,36,32\nmvva 100,32,24\nwl 8\nmvva 108,40,24\ns_mov_b32 s64, 1441792\ndt 32,0,43008\ndt 34,0,53248\nmvva 108,44,32\n"
"sm 126976\ndt 36,0,45056\ndt 38,0,55296\nmvva 104,44,28\nlb 18,4,64,0\nmvva 104,40,20\nwl 8\nmvva 112,48,20\n"
"dt 40,1,43008\ndt 42,1,53248\nmvva 112,52,28\nsm 143360\ndt 44,1,45056\ndt 46,1,55296\nmvva 116,52,32\nlb 19,4,64,0\n"
"mvva 116,48,24\nwl 8\nmvva 124,56,24\ns_mov_b32 s65, 1703936\ndt 48,2,43008\ndt 50,2,53248\nmvva 124,60,32\nsm 144384\n"
"dt 52,2,45056\ndt 54,2,55296\nmvva 120,60,28\nlb 18,4,65,0\nmvva 120,56,20\nwb 37,8\nmvva 64,32,36\ndt 56,3,43008\n"
"dt 58,3,53248\nmvva 64,36,44\ndt 60,3,45056\ndt 62,3,55296\nmvva 68,36,48\ns_add_u32 s66, s28, 73728\nmvva 68,32,40\nwl 8\n"
"lc 116,21,66,0\nmvva 76,40,40\ndt 32,0,44032\ndt 34,0,54272\nmvva 76,44,48\ndt 36,0,61440\ndt 38,6,6144\nmvva 72,44,44\n"
"mvva 72,40,36\nwl 8\nlc 120,21,66,1024\nmvva 80,48,36\ndt 40,1,44032\ndt 42,1,54272\nmvva 80,52,44\ndt 44,1,61440\n"
"dt 46,7,6144\nmvva 84,52,48\ns_add_u32 s67, s28, 90112\nmvva 84,48,40\nwl 8\nlc 124,21,67,0\nmvva 92,56,40\ndt 48,2,44032\n"
"dt 50,2,54272\nmvva 92,60,48\ndt 52,2,61440\ndt 54,8,6144\nmvva 88,60,44\nmvva 88,56,36\nwb 32,8\nlc 128,21,67,1024\n"
"s_barrier\nmvva 96,32,36\ndt 56,3,44032\ndt 58,3,54272\nmvva 96,36,44\nsm 145408\ndt 60,3,61440\ndt 62,9,6144\n"
"mvva 100,36,48\nlb 19,4,65,0\nmvva 100,32,40\nwl 8\nmvva 108,40,40\ns_mov_b32 s64, 1966080\ndt 32,0,62464\ndt 34,6,7168\n"
"mvva 108,44,48\nsm 146432\ndt 36,0,64512\ndt 38,6,9216\nmvva 104,44,44\nlb 18,4,64,0\nmvva 104,40,36\nwl 8\n"
"mvva 112,48,36\ndt 40,1,62464\ndt 42,7,7168\nmvva 112,52,44\nsm 147456\ndt 44,1,64512\ndt 46,7,9216\nmvva 116,52,48\n"
"lb 19,4,64,0\nmvva 116,48,40\nwl 8\nv_lshrrev_b32 v20, 2, v23\nv_and_b32 v20, 28, v20\nbuffer_load_dword v31, v20, s[56:59], s78 offen offset:64 sc1\nmvva 124,56,40\ndt 48,2,62464\n"
"dt 50,8,7168\nmvva 124,60,48\ndt 52,2,64512\ndt 54,8,9216\nmvva 120,60,44\ns_add_u32 s66, s28, 106496\nmvva 120,56,36\nwb 33,8\n"
"lc 132,21,66,0\nmvva 64,32,52\ndt 56,3,62464\ndt 58,9,7168\nmvva 64,36,60\ndt 60,3,64512\ndt 62,9,9216\nmvva 68,36,64\n"
"mvva 68,32,56\nwl 8\nlc 136,21,66,1024\nmvva 76,40,56\ndt 32,0,63488\ndt 34,6,8192\nmvva 76,44,64\ndt 36,6,0\n"
"dt 38,6,10240\nmvva 72,44,60\ns_add_u32 s67, s28, 122880\nmvva 72,40,52\nwl 8\nlc 140,21,67,0\nmvva 80,48,52\ndt 40,1,63488\n"
"dt 42,7,8192\nmvva 80,52,60\ndt 44,7,0\ndt 46,7,10240\nmvva 84,52,64\nmvva 84,48,56\nwl 8\nlc 144,21,67,1024\n"
"mvva 92,56,56\ns_mov_b32 s65, 2228224\ndt 48,2,63488\ndt 50,8,8192\nmvva 92,60,64\nsm 0\ndt 52,8,0\ndt 54,8,10240\n"
"mvva 88,60,60\nlb 18,4,65,0\nmvva 88,56,52\nwl 8\nmvva 96,32,52\ndt 56,3,63488\ndt 58,9,8192\nmvva 96,36,60\n"
"sm 1024\ndt 60,9,0\ndt 62,9,10240\nmvva 100,36,64\nlb 19,4,65,0\nmvva 100,32,56\nwl 8\nmvva 108,40,56\n"
"s_mov_b32 s64, 2490368\ndt 32,6,16384\ndt 34,6,26624\nmvva 108,44,64\nsm 2048\ndt 36,6,18432\ndt 38,6,28672\nmvva 104,44,60\n"
"lb 18,4,64,0\nmvva 104,40,52\nwl 8\nmvva 112,48,52\ndt 40,7,16384\ndt 42,7,26624\nmvva 112,52,60\nsm 3072\n"
"dt 44,7,18432\ndt 46,7,28672\nmvva 116,52,64\nlb 19,4,64,0\nmvva 116,48,56\nwl 8\nmvva 124,56,56\ns_mov_b32 s65, 2752512\n"
"dt 48,8,16384\ndt 50,8,26624\nmvva 124,60,64\nsm 4096\ndt 52,8,18432\ndt 54,8,28672\nmvva 120,60,60\nlb 18,4,65,0\n"
"mvva 120,56,52\nwb 38,8\nmvva 64,32,68\ndt 56,9,16384\ndt 58,9,26624\nmvva 64,36,76\ndt 60,9,18432\ndt 62,9,28672\n"
"mvva 68,36,80\nmvva 68,32,72\nwl 8\nmvva 76,40,72\ndt 32,6,17408\ndt 34,6,27648\nmvva 76,44,80\nsm 20480\n"
"dt 36,6,19456\ndt 38,6,29696\nmvva 72,44,76\nlb 19,4,65,0\nmvva 72,40,68\nwl 8\nmvva 80,48,68\ns_mov_b32 s64, 3014656\n"
"dt 40,7,17408\ndt 42,7,27648\nmvva 80,52,76\nsm 21504\ndt 44,7,19456\ndt 46,7,29696\nmvva 84,52,80\nlb 18,4,64,0\n"
"mvva 84,48,72\nwl 8\ns_waitcnt vmcnt(11)\nv_cmp_ne_u32 vcc, s34, v31\ns_cbranch_vccz 55f\ns_mov_b32 s69, 0x80000\n54:\ns_sleep 1\n"
"buffer_load_dword v31, v20, s[56:59], s78 offen offset:64 sc1\ns_waitcnt vmcnt(0)\nv_cmp_ne_u32 vcc, s34, v31\ns_cbranch_vccz 55f\ns_sub_u32 s69, s69, 1\ns_cbranch_scc0 54b\n55:\nv_lshrrev_b32 v20, 4, v23\n"
"v_and_b32 v20, 48, v20\nmvva 92,56,72\ndt 48,8,17408\ndt 50,8,27648\nmvva 92,60,80\ns_add_u32 s88, s74, 0\ndt 52,8,19456\ndt 54,8,29696\n"
"mvva 88,60,76\nlc 192,23,88,0\nmvva 88,56,68\nwl 8\nlc 196,23,88,1024\ns_barrier\nmvva 96,32,68\ndt 56,9,17408\n"
"dt 58,9,27648\nmvva 96,36,76\ndt 60,9,19456\ndt 62,9,29696\nmvva 100,36,80\ns_add_u32 s89, s74, 4096\nlc 200,20,88,2048\nmvva 100,32,72\n"
"wl 8\nlc 204,23,89,0\nmvva 108,40,72\ndt 32,6,20480\ndt 34,6,30720\nmvva 108,44,80\ndt 36,6,37888\ndt 38,6,48128\n"
"mvva 104,44,76\nlc 208,23,89,1024\nmvva 104,40,68\nwl 8\nlc 212,20,89,2048\nmvva 112,48,68\ndt 40,7,20480\ndt 42,7,30720\n"
"mvva 112,52,76\ns_add_u32 s88, s74, 8192\ndt 44,7,37888\ndt 46,7,48128\nmvva 116,52,80\nlc 216,23,88,0\nmvva 116,48,72\nwl 8\n"
"lc 220,23,88,1024\nmvva 124,56,72\ndt 48,8,20480\ndt 50,8,30720\nmvva 124,60,80\ndt 52,8,37888\ndt 54,8,48128\nmvva 120,60,76\n"
"s_add_u32 s89, s74, 12288\nlc 224,20,88,2048\nmvva 120,56,68\nwl 8\nlc 228,23,89,0\nmvva 64,32,84\ndt 56,9,20480\ndt 58,9,30720\n"
"mvva 64,36,92\ndt 60,9,37888\ndt 62,9,48128\nmvva 68,36,96\nlc 232,23,89,1024\nmvva 68,32,88\nwl 8\nlc 236,20,89,2048\n"
"mvva 76,40,88\ndt 32,6,36864\ndt 34,6,47104\nmvva 76,44,96\nsm 22528\ndt 36,6,38912\ndt 38,6,49152\nmvva 72,44,92\n"
"lb 19,4,64,0\nmvva 72,40,84\nwl 8\nmvva 80,48,84\ndt 40,7,36864\ndt 42,7,47104\nmvva 80,52,92\ndt 44,7,38912\n"
"dt 46,7,49152\nmvva 84,52,96\ns_add_u32 s66, s28, 139264\nmvva 84,48,88\nwl 8\nlc 148,21,66,0\nmvva 92,56,88\ndt 48,8,36864\n"
"dt 50,8,47104\nmvva 92,60,96\ndt 52,8,38912\ndt 54,8,49152\nmvva 88,60,92\nmvva 88,56,84\nwl 8\nlc 152,21,66,1024\n"
"mvva 96,32,84\ndt 56,9,36864\ndt 58,9,47104\nmvva 96,36,92\ndt 60,9,38912\ndt 62,9,49152\nmvva 100,36,96\ns_add_u32 s67, s28, 155648\n"
"mvva 100,32,88\nwl 8\nlc 156,21,67,0\nmvva 108,40,88\ndt 32,6,39936\ndt 34,6,50176\nmvva 108,44,96\ndt 36,6,57344\n"
"dt 38,12,2048\nmvva 104,44,92\nmvva 104,40,84\nwl 8\nlc 160,21,67,1024\nmvva 112,48,84\ndt 40,7,39936\ndt 42,7,50176\n"
"mvva 112,52,92\ndt 44,7,57344\ndt 46,13,2048\nmvva 116,52,96\ns_add_u32 s66, s28, 172032\nmvva 116,48,88\nwl 8\nlc 164,21,66,0\n"
"mvva 124,56,88\ndt 48,8,39936\ndt 50,8,50176\nmvva 124,60,96\ndt 52,8,57344\ndt 54,14,2048\nmvva 120,60,92\nmvva 120,56,84\n"
"wl 8\nlc 168,21,66,1024\nmvva 64,32,100\ndt 56,9,39936\ndt 58,9,50176\nmvva 64,36,108\ndt 60,9,57344\ndt 62,15,2048\n"
"mvva 68,36,112\ns_add_u32 s67, s28, 188416\nmvva 68,32,104\nwl 8\nlc 0,21,67,0\nlc 4,21,67,1024\ns_add_u32 s86, s73, 4096\ns_mov_b32 s87, 0x358637bd\n"
"zvav 152,240,192\ns_mov_b32 s65, 3276800\nsm 23552\ns_mov_b32 s64, 3538944\ns_waitcnt vmcnt(18)\nzvav 144,200,192\nzvav 156,240,208\nlb 18,4,65,0\n"
"s_waitcnt vmcnt(16)\nzvav 148,212,208\nsm 24576\nv_rcp_f32 v154, v152\nzvav 128,192,192\nzvav 132,196,192\nv_fma_f32 v144, v144, v154, s87\nzvav 136,204,208\n"
"v_rcp_f32 v155, v156\nzvav 140,208,208\nv_fma_f32 v144, v148, v155, v144\nlb 19,4,65,0\nvr 144\nsm 40960\nv_pk_mul_f32 v[154:155], v[154:155], v[144:145] op_sel:[0,0] op_sel_hi:[1,0]\nv_pk_mul_f32 v[128:129], v[128:129], v[154:155] op_sel:[0,0] op_sel_hi:[1,0]\n"
"v_pk_mul_f32 v[130:131], v[130:131], v[154:155] op_sel:[0,0] op_sel_hi:[1,0]\nv_pk_mul_f32 v[132:133], v[132:133], v[154:155] op_sel:[0,0] op_sel_hi:[1,0]\nv_pk_mul_f32 v[134:135], v[134:135], v[154:155] op_sel:[0,0] op_sel_hi:[1,0]\nv_pk_fma_f32 v[128:129], v[136:137], v[154:155], v[128:129] op_sel:[0,1,0] op_sel_hi:[1,1,1]\nv_pk_fma_f32 v[130:131], v[138:139], v[154:155], v[130:131] op_sel:[0,1,0] op_sel_hi:[1,1,1]\nv_pk_fma_f32 v[132:133], v[140:141], v[154:155], v[132:133] op_sel:[0,1,0] op_sel_hi:[1,1,1]\nv_pk_fma_f32 v[134:135], v[142:143], v[154:155], v[134:135] op_sel:[0,1,0] op_sel_hi:[1,1,1]\ncv 128,128,129\n"
"lb 18,4,64,0\ncv 129,130,131\ncv 130,132,133\ncv 131,134,135\nbuffer_store_dwordx4 v[128:131], v21, s[56:59], s73 offen offset:0 sc1\nsm 41984\nzvav 184,240,196\ns_mov_b32 s65, 3801088\n"
"zvav 176,200,196\nlb 19,4,64,0\nzvav 188,240,212\nsm 43008\ns_mov_b32 s64, 4063232\nzvav 180,212,212\ns_nop 0\nv_rcp_f32 v186, v184\n"
"zvav 160,192,196\nv_fma_f32 v176, v176, v186, s87\nlb 18,4,65,0\nzvav 164,196,196\nv_rcp_f32 v187, v188\nzvav 168,204,212\nv_fma_f32 v176, v180, v187, v176\nvr 176\n"
"zvav 172,208,212\nsm 44032\ns_nop 0\nlb 19,4,65,0\nsm 45056\nzvav 152,240,200\ns_mov_b32 s65, 196608\nzvav 144,200,200\n"
"zvav 156,240,216\nlb 18,4,64,0\nsm 61440\nv_pk_mul_f32 v[186:187], v[186:187], v[176:177] op_sel:[0,0] op_sel_hi:[1,0]\nv_pk_mul_f32 v[160:161], v[160:161], v[186:187] op_sel:[0,0] op_sel_hi:[1,0]\nv_pk_mul_f32 v[162:163], v[162:163], v[186:187] op_sel:[0,0] op_sel_hi:[1,0]\nv_pk_mul_f32 v[164:165], v[164:165], v[186:187] op_sel:[0,0] op_sel_hi:[1,0]\nv_pk_mul_f32 v[166:167], v[166:167], v[186:187] op_sel:[0,0] op_sel_hi:[1,0]\n"
"v_pk_fma_f32 v[160:161], v[168:169], v[186:187], v[160:161] op_sel:[0,1,0] op_sel_hi:[1,1,1]\nv_pk_fma_f32 v[162:163], v[170:171], v[186:187], v[162:163] op_sel:[0,1,0] op_sel_hi:[1,1,1]\nv_pk_fma_f32 v[164:165], v[172:173], v[186:187], v[164:165] op_sel:[0,1,0] op_sel_hi:[1,1,1]\nv_pk_fma_f32 v[166:167], v[174:175], v[186:187], v[166:167] op_sel:[0,1,0] op_sel_hi:[1,1,1]\ncv 160,160,161\ncv 161,162,163\ncv 162,164,165\ncv 163,166,167\n"
"buffer_store_dwordx4 v[160:163], v21, s[56:59], s73 offen offset:1024 sc1\nlb 19,4,64,0\nzvav 148,212,216\nv_rcp_f32 v154, v152\nzvav 128,192,200\ns_add_u32 s66, s28, 204800\nv_rcp_f32 v155, v156\nzvav 132,196,200\n"
"v_fma_f32 v144, v144, v154, s87\ns_add_u32 s67, s28, 221184\ns_mov_b32 s64, 458752\nv_fma_f32 v144, v148, v155, v144\nzvav 136,204,216\nlc 8,21,66,0\nzvav 140,208,216\nvr 144\n"
"zvav 184,240,204\nzvav 176,200,204\nlc 12,21,66,1024\nv_pk_mul_f32 v[154:155], v[154:155], v[144:145] op_sel:[0,0] op_sel_hi:[1,0]\nv_pk_mul_f32 v[128:129], v[128:129], v[154:155] op_sel:[0,0] op_sel_hi:[1,0]\nv_pk_mul_f32 v[130:131], v[130:131], v[154:155] op_sel:[0,0] op_sel_hi:[1,0]\nv_pk_mul_f32 v[132:133], v[132:133], v[154:155] op_sel:[0,0] op_sel_hi:[1,0]\nv_pk_mul_f32 v[134:135], v[134:135], v[154:155] op_sel:[0,0] op_sel_hi:[1,0]\n"
"v_pk_fma_f32 v[128:129], v[136:137], v[154:155], v[128:129] op_sel:[0,1,0] op_sel_hi:[1,1,1]\nv_pk_fma_f32 v[130:131], v[138:139], v[154:155], v[130:131] op_sel:[0,1,0] op_sel_hi:[1,1,1]\nv_pk_fma_f32 v[132:133], v[140:141], v[154:155], v[132:133] op_sel:[0,1,0] op_sel_hi:[1,1,1]\nv_pk_fma_f32 v[134:135], v[142:143], v[154:155], v[134:135] op_sel:[0,1,0] op_sel_hi:[1,1,1]\ncv 128,128,129\ncv 129,130,131\ncv 130,132,133\nlc 16,21,67,0\n"
"cv 131,134,135\nbuffer_store_dwordx4 v[128:131], v21, s[56:59], s73 offen offset:2048 sc1\nzvav 188,240,220\nv_rcp_f32 v186, v184\nzvav 180,212,220\nv_fma_f32 v176, v176, v186, s87\nlc 20,21,67,1024\nzvav 160,192,204\n"
"s_add_u32 s66, s28, 237568\ns_add_u32 s67, s28, 253952\nsm 62464\nv_rcp_f32 v187, v188\nzvav 164,196,204\nv_fma_f32 v176, v180, v187, v176\nvr 176\nzvav 168,204,220\n"
"lc 24,21,66,0\nzvav 172,208,220\nzvav 152,240,224\ns_waitcnt vmcnt(28)\nzvav 144,224,224\nlc 28,21,66,1024\nv_pk_mul_f32 v[186:187], v[186:187], v[176:177] op_sel:[0,0] op_sel_hi:[1,0]\nv_pk_mul_f32 v[160:161], v[160:161], v[186:187] op_sel:[0,0] op_sel_hi:[1,0]\n"
"v_pk_mul_f32 v[162:163], v[162:163], v[186:187] op_sel:[0,0] op_sel_hi:[1,0]\nv_pk_mul_f32 v[164:165], v[164:165], v[186:187] op_sel:[0,0] op_sel_hi:[1,0]\nv_pk_mul_f32 v[166:167], v[166:167], v[186:187] op_sel:[0,0] op_sel_hi:[1,0]\nv_pk_fma_f32 v[160:161], v[168:169], v[186:187], v[160:161] op_sel:[0,1,0] op_sel_hi:[1,1,1]\nv_pk_fma_f32 v[162:163], v[170:171], v[186:187], v[162:163] op_sel:[0,1,0] op_sel_hi:[1,1,1]\nv_pk_fma_f32 v[164:165], v[172:173], v[186:187], v[164:165] op_sel:[0,1,0] op_sel_hi:[1,1,1]\nv_pk_fma_f32 v[166:167], v[174:175], v[186:187], v[166:167] op_sel:[0,1,0] op_sel_hi:[1,1,1]\ncv 160,160,161\n"
"cv 161,162,163\ncv 162,164,165\ncv 163,166,167\nlc 32,21,67,0\nbuffer_store_dwordx4 v[160:163], v21, s[56:59], s73 offen offset:3072 sc1\nzvav 156,240,240\nv_rcp_f32 v154, v152\ns_waitcnt vmcnt(28)\n"
"zvav 148,236,240\nv_fma_f32 v144, v144, v154, s87\nzvav 128,216,224\nlc 36,21,67,1024\nzvav 132,220,224\nzvav 136,228,240\nv_rcp_f32 v155, v156\nzvav 140,232,240\n"
"s_nop 0\nv_fma_f32 v144, v148, v155, v144\nvr 144\nlb 18,4,65,0\nv_pk_mul_f32 v[154:155], v[154:155], v[144:145] op_sel:[0,0] op_sel_hi:[1,0]\nv_pk_mul_f32 v[128:129], v[128:129], v[154:155] op_sel:[0,0] op_sel_hi:[1,0]\nv_pk_mul_f32 v[130:131], v[130:131], v[154:155] op_sel:[0,0] op_sel_hi:[1,0]\nv_pk_mul_f32 v[132:133], v[132:133], v[154:155] op_sel:[0,0] op_sel_hi:[1,0]\n"
"v_pk_mul_f32 v[134:135], v[134:135], v[154:155] op_sel:[0,0] op_sel_hi:[1,0]\nv_pk_fma_f32 v[128:129], v[136:137], v[154:155], v[128:129] op_sel:[0,1,0] op_sel_hi:[1,1,1]\nv_pk_fma_f32 v[130:131], v[138:139], v[154:155], v[130:131] op_sel:[0,1,0] op_sel_hi:[1,1,1]\nsm 63488\nv_pk_fma_f32 v[132:133], v[140:141], v[154:155], v[132:133] op_sel:[0,1,0] op_sel_hi:[1,1,1]\nv_pk_fma_f32 v[134:135], v[142:143], v[154:155], v[134:135] op_sel:[0,1,0] op_sel_hi:[1,1,1]\ncv 128,128,129\ncv 129,130,131\n"
"cv 130,132,133\nlb 19,4,65,0\ncv 131,134,135\nbuffer_store_dwordx4 v[128:131], v21, s[56:59], s86 offen offset:0 sc1\nzvav 184,240,228\nsm 64512\ns_mov_b32 s65, 720896\nzvav 176,224,228\n"
"lb 18,4,64,0\nzvav 188,240,244\nsm 65536\nzvav 180,236,244\ns_nop 0\nv_rcp_f32 v186, v184\nzvav 160,216,228\nlb 19,4,64,0\n"
"zvav 164,220,228\nv_fma_f32 v176, v176, v186, s87\nv_rcp_f32 v187, v188\nzvav 168,228,244\nv_fma_f32 v176, v180, v187, v176\nvr 176\nzvav 172,232,244\nsm 81920\n"
"s_add_u32 s66, s28, 12288\nlb 19,4,65,0\nzvav 152,240,232\ns_add_u32 s67, s28, 28672\nzvav 144,224,232\nzvav 156,240,248\nlc 40,21,66,0\nv_pk_mul_f32 v[186:187], v[186:187], v[176:177] op_sel:[0,0] op_sel_hi:[1,0]\n"
"v_pk_mul_f32 v[160:161], v[160:161], v[186:187] op_sel:[0,0] op_sel_hi:[1,0]\nv_pk_mul_f32 v[162:163], v[162:163], v[186:187] op_sel:[0,0] op_sel_hi:[1,0]\nv_pk_mul_f32 v[164:165], v[164:165], v[186:187] op_sel:[0,0] op_sel_hi:[1,0]\nv_pk_mul_f32 v[166:167], v[166:167], v[186:187] op_sel:[0,0] op_sel_hi:[1,0]\nv_pk_fma_f32 v[160:161], v[168:169], v[186:187], v[160:161] op_sel:[0,1,0] op_sel_hi:[1,1,1]\nv_pk_fma_f32 v[162:163], v[170:171], v[186:187], v[162:163] op_sel:[0,1,0] op_sel_hi:[1,1,1]\nv_pk_fma_f32 v[164:165], v[172:173], v[186:187], v[164:165] op_sel:[0,1,0] op_sel_hi:[1,1,1]\nv_pk_fma_f32 v[166:167], v[174:175], v[186:187], v[166:167] op_sel:[0,1,0] op_sel_hi:[1,1,1]\n"
"cv 160,160,161\ncv 161,162,163\ncv 162,164,165\ncv 163,166,167\nbuffer_store_dwordx4 v[160:163], v21, s[56:59], s86 offen offset:1024 sc1\nlc 44,21,66,1024\nzvav 148,236,248\nv_rcp_f32 v154, v152\n"
"zvav 128,216,232\nv_rcp_f32 v155, v156\nzvav 132,220,232\nv_fma_f32 v144, v144, v154, s87\ns_add_u32 s66, s28, 45056\nzvav 136,228,248\nlc 48,21,67,0\nzvav 140,232,248\n"
"v_fma_f32 v144, v148, v155, v144\nvr 144\nzvav 184,240,236\nzvav 176,224,236\nlc 52,21,67,1024\nzvav 188,240,252\ns_add_u32 s67, s28, 61440\nzvav 180,236,252\n"
"zvav 160,216,236\nlc 56,21,66,0\nv_pk_mul_f32 v[154:155], v[154:155], v[144:145] op_sel:[0,0] op_sel_hi:[1,0]\nv_pk_mul_f32 v[128:129], v[128:129], v[154:155] op_sel:[0,0] op_sel_hi:[1,0]\nv_pk_mul_f32 v[130:131], v[130:131], v[154:155] op_sel:[0,0] op_sel_hi:[1,0]\nv_pk_mul_f32 v[132:133], v[132:133], v[154:155] op_sel:[0,0] op_sel_hi:[1,0]\nv_pk_mul_f32 v[134:135], v[134:135], v[154:155] op_sel:[0,0] op_sel_hi:[1,0]\nv_pk_fma_f32 v[128:129], v[136:137], v[154:155], v[128:129] op_sel:[0,1,0] op_sel_hi:[1,1,1]\n"
"v_pk_fma_f32 v[130:131], v[138:139], v[154:155], v[130:131] op_sel:[0,1,0] op_sel_hi:[1,1,1]\nv_pk_fma_f32 v[132:133], v[140:141], v[154:155], v[132:133] op_sel:[0,1,0] op_sel_hi:[1,1,1]\nv_pk_fma_f32 v[134:135], v[142:143], v[154:155], v[134:135] op_sel:[0,1,0] op_sel_hi:[1,1,1]\ncv 128,128,129\ncv 129,130,131\ncv 130,132,133\ncv 131,134,135\nbuffer_store_dwordx4 v[128:131], v21, s[56:59], s86 offen offset:2048 sc1\n"
"lc 60,21,66,1024\nzvav 164,220,236\nv_rcp_f32 v186, v184\nzvav 168,228,252\nv_rcp_f32 v187, v188\nzvav 172,232,252\nv_fma_f32 v176, v176, v186, s87\nv_fma_f32 v176, v180, v187, v176\n"
"lc 64,21,67,0\nvr 176\ns_nop 0\nv_pk_mul_f32 v[186:187], v[186:187], v[176:177] op_sel:[0,0] op_sel_hi:[1,0]\nv_pk_mul_f32 v[160:161], v[160:161], v[186:187] op_sel:[0,0] op_sel_hi:[1,0]\nv_pk_mul_f32 v[162:163], v[162:163], v[186:187] op_sel:[0,0] op_sel_hi:[1,0]\nv_pk_mul_f32 v[164:165], v[164:165], v[186:187] op_sel:[0,0] op_sel_hi:[1,0]\nv_pk_mul_f32 v[166:167], v[166:167], v[186:187] op_sel:[0,0] op_sel_hi:[1,0]\n"
"v_pk_fma_f32 v[160:161], v[168:169], v[186:187], v[160:161] op_sel:[0,1,0] op_sel_hi:[1,1,1]\nv_pk_fma_f32 v[162:163], v[170:171], v[186:187], v[162:163] op_sel:[0,1,0] op_sel_hi:[1,1,1]\nv_pk_fma_f32 v[164:165], v[172:173], v[186:187], v[164:165] op_sel:[0,1,0] op_sel_hi:[1,1,1]\nv_pk_fma_f32 v[166:167], v[174:175], v[186:187], v[166:167] op_sel:[0,1,0] op_sel_hi:[1,1,1]\nlc 68,21,67,1024\ncv 160,160,161\ncv 161,162,163\ncv 162,164,165\n"
"cv 163,166,167\nbuffer_store_dwordx4 v[160:163], v21, s[56:59], s86 offen offset:3072 sc1\nmvva 76,40,104\ndt 32,6,40960\ndt 34,6,51200\nmvva 76,44,112\ndt 36,6,58368\ndt 38,12,3072\n"
"mvva 72,44,108\nmvva 72,40,100\nwl 8\nmvva 80,48,100\ndt 40,7,40960\ndt 42,7,51200\nmvva 80,52,108\ndt 44,7,58368\n"
"dt 46,13,3072\nmvva 84,52,112\ns_add_u32 s66, s28, 77824\nmvva 84,48,104\nwl 8\nlc 72,21,66,0\nmvva 92,56,104\ndt 48,8,40960\n"
"dt 50,8,51200\nmvva 92,60,112\ndt 52,8,58368\ndt 54,14,3072\nmvva 88,60,108\nmvva 88,56,100\nwl 8\nlc 76,21,66,1024\n"
"s_barrier\nmvva 96,32,100\ns_mov_b32 s64, 983040\ndt 56,9,40960\ndt 58,9,51200\nmvva 96,36,108\nsm 82944\ndt 60,9,58368\n"
"dt 62,15,3072\nmvva 100,36,112\nlb 19,4,64,0\nmvva 100,32,104\nwl 8\nmvva 108,40,104\ns_mov_b32 s65, 1245184\ndt 32,6,59392\n"
"dt 34,12,4096\nmvva 108,44,112\nsm 83968\ndt 36,6,61440\ndt 38,12,6144\nmvva 104,44,108\nlb 19,4,65,0\nmvva 104,40,100\n"
"wl 8\nmvva 112,48,100\ns_mov_b32 s64, 1507328\ndt 40,7,59392\ndt 42,13,4096\nmvva 112,52,108\nsm 84992\ndt 44,7,61440\n"
"dt 46,13,6144\nmvva 116,52,112\nlb 19,4,64,0\nmvva 116,48,104\nwl 8\nmvva 124,56,104\ndt 48,8,59392\ndt 50,14,4096\n"
"mvva 124,60,112\ndt 52,8,61440\ndt 54,14,6144\nmvva 120,60,108\ns_add_u32 s67, s28, 94208\nmvva 120,56,100\nwl 8\nlc 80,21,67,0\n"
"mvva 64,32,116\ndt 56,9,59392\ndt 58,15,4096\nmvva 64,36,124\ndt 60,9,61440\ndt 62,15,6144\nmvva 68,36,128\nmvva 68,32,120\n"
"wl 8\nlc 84,21,67,1024\nmvva 76,40,120\ns_mov_b32 s65, 1769472\ndt 32,6,60416\ndt 34,12,5120\nmvva 76,44,128\nsm 86016\n"
"dt 36,12,12288\ndt 38,12,22528\nmvva 72,44,124\nlb 19,4,65,0\nmvva 72,40,116\nwl 8\nmvva 80,48,116\ns_mov_b32 s64, 2031616\n"
"dt 40,7,60416\ndt 42,13,5120\nmvva 80,52,124\nsm 102400\ndt 44,13,12288\ndt 46,13,22528\nmvva 84,52,128\nlb 19,4,64,0\n"
"mvva 84,48,120\nwl 8\nmvva 92,56,120\ns_mov_b32 s65, 2293760\ndt 48,8,60416\ndt 50,14,5120\nmvva 92,60,128\nsm 103424\n"
"dt 52,14,12288\ndt 54,14,22528\nmvva 88,60,124\nlb 19,4,65,0\nmvva 88,56,116\nwl 8\nmvva 96,32,116\ns_mov_b32 s64, 2555904\n"
"dt 56,9,60416\ndt 58,15,5120\nmvva 96,36,124\nsm 104448\ndt 60,15,12288\ndt 62,15,22528\nmvva 100,36,128\nlb 19,4,64,0\n"
"mvva 100,32,120\nwl 8\nmvva 108,40,120\ns_mov_b32 s65, 720896\ndt 32,12,13312\ndt 34,12,23552\nmvva 108,44,128\nsm 105472\n"
"dt 36,12,15360\ndt 38,12,25600\nmvva 104,44,124\nlb 18,4,65,0\nmvva 104,40,116\nwl 8\nmvva 112,48,116\ndt 40,13,13312\n"
"dt 42,13,23552\nmvva 112,52,124\ndt 44,13,15360\ndt 46,13,25600\nmvva 116,52,128\ns_add_u32 s66, s28, 110592\nmvva 116,48,120\nwb 12,8\n"
"lc 88,21,66,0\ns_barrier\nv_mov_b32 v22, s34\nbuffer_store_dword v22, v30, s[56:59], s76 offen offset:128 sc1\nmvva 124,56,120\ns_mov_b32 s64, 983040\ndt 48,14,13312\ndt 50,14,23552\n"
"mvva 124,60,128\nsm 106496\ndt 52,14,15360\ndt 54,14,25600\nmvva 120,60,124\nlb 18,4,64,0\nmvva 120,56,116\nwl 8\n"
"mvva 64,32,132\ns_mov_b32 s65, 1245184\ndt 56,15,13312\ndt 58,15,23552\nmvva 64,36,140\nsm 122880\ndt 60,15,15360\ndt 62,15,25600\n"
"mvva 68,36,144\nlb 18,4,65,0\nmvva 68,32,136\nwl 8\nmvva 76,40,136\ns_mov_b32 s64, 1507328\ndt 32,12,14336\ndt 34,12,24576\n"
"mvva 76,44,144\nsm 123904\ndt 36,12,16384\ndt 38,12,26624\nmvva 72,44,140\nlb 18,4,64,0\nmvva 72,40,132\nwl 8\n"
"mvva 80,48,132\ndt 40,13,14336\ndt 42,13,24576\nmvva 80,52,140\ndt 44,13,16384\ndt 46,13,26624\nmvva 84,52,144\nmvva 84,48,136\n"
"wl 8\nlc 92,21,66,1024\nmvva 92,56,136\ndt 48,14,14336\ndt 50,14,24576\nmvva 92,60,144\ndt 52,14,16384\ndt 54,14,26624\n"
"mvva 88,60,140\ns_add_u32 s67, s28, 126976\nmvva 88,56,132\nwl 8\nlc 96,21,67,0\ns_barrier\nmvva 96,32,132\ndt 56,15,14336\n"
"dt 58,15,24576\nmvva 96,36,140\ndt 60,15,16384\ndt 62,15,26624\nmvva 100,36,144\nmvva 100,32,136\nwl 8\nmvva 108,40,136\n"
"dt 32,0,0\ndt 34,0,10240\nmvva 108,44,144\ndt 36,0,2048\ndt 38,0,12288\nmvva 104,44,140\nmvva 104,40,132\nwl 8\n"
"lc 100,21,67,1024\nmvva 112,48,132\ndt 40,1,0\ndt 42,1,10240\nmvva 112,52,140\ndt 44,1,2048\ndt 46,1,12288\nmvva 116,52,144\n"
"s_add_u32 s66, s28, 143360\nmvva 116,48,136\nwl 8\nlc 104,21,66,0\nmvva 124,56,136\ndt 48,2,0\ndt 50,2,10240\nmvva 124,60,144\n"
"dt 52,2,2048\ndt 54,2,12288\nmvva 120,60,140\nmvva 120,56,132\nwl 8\nlc 108,21,66,1024\nmvva 64,32,148\ndt 56,3,0\n"
"dt 58,3,10240\nmvva 64,36,156\ndt 60,3,2048\ndt 62,3,12288\nmvva 68,36,160\ns_add_u32 s67, s28, 159744\nmvva 68,32,152\nwl 8\n"
"lc 112,21,67,0\nmvva 76,40,152\ndt 32,0,1024\ndt 34,0,11264\nmvva 76,44,160\ndt 36,0,3072\ndt 38,0,13312\nmvva 72,44,156\n"
"mvva 72,40,148\nwl 8\nlc 116,21,67,1024\nmvva 80,48,148\ns_mov_b32 s65, 1769472\ndt 40,1,1024\ndt 42,1,11264\nmvva 80,52,156\n"
"sm 124928\ndt 44,1,3072\ndt 46,1,13312\nmvva 84,52,160\nlb 18,4,65,0\nmvva 84,48,152\nwl 8\nmvva 92,56,152\n"
"s_mov_b32 s64, 2031616\ndt 48,2,1024\ndt 50,2,11264\nmvva 92,60,160\nsm 125952\ndt 52,2,3072\ndt 54,2,13312\nmvva 88,60,156\n"
"lb 18,4,64,0\nmvva 88,56,148\nwl 8\nmvva 96,32,148\ns_mov_b32 s65, 2293760\ndt 56,3,1024\ndt 58,3,11264\nmvva 96,36,156\n"
"sm 126976\ndt 60,3,3072\ndt 62,3,13312\nmvva 100,36,160\nlb 18,4,65,0\nmvva 100,32,152\nwl 8\nmvva 108,40,152\n"
"s_mov_b32 s64, 2555904\ndt 32,0,4096\ndt 34,0,14336\nmvva 108,44,160\nsm 143360\ndt 36,0,21504\ndt 38,0,31744\nmvva 104,44,156\n"
"lb 18,4,64,0\nmvva 104,40,148\nwl 8\nmvva 112,48,148\ns_mov_b32 s65, 0\ndt 40,1,4096\ndt 42,1,14336\nmvva 112,52,156\n"
"sm 144384\ndt 44,1,21504\ndt 46,1,31744\nmvva 116,52,160\nlb 18,8,65,0\nmvva 116,48,152\nwl 8\nmvva 124,56,152\n"
"dt 48,2,4096\ndt 50,2,14336\nmvva 124,60,160\ndt 52,2,21504\ndt 54,2,31744\nmvva 120,60,156\nmvva 120,56,148\nwl 8\n"
"mvva 64,32,164\ndt 56,3,4096\ndt 58,3,14336\nmvva 64,36,0\ndt 60,3,21504\ndt 62,3,31744\nmvva 68,36,4\nmvva 68,32,168\n"
"wl 8\nmvva 76,40,168\ndt 32,0,20480\ndt 34,0,30720\nmvva 76,44,4\ndt 36,0,22528\ndt 38,0,32768\nmvva 72,44,0\n"
"mvva 72,40,164\nwl 8\nmvva 80,48,164\ndt 40,1,20480\ndt 42,1,30720\nmvva 80,52,0\ndt 44,1,22528\ndt 46,1,32768\n"
"mvva 84,52,4\nmvva 84,48,168\nwl 8\nmvva 92,56,168\ndt 48,2,20480\ndt 50,2,30720\nmvva 92,60,4\ndt 52,2,22528\n"
"dt 54,2,32768\nmvva 88,60,0\nmvva 88,56,164\nwl 8\ns_barrier\nmvva 96,32,164\ns_mov_b32 s64, 65536\ndt 56,3,20480\n"
"dt 58,3,30720\nmvva 96,36,0\nsm 145408\ndt 60,3,22528\ndt 62,3,32768\nmvva 100,36,4\ns_mov_b32 s65, 131072\nlb 18,8,64,0\n"
"sm 146432\nmvva 100,32,168\nwl 8\nlb 18,8,65,0\nmvva 108,40,168\ndt 32,0,23552\ndt 34,0,33792\nmvva 108,44,4\n"
"dt 36,0,40960\ndt 38,0,51200\nmvva 104,44,0\nmvva 104,40,164\nwl 8\nmvva 112,48,164\ns_mov_b32 s64, 196608\ndt 40,1,23552\n"
"dt 42,1,33792\nmvva 112,52,0\nsm 147456\ndt 44,1,40960\ndt 46,1,51200\nmvva 116,52,4\nlb 18,8,64,0\nmvva 116,48,168\n"
"wl 8\nv_lshrrev_b32 v20, 2, v23\nv_and_b32 v20, 60, v20\nbuffer_load_dword v31, v20, s[56:59], s77 offen offset:128 sc1\nmvva 124,56,168\ns_mov_b32 s65, 262144\ndt 48,2,23552\ndt 50,2,33792\n"
"mvva 124,60,4\nsm 0\ndt 52,2,40960\ndt 54,2,51200\nmvva 120,60,0\nlb 18,8,65,0\nmvva 120,56,164\nwl 8\n"
"mvva 64,32,8\ns_mov_b32 s64, 327680\ndt 56,3,23552\ndt 58,3,33792\nmvva 64,36,16\nsm 1024\ndt 60,3,40960\ndt 62,3,51200\n"
"mvva 68,36,20\nlb 18,8,64,0\nmvva 68,32,12\nwl 8\nmvva 76,40,12\ns_mov_b32 s65, 393216\ndt 32,0,24576\ndt 34,0,34816\n"
"mvva 76,44,20\nsm 2048\ndt 36,0,41984\ndt 38,0,52224\nmvva 72,44,16\nlb 18,8,65,0\nmvva 72,40,8\nwl 8\n"
"mvva 80,48,8\ns_mov_b32 s64, 458752\ndt 40,1,24576\ndt 42,1,34816\nmvva 80,52,16\nsm 3072\ndt 44,1,41984\ndt 46,1,52224\n"
"mvva 84,52,20\nlb 18,8,64,0\nmvva 84,48,12\nwl 8\nmvva 92,56,12\ns_mov_b32 s65, 524288\ndt 48,2,24576\ndt 50,2,34816\n"
"mvva 92,60,20\nsm 4096\ndt 52,2,41984\ndt 54,2,52224\nmvva 88,60,16\nlb 18,8,65,0\nmvva 88,56,8\nwl 8\n"
"mvva 96,32,8\ndt 56,3,24576\ndt 58,3,34816\nmvva 96,36,16\ndt 60,3,41984\ndt 62,3,52224\nmvva 100,36,20\nmvva 100,32,12\n"
"wl 8\nmvva 108,40,12\ndt 32,0,43008\ndt 34,0,53248\nmvva 108,44,20\ndt 36,0,45056\ndt 38,0,55296\nmvva 104,44,16\n"
"mvva 104,40,8\nwl 8\nmvva 112,48,8\ndt 40,1,43008\ndt 42,1,53248\nmvva 112,52,16\ndt 44,1,45056\ndt 46,1,55296\n"
"mvva 116,52,20\nmvva 116,48,12\nwl 8\nmvva 124,56,12\ndt 48,2,43008\ndt 50,2,53248\nmvva 124,60,20\ndt 52,2,45056\n"
"dt 54,2,55296\nmvva 120,60,16\nmvva 120,56,8\nwl 8\nmvva 64,32,24\ndt 56,3,43008\ndt 58,3,53248\nmvva 64,36,32\n"
"dt 60,3,45056\ndt 62,3,55296\nmvva 68,36,36\nmvva 68,32,28\nwl 8\nmvva 76,40,28\ndt 32,0,44032\ndt 34,0,54272\n"
"mvva 76,44,36\ndt 36,0,61440\ndt 38,6,6144\nmvva 72,44,32\nmvva 72,40,24\nwl 8\nmvva 80,48,24\ndt 40,1,44032\n"
"dt 42,1,54272\nmvva 80,52,32\ndt 44,1,61440\ndt 46,7,6144\nmvva 84,52,36\nmvva 84,48,28\nwl 8\nmvva 92,56,28\n"
"dt 48,2,44032\ndt 50,2,54272\nmvva 92,60,36\ndt 52,2,61440\ndt 54,8,6144\nmvva 88,60,32\nmvva 88,56,24\nwb 33,8\n"
"s_barrier\nmvva 96,32,24\ns_mov_b32 s64, 589824\ndt 56,3,44032\ndt 58,3,54272\nmvva 96,36,32\nsm 20480\ndt 60,3,61440\n"
"dt 62,9,6144\nmvva 100,36,36\ns_mov_b32 s65, 655360\nlb 18,8,64,0\nsm 21504\nmvva 100,32,28\nwl 8\nlb 18,8,65,0\n"
"s_waitcnt vmcnt(7)\nv_cmp_ne_u32 vcc, s34, v31\ns_cbranch_vccz 57f\ns_mov_b32 s69, 0x80000\n56:\ns_sleep 1\nbuffer_load_dword v31, v20, s[56:59], s77 offen offset:128 sc1\ns_waitcnt vmcnt(0)\n"
"v_cmp_ne_u32 vcc, s34, v31\ns_cbranch_vccz 57f\ns_sub_u32 s69, s69, 1\ns_cbranch_scc0 56b\n57:\ns_mov_b32 s69, 0x70000000\ns_cmp_eq_u32 s79, 4333568\ns_cmov_b32 s69, 4386816\n"
"v_mov_b32 v22, s34\nbuffer_store_dword v22, v30, s[56:59], s69 offen offset:0 sc1\nmvva 108,40,28\ndt 32,0,62464\ndt 34,6,7168\nmvva 108,44,36\ndt 36,0,64512\ndt 38,6,9216\n"
"mvva 104,44,32\ns_add_u32 s66, s29, 0\nmvva 104,40,24\nwl 8\nlc 120,21,66,0\nmvva 112,48,24\ndt 40,1,62464\ndt 42,7,7168\n"
"mvva 112,52,32\ndt 44,1,64512\ndt 46,7,9216\nmvva 116,52,36\nmvva 116,48,28\nwl 8\nlc 124,21,66,1024\nmvva 124,56,28\n"
"dt 48,2,62464\ndt 50,8,7168\nmvva 124,60,36\ndt 52,2,64512\ndt 54,8,9216\nmvva 120,60,32\ns_add_u32 s67, s29, 4096\nmvva 120,56,24\n"
"wl 8\nlc 128,21,67,0\nmvva 64,32,40\ndt 56,3,62464\ndt 58,9,7168\nmvva 64,36,48\ndt 60,3,64512\ndt 62,9,9216\n"
"mvva 68,36,52\nmvva 68,32,44\nwl 8\nlc 132,21,67,1024\nar 192,172\nmvva 76,40,44\ndt 32,0,63488\ndt 34,6,8192\n"
"mvva 76,44,52\ndt 36,6,0\ndt 38,6,10240\nmvva 72,44,48\ns_add_u32 s66, s29, 8192\nmvva 72,40,40\nwl 8\nlc 136,21,66,0\n"
"ar 193,173\nmvva 80,48,40\ndt 40,1,63488\ndt 42,7,8192\nmvva 80,52,48\ndt 44,7,0\ndt 46,7,10240\nmvva 84,52,52\n"
"mvva 84,48,44\nwl 8\nlc 140,21,66,1024\nar 194,174\nmvva 92,56,44\ndt 48,2,63488\ndt 50,8,8192\nmvva 92,60,52\n"
"s_add_u32 s67, s29, 12288\ndt 52,8,0\ndt 54,8,10240\nmvva 88,60,48\nlc 144,21,67,0\nmvva 88,56,40\nwl 8\nlc 148,21,67,1024\n"
"ar 195,175\nmvva 96,32,40\ns_mov_b32 s64, 720896\ndt 56,3,63488\ndt 58,9,8192\nmvva 96,36,48\nsm 22528\ndt 60,9,0\n"
"dt 62,9,10240\nmvva 100,36,52\nlb 18,8,64,0\nmvva 100,32,44\nwl 8\nar 196,244\nmvva 108,40,44\ndt 32,6,16384\n"
"dt 34,6,26624\nmvva 108,44,52\ndt 36,6,17408\ndt 38,6,27648\nmvva 104,44,48\ns_add_u32 s66, s29, 16384\nmvva 104,40,40\nwl 8\n"
"lc 152,21,66,0\nar 197,245\nmvva 112,48,40\ndt 40,7,16384\ndt 42,7,26624\nmvva 112,52,48\ndt 44,7,17408\ndt 46,7,27648\n"
"mvva 116,52,52\nmvva 116,48,44\nwl 8\nlc 156,21,66,1024\nar 198,246\nmvva 124,56,44\ndt 48,8,16384\ndt 50,8,26624\n"
"mvva 124,60,52\ndt 52,8,17408\ndt 54,8,27648\nmvva 120,60,48\ns_add_u32 s67, s29, 20480\nmvva 120,56,40\nwl 8\nlc 160,21,67,0\n"
"ar 199,247\nmvva 96,32,56\ndt 56,9,16384\ndt 58,9,26624\nmvva 96,36,64\ndt 60,9,17408\ndt 62,9,27648\nmvva 100,36,68\n"
"mvva 100,32,60\nwl 8\nlc 164,21,67,1024\nar 200,248\nmvva 108,40,60\ndt 32,6,18432\ndt 34,6,28672\nmvva 108,44,68\n"
"dt 36,6,19456\ndt 38,6,29696\nmvva 104,44,64\ns_add_u32 s66, s29, 24576\nmvva 104,40,56\nwl 8\nlc 168,21,66,0\nar 201,249\n"
"mvva 112,48,56\ndt 40,7,18432\ndt 42,7,28672\nmvva 112,52,64\ndt 44,7,19456\ndt 46,7,29696\nmvva 116,52,68\ns_add_u32 s67, s29, 28672\n"
"lc 0,21,66,1024\nmvva 116,48,60\nwl 8\nlc 4,21,67,0\nar 202,250\nmvva 124,56,60\ndt 48,8,18432\ndt 50,8,28672\n"
"mvva 124,60,68\ndt 52,8,19456\ndt 54,8,29696\nmvva 120,60,64\nmvva 120,56,56\nwl 8\nlc 8,21,67,1024\ns_barrier\n"
"ar 203,251\nmvva 96,32,72\ndt 56,9,18432\ndt 58,9,28672\nmvva 96,36,80\ndt 60,9,19456\ndt 62,9,29696\nmvva 100,36,84\n"
"s_add_u32 s66, s29, 32768\nmvva 100,32,76\nwl 8\nlc 12,21,66,0\nar 204,252\nmvva 108,40,76\ndt 32,6,20480\ndt 34,6,30720\n"
"mvva 108,44,84\ndt 36,6,36864\ndt 38,6,47104\nmvva 104,44,80\nmvva 104,40,72\nwl 8\nlc 16,21,66,1024\nar 205,253\n"
"mvva 112,48,72\ndt 40,7,20480\ndt 42,7,30720\nmvva 112,52,80\ndt 44,7,36864\ndt 46,7,47104\nmvva 116,52,84\ns_add_u32 s67, s29, 36864\n"
"mvva 116,48,76\nwl 8\nlc 20,21,67,0\nar 206,254\nmvva 124,56,76\ndt 48,8,20480\ndt 50,8,30720\nmvva 124,60,84\n"
"dt 52,8,36864\ndt 54,8,47104\nmvva 120,60,80\nmvva 120,56,72\nwl 8\nlc 24,21,67,1024\nar 207,255\nmvva 96,32,88\n"
"dt 56,9,20480\ndt 58,9,30720\nmvva 96,36,96\ndt 60,9,36864\ndt 62,9,47104\nmvva 100,36,100\ns_add_u32 s66, s29, 40960\nmvva 100,32,92\n"
"wl 8\nlc 28,21,66,0\nmvva 108,40,92\ndt 32,6,37888\ndt 34,6,48128\nmvva 108,44,100\ndt 36,6,38912\ndt 38,6,49152\n"
"mvva 104,44,96\ns_add_u32 s67, s29, 45056\nlc 32,21,66,1024\nmvva 104,40,88\nwl 8\nlc 36,21,67,0\nmvva 112,48,88\ndt 40,7,37888\n"
"dt 42,7,48128\nmvva 112,52,96\ndt 44,7,38912\ndt 46,7,49152\nmvva 116,52,100\nmvva 116,48,92\nwl 8\nlc 40,21,67,1024\n"
"mvva 124,56,92\ns_mov_b32 s65, 786432\ndt 48,8,37888\ndt 50,8,48128\nmvva 124,60,100\nsm 23552\ndt 52,8,38912\ndt 54,8,49152\n"
"mvva 120,60,96\nlb 18,8,65,0\nmvva 120,56,88\nwl 8\nmvva 96,32,104\ns_mov_b32 s64, 851968\ndt 56,9,37888\ndt 58,9,48128\n"
"mvva 96,36,112\nsm 24576\ndt 60,9,38912\ndt 62,9,49152\nmvva 100,36,116\nlb 18,8,64,0\nmvva 100,32,108\nwl 8\n"
"mvva 108,40,108\ns_mov_b32 s65, 917504\ndt 32,6,39936\ndt 34,6,50176\nmvva 108,44,116\nsm 40960\ndt 36,6,40960\ndt 38,6,51200\n"
"mvva 104,44,112\nlb 18,8,65,0\nmvva 104,40,104\nwl 8\nmvva 112,48,104\ns_mov_b32 s64, 983040\ndt 40,7,39936\ndt 42,7,50176\n"
"mvva 112,52,112\nsm 41984\ndt 44,7,40960\ndt 46,7,51200\nmvva 116,52,116\nlb 18,8,64,0\nmvva 116,48,108\nwl 8\n"
"mvva 124,56,108\ns_mov_b32 s65, 1048576\ndt 48,8,39936\ndt 50,8,50176\nmvva 124,60,116\nsm 43008\ndt 52,8,40960\ndt 54,8,51200\n"
"mvva 120,60,112\nlb 18,8,65,0\nmvva 120,56,104\nwl 8\nmvva 64,32,56\ns_mov_b32 s64, 1114112\ndt 56,9,39936\ndt 58,9,50176\n"
"mvva 64,36,64\nsm 44032\ndt 60,9,40960\ndt 62,9,51200\nmvva 68,36,68\ns_mov_b32 s65, 1179648\nlb 18,8,64,0\nsm 45056\n"
"mvva 68,32,60\nwl 8\nlb 18,8,65,0\nmvva 76,40,60\ndt 32,6,57344\ndt 34,12,2048\nmvva 76,44,68\ndt 36,6,58368\n"
"dt 38,12,3072\nmvva 72,44,64\nbuffer_store_dwordx4 v[96:99], v21, s[56:59], s30 offen offset:0 sc1\nmvva 72,40,56\nwl 8\nbuffer_store_dwordx4 v[100:103], v21, s[56:59], s30 offen offset:1024 sc1\nmvva 80,48,56\ndt 40,7,57344\n"
"dt 42,13,2048\nmvva 80,52,64\ndt 44,7,58368\ndt 46,13,3072\nmvva 84,52,68\nbuffer_store_dwordx4 v[104:107], v21, s[56:59], s30 offen offset:2048 sc1\nmvva 84,48,60\nwl 8\n"
"buffer_store_dwordx4 v[108:111], v21, s[56:59], s30 offen offset:3072 sc1\nmvva 92,56,60\ndt 48,8,57344\ndt 50,14,2048\nmvva 92,60,68\ns_add_u32 s30, s30, 4096\ndt 52,8,58368\ndt 54,14,3072\n"
"mvva 88,60,64\nbuffer_store_dwordx4 v[112:115], v21, s[56:59], s30 offen offset:0 sc1\nmvva 88,56,56\nwl 8\nbuffer_store_dwordx4 v[116:119], v21, s[56:59], s30 offen offset:1024 sc1\ns_barrier\nmvva 64,32,72\ndt 56,9,57344\n"
"dt 58,15,2048\nmvva 64,36,80\ndt 60,9,58368\ndt 62,15,3072\nmvva 68,36,84\nbuffer_store_dwordx4 v[120:123], v21, s[56:59], s30 offen offset:2048 sc1\nmvva 68,32,76\nwl 8\n"
"buffer_store_dwordx4 v[124:127], v21, s[56:59], s30 offen offset:3072 sc1\nmvva 76,40,76\ns_mov_b32 s64, 1245184\ndt 32,6,59392\ndt 34,12,4096\nmvva 76,44,84\nsm 61440\ndt 36,6,60416\n"
"dt 38,12,5120\nmvva 72,44,80\nlb 18,8,64,0\nmvva 72,40,72\nwl 8\nmvva 80,48,72\ndt 40,7,59392\ndt 42,13,4096\n"
"mvva 80,52,80\ndt 44,7,60416\ndt 46,13,5120\nmvva 84,52,84\ns_add_u32 s66, s29, 49152\nmvva 84,48,76\nwl 8\nlc 44,21,66,0\n"
"mvva 92,56,76\ndt 48,8,59392\ndt 50,14,4096\nmvva 92,60,84\ndt 52,8,60416\ndt 54,14,5120\nmvva 88,60,80\ns_add_u32 s67, s29, 53248\n"
"lc 48,21,66,1024\nmvva 88,56,72\nwl 8\nlc 52,21,67,0\nmvva 64,32,88\ndt 56,9,59392\ndt 58,15,4096\nmvva 64,36,96\n"
"dt 60,9,60416\ndt 62,15,5120\nmvva 68,36,100\nmvva 68,32,92\nwl 8\nlc 56,21,67,1024\nmvva 76,40,92\ndt 32,6,61440\n"
"dt 34,12,6144\nmvva 76,44,100\ndt 36,12,12288\ndt 38,12,22528\nmvva 72,44,96\ns_add_u32 s66, s29, 57344\nmvva 72,40,88\nwl 8\n"
"lc 60,21,66,0\nmvva 80,48,88\ndt 40,7,61440\ndt 42,13,6144\nmvva 80,52,96\ndt 44,13,12288\ndt 46,13,22528\nmvva 84,52,100\n"
"mvva 84,48,92\nwl 8\nlc 64,21,66,1024\nmvva 92,56,92\ndt 48,8,61440\ndt 50,14,6144\nmvva 92,60,100\ndt 52,14,12288\n"
"dt 54,14,22528\nmvva 88,60,96\ns_add_u32 s67, s29, 61440\nmvva 88,56,88\nwl 8\nlc 68,21,67,0\nmvva 64,32,104\ndt 56,9,61440\n"
"dt 58,15,6144\nmvva 64,36,112\ndt 60,15,12288\ndt 62,15,22528\nmvva 68,36,116\nmvva 68,32,108\nwl 8\nlc 72,21,67,1024\n"
"mvva 76,40,108\ndt 32,12,13312\ndt 34,12,23552\nmvva 76,44,116\ndt 36,12,14336\ndt 38,12,24576\nmvva 72,44,112\ns_add_u32 s66, s29, 65536\n"
"mvva 72,40,104\nwl 8\nlc 76,21,66,0\nmvva 80,48,104\ndt 40,13,13312\ndt 42,13,23552\nmvva 80,52,112\ndt 44,13,14336\n"
"dt 46,13,24576\nmvva 84,52,116\ns_add_u32 s67, s29, 69632\nlc 80,21,66,1024\nmvva 84,48,108\nwl 8\nlc 84,21,67,0\nmvva 92,56,108\n"
"dt 48,14,13312\ndt 50,14,23552\nmvva 92,60,116\ndt 52,14,14336\ndt 54,14,24576\nmvva 88,60,112\nmvva 88,56,104\nwb 48,8\n"
"lc 88,21,67,1024\nzvva 128,32,120\ndt 56,15,13312\ndt 58,15,23552\nmvva 128,36,128\ndt 60,15,14336\ndt 62,15,24576\nzvva 132,36,132\n"
"s_add_u32 s66, s29, 73728\nmvva 132,32,124\nwl 8\nlc 92,21,66,0\ns_add_u32 s95, s32, 32768\nzvva 140,40,124\ndt 32,12,15360\ndt 34,12,25600\n"
"mvva 140,44,132\ndt 36,12,16384\ndt 38,12,26624\nzvva 136,44,128\nv_permlane16_swap_b32 v192, v194\nmvva 136,40,120\nwl 8\nlc 96,21,66,1024\n"
"v_permlane16_swap_b32 v193, v195\nzvva 144,48,120\ndt 40,13,15360\ndt 42,13,25600\nmvva 144,52,128\ndt 44,13,16384\ndt 46,13,26624\nzvva 148,52,132\n"
"s_add_u32 s67, s29, 77824\nv_permlane16_swap_b32 v196, v198\nmvva 148,48,124\nwl 8\nlc 100,21,67,0\nv_permlane16_swap_b32 v197, v199\nzvva 156,56,124\ndt 48,14,15360\n"
"dt 50,14,25600\nmvva 156,60,132\ndt 52,14,16384\ndt 54,14,26624\nzvva 152,60,128\nv_permlane16_swap_b32 v200, v202\nmvva 152,56,120\nwb 16,8\n"
"lc 104,21,67,1024\nv_permlane16_swap_b32 v201, v203\ns_barrier\nv_mov_b32 v22, s34\nbuffer_store_dword v22, v30, s[56:59], s79 offen offset:0 sc1\nmvva 128,32,136\ns_mov_b32 s65, 1310720\ndt 56,15,15360\n"
"dt 58,15,25600\nmvva 128,36,144\nsm 62464\ndt 60,15,16384\ndt 62,15,26624\nmvva 132,36,148\nlb 18,8,65,0\nv_permlane16_swap_b32 v204, v206\n"
"mvva 132,32,140\nwl 8\nv_permlane16_swap_b32 v205, v207\nmvva 140,40,140\ns_mov_b32 s64, 1376256\ndt 32,0,0\ndt 34,0,10240\nmvva 140,44,148\n"
"sm 63488\ndt 36,0,1024\ndt 38,0,11264\nmvva 136,44,144\ns_mov_b32 s65, 1441792\nlb 18,8,64,0\nsm 64512\nv_lshlrev_b32 v208, 16, v192\n"
"mvva 136,40,136\nwl 8\nlb 18,8,65,0\nv_and_b32 v209, 0xffff0000, v192\nmvva 144,48,136\ns_mov_b32 s64, 1507328\ndt 40,1,0\ndt 42,1,10240\n"
"mvva 144,52,144\nsm 65536\ndt 44,1,1024\ndt 46,1,11264\nmvva 148,52,148\nlb 18,8,64,0\nv_lshlrev_b32 v210, 16, v193\nmvva 148,48,140\n"
"wl 8\nv_and_b32 v211, 0xffff0000, v193\nmvva 156,56,140\ns_mov_b32 s65, 1572864\ndt 48,2,0\ndt 50,2,10240\nmvva 156,60,148\nsm 81920\n"
"dt 52,2,1024\ndt 54,2,11264\nmvva 152,60,144\nlb 18,8,65,0\nv_lshlrev_b32 v212, 16, v194\nmvva 152,56,136\nwl 8\nv_and_b32 v213, 0xffff0000, v194\n"
"mvva 128,32,152\ns_mov_b32 s64, 1638400\ndt 56,3,0\ndt 58,3,10240\nmvva 128,36,160\nsm 82944\ndt 60,3,1024\ndt 62,3,11264\n"
"mvva 132,36,164\nlb 18,8,64,0\nv_lshlrev_b32 v214, 16, v195\nmvva 132,32,156\nwl 8\nv_and_b32 v215, 0xffff0000, v195\nmvva 140,40,156\ns_mov_b32 s65, 1703936\n"
"dt 32,0,2048\ndt 34,0,12288\nmvva 140,44,164\nsm 83968\ndt 36,0,3072\ndt 38,0,13312\nmvva 136,44,160\nlb 18,8,65,0\n"
"v_lshlrev_b32 v216, 16, v196\nmvva 136,40,152\nwl 8\nv_and_b32 v217, 0xffff0000, v196\nmvva 144,48,152\ns_mov_b32 s64, 1769472\ndt 40,1,2048\ndt 42,1,12288\n"
"mvva 144,52,160\nsm 84992\ndt 44,1,3072\ndt 46,1,13312\nmvva 148,52,164\nlb 18,8,64,0\nv_lshlrev_b32 v218, 16, v197\nmvva 148,48,156\n"
"wl 8\nv_and_b32 v219, 0xffff0000, v197\nmvva 156,56,156\ndt 48,2,2048\ndt 50,2,12288\nmvva 156,60,164\ndt 52,2,3072\ndt 54,2,13312\n"
"mvva 152,60,160\ns_add_u32 s66, s29, 81920\nv_lshlrev_b32 v220, 16, v198\nmvva 152,56,152\nwl 8\nlc 108,21,66,0\nv_and_b32 v221, 0xffff0000, v198\nmvva 128,32,168\n"
"dt 56,3,2048\ndt 58,3,12288\nmvva 128,36,4\ndt 60,3,3072\ndt 62,3,13312\nmvva 132,36,8\ns_add_u32 s67, s29, 86016\nlc 112,21,66,1024\n"
"v_lshlrev_b32 v222, 16, v199\nmvva 132,32,0\nwl 8\nlc 116,21,67,0\nv_and_b32 v223, 0xffff0000, v199\nmvva 140,40,0\ndt 32,0,4096\ndt 34,0,14336\n"
"mvva 140,44,8\ndt 36,0,20480\ndt 38,0,30720\nmvva 136,44,4\nv_lshlrev_b32 v224, 16, v200\nmvva 136,40,168\nwl 8\nlc 120,21,67,1024\n"
"v_and_b32 v225, 0xffff0000, v200\nmvva 144,48,168\ndt 40,1,4096\ndt 42,1,14336\nmvva 144,52,4\ndt 44,1,20480\ndt 46,1,30720\nmvva 148,52,8\n"
"s_add_u32 s66, s29, 90112\nv_lshlrev_b32 v226, 16, v201\nmvva 148,48,0\nwl 8\nlc 124,21,66,0\nv_and_b32 v227, 0xffff0000, v201\nmvva 156,56,0\ndt 48,2,4096\n"
"dt 50,2,14336\nmvva 156,60,8\ndt 52,2,20480\ndt 54,2,30720\nmvva 152,60,4\nv_lshlrev_b32 v228, 16, v202\nmvva 152,56,168\nwl 8\n"
"lc 128,21,66,1024\nv_and_b32 v229, 0xffff0000, v202\nmvva 128,32,12\ndt 56,3,4096\ndt 58,3,14336\nmvva 128,36,20\ndt 60,3,20480\ndt 62,3,30720\n"
"mvva 132,36,24\ns_add_u32 s67, s29, 94208\nv_lshlrev_b32 v230, 16, v203\nmvva 132,32,16\nwl 8\nlc 132,21,67,0\nv_and_b32 v231, 0xffff0000, v203\nmvva 140,40,16\n"
"dt 32,0,21504\ndt 34,0,31744\nmvva 140,44,24\ndt 36,0,22528\ndt 38,0,32768\nmvva 136,44,20\nv_lshlrev_b32 v232, 16, v204\nmvva 136,40,12\n"
"wl 8\nlc 136,21,67,1024\nv_and_b32 v233, 0xffff0000, v204\nmvva 144,48,12\ndt 40,1,21504\ndt 42,1,31744\nmvva 144,52,20\ndt 44,1,22528\n"
"dt 46,1,32768\nmvva 148,52,24\ns_add_u32 s66, s29, 98304\nv_lshlrev_b32 v234, 16, v205\nmvva 148,48,16\nwl 8\nlc 140,21,66,0\nv_and_b32 v235, 0xffff0000, v205\n"
"mvva 156,56,16\ndt 48,2,21504\ndt 50,2,31744\nmvva 156,60,24\ndt 52,2,22528\ndt 54,2,32768\nmvva 152,60,20\ns_add_u32 s67, s29, 102400\n"
"lc 144,21,66,1024\nv_lshlrev_b32 v236, 16, v206\nmvva 152,56,12\nwb 35,8\nlc 148,21,67,0\nv_and_b32 v237, 0xffff0000, v206\ns_barrier\nmvva 128,32,28\n"
"dt 56,3,21504\ndt 58,3,31744\nmvva 128,36,36\ndt 60,3,22528\ndt 62,3,32768\nmvva 132,36,40\nv_lshlrev_b32 v238, 16, v207\nmvva 132,32,32\n"
"wl 8\nlc 152,21,67,1024\nv_and_b32 v239, 0xffff0000, v207\nmvva 140,40,32\ndt 32,0,23552\ndt 34,0,33792\nmvva 140,44,40\ndt 36,0,24576\n"
"dt 38,0,34816\nmvva 136,44,36\ns_add_u32 s66, s29, 106496\nmvva 136,40,28\nwl 8\nlc 156,21,66,0\ns_xor_b32 s79, s79, 512\nbuffer_load_dword v31, off, s[56:59], s79 offset:0 sc1\n"
"mvva 144,48,28\ndt 40,1,23552\ndt 42,1,33792\nmvva 144,52,36\ndt 44,1,24576\ndt 46,1,34816\nmvva 148,52,40\nmvva 148,48,32\n"
"wl 8\nlc 160,21,66,1024\nmvva 156,56,32\ndt 48,2,23552\ndt 50,2,33792\nmvva 156,60,40\ndt 52,2,24576\ndt 54,2,34816\n"
"mvva 152,60,36\ns_add_u32 s67, s29, 110592\nmvva 152,56,28\nwb 36,8\nlc 164,21,67,0\nmvva 128,32,44\ndt 56,3,23552\ndt 58,3,33792\n"
"mvva 128,36,52\ndt 60,3,24576\ndt 62,3,34816\nmvva 132,36,56\nmvva 132,32,48\nwl 8\nlc 168,21,67,1024\nmvva 140,40,48\n"
"s_mov_b32 s65, 1835008\ndt 32,0,40960\ndt 34,0,51200\nmvva 140,44,56\nsm 86016\ndt 36,0,41984\ndt 38,0,52224\nmvva 136,44,52\n"
"lb 18,8,65,0\nmvva 136,40,44\nwl 8\nmvva 144,48,44\ns_mov_b32 s64, 1900544\ndt 40,1,40960\ndt 42,1,51200\nmvva 144,52,52\n"
"sm 102400\ndt 44,1,41984\ndt 46,1,52224\nmvva 148,52,56\ns_mov_b32 s65, 1966080\nlb 18,8,64,0\nsm 103424\nmvva 148,48,48\n"
"wl 8\nlb 18,8,65,0\nmvva 156,56,48\ns_mov_b32 s64, 2031616\ndt 48,2,40960\ndt 50,2,51200\nmvva 156,60,56\nsm 104448\n"
"dt 52,2,41984\ndt 54,2,52224\nmvva 152,60,52\nlb 18,8,64,0\nmvva 152,56,44\nwb 38,8\nmvva 128,32,60\ndt 56,3,40960\n"
"dt 58,3,51200\nmvva 128,36,68\ndt 60,3,41984\ndt 62,3,52224\nmvva 132,36,72\ns_add_u32 s66, s29, 114688\nmvva 132,32,64\nwl 8\n"
"lc 0,21,66,0\nmvva 140,40,64\ndt 32,0,43008\ndt 34,0,53248\nmvva 140,44,72\ndt 36,0,44032\ndt 38,0,54272\nmvva 136,44,68\n"
"mvva 136,40,60\nwl 8\nlc 4,21,66,1024\nmvva 144,48,60\ndt 40,1,43008\ndt 42,1,53248\nmvva 144,52,68\ndt 44,1,44032\n"
"dt 46,1,54272\nmvva 148,52,72\ns_add_u32 s67, s29, 118784\nmvva 148,48,64\nwl 8\nlc 8,21,67,0\nmvva 156,56,64\ndt 48,2,43008\n"
"dt 50,2,53248\nmvva 156,60,72\ndt 52,2,44032\ndt 54,2,54272\nmvva 152,60,68\nmvva 152,56,60\nwb 37,8\nlc 12,21,67,1024\n"
"mvva 128,32,76\ndt 56,3,43008\ndt 58,3,53248\nmvva 128,36,84\ndt 60,3,44032\ndt 62,3,54272\nmvva 132,36,88\ns_add_u32 s66, s29, 122880\n"
"mvva 132,32,80\nwl 8\nlc 16,21,66,0\nmvva 140,40,80\ndt 32,0,45056\ndt 34,0,55296\nmvva 140,44,88\ndt 36,0,61440\n"
"dt 38,6,6144\nmvva 136,44,84\ns_add_u32 s67, s29, 126976\nlc 20,21,66,1024\nmvva 136,40,76\nwl 8\nlc 24,21,67,0\ns_waitcnt vmcnt(14)\n"
"v_cmp_ne_u32 vcc, s34, v31\ns_cbranch_vccz 59f\ns_mov_b32 s69, 0x80000\n58:\ns_sleep 1\nbuffer_load_dword v31, off, s[56:59], s79 offset:0 sc1\ns_waitcnt vmcnt(0)\nv_cmp_ne_u32 vcc, s34, v31\n"
"s_cbranch_vccz 59f\ns_sub_u32 s69, s69, 1\ns_cbranch_scc0 58b\n59:\nmvva 144,48,76\ndt 40,1,45056\ndt 42,1,55296\nmvva 144,52,84\n"
"dt 44,1,61440\ndt 46,7,6144\nmvva 148,52,88\nbuffer_load_dwordx4 v[160:163], v21, s[56:59], s31 offen offset:0 sc1\nmvva 148,48,80\nwl 8\nbuffer_load_dwordx4 v[164:167], v21, s[56:59], s31 offen offset:1024 sc1\nmvva 156,56,80\n"
"dt 48,2,45056\ndt 50,2,55296\nmvva 156,60,88\ndt 52,2,61440\ndt 54,8,6144\nmvva 152,60,84\nbuffer_load_dwordx4 v[168:171], v21, s[56:59], s31 offen offset:2048 sc1\nmvva 152,56,76\n"
"wl 8\nbuffer_load_dwordx4 v[172:175], v21, s[56:59], s31 offen offset:3072 sc1\ns_barrier\nmvva 128,32,92\ndt 56,3,45056\ndt 58,3,55296\nmvva 128,36,100\ns_add_u32 s31, s31, 4096\n"
"dt 60,3,61440\ndt 62,9,6144\nmvva 132,36,104\nbuffer_load_dwordx4 v[176:179], v21, s[56:59], s31 offen offset:0 sc1\nmvva 132,32,96\nwl 8\nbuffer_load_dwordx4 v[180:183], v21, s[56:59], s31 offen offset:1024 sc1\nmvva 140,40,96\n"
"dt 32,0,62464\ndt 34,6,7168\nmvva 140,44,104\ndt 36,0,63488\ndt 38,6,8192\nmvva 136,44,100\nbuffer_load_dwordx4 v[184:187], v21, s[56:59], s31 offen offset:2048 sc1\nmvva 136,40,92\n"
"wl 8\nbuffer_load_dwordx4 v[188:191], v21, s[56:59], s31 offen offset:3072 sc1\nmvva 144,48,92\ndt 40,1,62464\ndt 42,7,7168\nmvva 144,52,100\ndt 44,1,63488\ndt 46,7,8192\n"
"mvva 148,52,104\nmvva 148,48,96\nwl 8\nlc 28,21,67,1024\nmvva 156,56,96\ndt 48,2,62464\ndt 50,8,7168\nmvva 156,60,104\n"
"dt 52,2,63488\ndt 54,8,8192\nmvva 152,60,100\nmvva 152,56,92\nwl 8\nmvva 128,32,108\ndt 56,3,62464\ndt 58,9,7168\n"
"mvva 128,36,116\ndt 60,3,63488\ndt 62,9,8192\nmvva 132,36,120\nmvva 132,32,112\nwl 8\nmvva 140,40,112\ndt 32,0,64512\n"
"dt 34,6,9216\nmvva 140,44,120\ndt 36,6,0\ndt 38,6,10240\nmvva 136,44,116\nmvva 136,40,108\nwl 8\nmvva 144,48,108\n"
"dt 40,1,64512\ndt 42,7,9216\nmvva 144,52,116\ndt 44,7,0\ndt 46,7,10240\nmvva 148,52,120\nmvva 148,48,112\nwl 8\n"
"mvva 156,56,112\ndt 48,2,64512\ndt 50,8,9216\nmvva 156,60,120\ndt 52,8,0\ndt 54,8,10240\nmvva 152,60,116\nmvva 152,56,108\n"
"wl 8\nmvva 128,32,124\ndt 56,3,64512\ndt 58,9,9216\ns_waitcnt vmcnt(8)\nv_add_f32 v64, v64, v160\nmvva 128,36,132\ndt 60,9,0\n"
"dt 62,9,10240\nv_add_f32 v65, v65, v161\nmvva 132,36,136\nv_add_f32 v66, v66, v162\nv_add_f32 v67, v67, v163\nmvva 132,32,128\nwl 8\nv_mul_f32 v160, s82, v64\n"
"v_mul_f32 v161, s82, v65\nmvva 140,40,128\ndt 32,6,16384\ndt 34,6,26624\nv_mul_f32 v162, s82, v66\nmvva 140,44,136\ndt 36,6,17408\ndt 38,6,27648\n"
"v_mul_f32 v163, s82, v67\nmvva 136,44,132\nve 160\nmvva 136,40,124\nwl 8\nve 161\nmvva 144,48,124\ndt 40,7,16384\n"
"dt 42,7,26624\nve 162\nmvva 144,52,132\ndt 44,7,17408\ndt 46,7,27648\nve 163\nmvva 148,52,136\nv_add_f32 v160, 1.0, v160\n"
"v_add_f32 v161, 1.0, v161\nmvva 148,48,128\nwl 8\nv_add_f32 v162, 1.0, v162\nv_add_f32 v163, 1.0, v163\nmvva 156,56,128\ndt 48,8,16384\ndt 50,8,26624\n"
"vr 160\nmvva 156,60,136\ndt 52,8,17408\ndt 54,8,27648\nvr 161\nmvva 152,60,132\nvr 162\nmvva 152,56,124\n"
"wl 8\nvr 163\nmvva 128,32,140\ndt 56,9,16384\ndt 58,9,26624\nv_mul_f32 v64, v64, v160\nmvva 128,36,148\ndt 60,9,17408\n"
"dt 62,9,27648\nv_mul_f32 v65, v65, v161\nmvva 132,36,152\nv_mul_f32 v66, v66, v162\nv_mul_f32 v67, v67, v163\nmvva 132,32,144\nwl 8\ns_waitcnt vmcnt(7)\n"
"v_add_f32 v68, v68, v164\nv_add_f32 v69, v69, v165\nmvva 140,40,144\ndt 32,6,18432\ndt 34,6,28672\nv_add_f32 v70, v70, v166\nmvva 140,44,152\ndt 36,6,19456\n"
"dt 38,6,29696\nv_add_f32 v71, v71, v167\nmvva 136,44,148\nv_mul_f32 v164, s82, v68\nv_mul_f32 v165, s82, v69\nmvva 136,40,140\nwl 8\nv_mul_f32 v166, s82, v70\n"
"v_mul_f32 v167, s82, v71\nmvva 144,48,140\ndt 40,7,18432\ndt 42,7,28672\nve 164\nmvva 144,52,148\ndt 44,7,19456\ndt 46,7,29696\n"
"ve 165\nmvva 148,52,152\nve 166\nmvva 148,48,144\nwl 8\nve 167\nmvva 156,56,144\ndt 48,8,18432\n"
"dt 50,8,28672\nv_add_f32 v164, 1.0, v164\nmvva 156,60,152\ndt 52,8,19456\ndt 54,8,29696\nv_add_f32 v165, 1.0, v165\nmvva 152,60,148\nv_add_f32 v166, 1.0, v166\n"
"v_add_f32 v167, 1.0, v167\nmvva 152,56,140\nwl 8\nvr 164\ns_barrier\nmvva 128,32,156\ndt 56,9,18432\ndt 58,9,28672\n"
"vr 165\nmvva 128,36,164\ndt 60,9,19456\ndt 62,9,29696\nvr 166\nmvva 132,36,168\nvr 167\nmvva 132,32,160\n"
"wl 8\nv_mul_f32 v68, v68, v164\nv_mul_f32 v69, v69, v165\nmvva 140,40,160\ndt 32,6,20480\ndt 34,6,30720\nv_mul_f32 v70, v70, v166\nmvva 140,44,168\n"
"dt 36,6,36864\ndt 38,6,47104\nv_mul_f32 v71, v71, v167\nmvva 136,44,164\ns_waitcnt vmcnt(6)\nv_add_f32 v72, v72, v168\nv_add_f32 v73, v73, v169\nmvva 136,40,156\n"
"wl 8\nv_add_f32 v74, v74, v170\nv_add_f32 v75, v75, v171\nmvva 144,48,156\ndt 40,7,20480\ndt 42,7,30720\nv_mul_f32 v168, s82, v72\nmvva 144,52,164\n"
"dt 44,7,36864\ndt 46,7,47104\nv_mul_f32 v169, s82, v73\nmvva 148,52,168\nv_mul_f32 v170, s82, v74\nv_mul_f32 v171, s82, v75\nmvva 148,48,160\nwl 8\n"
"ve 168\nmvva 156,56,160\ndt 48,8,20480\ndt 50,8,30720\nve 169\nmvva 156,60,168\ndt 52,8,36864\ndt 54,8,47104\n"
"ve 170\nmvva 152,60,164\nve 171\nmvva 152,56,156\nwl 8\nv_add_f32 v168, 1.0, v168\nv_add_f32 v169, 1.0, v169\nmvva 128,32,0\n"
"dt 56,9,20480\ndt 58,9,30720\nv_add_f32 v170, 1.0, v170\nmvva 128,36,8\ndt 60,9,36864\ndt 62,9,47104\nv_add_f32 v171, 1.0, v171\nmvva 132,36,12\n"
"vr 168\nmvva 132,32,4\nwl 8\nvr 169\nmvva 140,40,4\ndt 32,6,37888\ndt 34,6,48128\nvr 170\n"
"mvva 140,44,12\ndt 36,6,38912\ndt 38,6,49152\nvr 171\nmvva 136,44,8\nv_mul_f32 v72, v72, v168\nv_mul_f32 v73, v73, v169\nmvva 136,40,0\n"
"wl 8\nv_mul_f32 v74, v74, v170\nv_mul_f32 v75, v75, v171\nmvva 144,48,0\ndt 40,7,37888\ndt 42,7,48128\ns_waitcnt vmcnt(5)\nv_add_f32 v76, v76, v172\n"
"mvva 144,52,8\ndt 44,7,38912\ndt 46,7,49152\nv_add_f32 v77, v77, v173\nmvva 148,52,12\nv_add_f32 v78, v78, v174\nv_add_f32 v79, v79, v175\nmvva 148,48,4\n"
"wl 8\nv_mul_f32 v172, s82, v76\nv_mul_f32 v173, s82, v77\nmvva 156,56,4\ndt 48,8,37888\ndt 50,8,48128\nv_mul_f32 v174, s82, v78\nmvva 156,60,12\n"
"dt 52,8,38912\ndt 54,8,49152\nv_mul_f32 v175, s82, v79\nmvva 152,60,8\nve 172\nmvva 152,56,0\nwb 0,8\nve 173\n"
"mvva 128,32,16\ndt 56,9,37888\ndt 58,9,48128\nve 174\nmvva 128,36,24\ndt 60,9,38912\ndt 62,9,49152\nve 175\n"
"mvva 132,36,28\nv_add_f32 v172, 1.0, v172\nv_add_f32 v173, 1.0, v173\nmvva 132,32,20\nwl 8\nv_add_f32 v174, 1.0, v174\nv_add_f32 v175, 1.0, v175\nmvva 140,40,20\n"
"vr 172\nmvva 140,44,28\nvr 173\nmvva 136,44,24\nvr 174\nmvva 136,40,16\nwl 4\nvr 175\n"
"mvva 144,48,16\nv_mul_f32 v76, v76, v172\nmvva 144,52,24\nv_mul_f32 v77, v77, v173\nmvva 148,52,28\nv_mul_f32 v78, v78, v174\nv_mul_f32 v79, v79, v175\nmvva 148,48,20\n"
"wl 0\nmvva 156,56,20\nmvva 156,60,28\nmvva 152,60,24\nmvva 152,56,16\nv_pk_fma_f32 v[64:65], v[64:65], v[128:129], v[208:209]\nv_pk_fma_f32 v[66:67], v[66:67], v[130:131], v[210:211]\ncv 192,64,65\n"
"cv 193,66,67\nv_pk_fma_f32 v[72:73], v[72:73], v[136:137], v[212:213]\nv_pk_fma_f32 v[74:75], v[74:75], v[138:139], v[214:215]\ncv 194,72,73\ncv 195,74,75\nv_pk_add_f32 v[80:81], v[80:81], v[176:177]\nv_permlane16_swap_b32 v192, v194\nv_permlane16_swap_b32 v193, v195\n"
"buffer_store_dwordx4 v[192:195], v29, s[16:19], s32 offen offset:0 sc1\nv_pk_add_f32 v[82:83], v[82:83], v[178:179]\nv_pk_mul_f32 v[176:177], v[80:81], s[82:83]\nv_pk_mul_f32 v[178:179], v[82:83], s[82:83]\nve 176\nve 177\nve 178\nve 179\n"
"v_pk_add_f32 v[176:177], v[176:177], s[84:85]\nv_pk_add_f32 v[178:179], v[178:179], s[84:85]\nvr 176\nvr 177\nvr 178\nvr 179\nv_pk_mul_f32 v[80:81], v[80:81], v[176:177]\nv_pk_mul_f32 v[82:83], v[82:83], v[178:179]\n"
"v_pk_fma_f32 v[80:81], v[80:81], v[144:145], v[216:217]\nv_pk_fma_f32 v[82:83], v[82:83], v[146:147], v[218:219]\ncv 196,80,81\ncv 197,82,83\nv_pk_add_f32 v[88:89], v[88:89], v[184:185]\nv_pk_add_f32 v[90:91], v[90:91], v[186:187]\nv_pk_mul_f32 v[184:185], v[88:89], s[82:83]\nv_pk_mul_f32 v[186:187], v[90:91], s[82:83]\n"
"ve 184\nve 185\nve 186\nve 187\nv_pk_add_f32 v[184:185], v[184:185], s[84:85]\nv_pk_add_f32 v[186:187], v[186:187], s[84:85]\nvr 184\nvr 185\n"
"vr 186\nvr 187\nv_pk_mul_f32 v[88:89], v[88:89], v[184:185]\nv_pk_mul_f32 v[90:91], v[90:91], v[186:187]\nv_pk_fma_f32 v[88:89], v[88:89], v[152:153], v[220:221]\nv_pk_fma_f32 v[90:91], v[90:91], v[154:155], v[222:223]\ncv 198,88,89\ncv 199,90,91\n"
"v_pk_fma_f32 v[68:69], v[68:69], v[132:133], v[224:225]\nv_permlane16_swap_b32 v196, v198\nv_permlane16_swap_b32 v197, v199\nbuffer_store_dwordx4 v[196:199], v29, s[16:19], s32 offen offset:64 sc1\nv_pk_fma_f32 v[70:71], v[70:71], v[134:135], v[226:227]\ncv 200,68,69\ncv 201,70,71\nv_pk_fma_f32 v[76:77], v[76:77], v[140:141], v[228:229]\n"
"v_pk_fma_f32 v[78:79], v[78:79], v[142:143], v[230:231]\ncv 202,76,77\ncv 203,78,79\nv_pk_add_f32 v[84:85], v[84:85], v[180:181]\nv_permlane16_swap_b32 v200, v202\nv_permlane16_swap_b32 v201, v203\nbuffer_store_dwordx4 v[200:203], v29, s[16:19], s95 offen offset:0 sc1\nv_pk_add_f32 v[86:87], v[86:87], v[182:183]\n"
"v_pk_mul_f32 v[180:181], v[84:85], s[82:83]\nv_pk_mul_f32 v[182:183], v[86:87], s[82:83]\nve 180\nve 181\nve 182\nve 183\nv_pk_add_f32 v[180:181], v[180:181], s[84:85]\nv_pk_add_f32 v[182:183], v[182:183], s[84:85]\n"
"vr 180\nvr 181\nvr 182\nvr 183\nv_pk_mul_f32 v[84:85], v[84:85], v[180:181]\nv_pk_mul_f32 v[86:87], v[86:87], v[182:183]\nv_pk_fma_f32 v[84:85], v[84:85], v[148:149], v[232:233]\nv_pk_fma_f32 v[86:87], v[86:87], v[150:151], v[234:235]\n"
"cv 204,84,85\ncv 205,86,87\nv_pk_add_f32 v[92:93], v[92:93], v[188:189]\nv_pk_add_f32 v[94:95], v[94:95], v[190:191]\nv_pk_mul_f32 v[188:189], v[92:93], s[82:83]\nv_pk_mul_f32 v[190:191], v[94:95], s[82:83]\nve 188\nve 189\n"
"ve 190\nve 191\nv_pk_add_f32 v[188:189], v[188:189], s[84:85]\nv_pk_add_f32 v[190:191], v[190:191], s[84:85]\nvr 188\nvr 189\nvr 190\nvr 191\n"
"v_pk_mul_f32 v[92:93], v[92:93], v[188:189]\nv_pk_mul_f32 v[94:95], v[94:95], v[190:191]\nv_pk_fma_f32 v[92:93], v[92:93], v[156:157], v[236:237]\nv_pk_fma_f32 v[94:95], v[94:95], v[158:159], v[238:239]\ncv 206,92,93\ncv 207,94,95\ns_nop 0\nv_permlane16_swap_b32 v204, v206\n"
"v_permlane16_swap_b32 v205, v207\nbuffer_store_dwordx4 v[204:207], v29, s[16:19], s95 offen offset:64 sc1\n39:\ns_endpgm\n22:\nLKV0:\ns_lshr_b32 s87, s62, 10\ns_mul_i32 s87, s87, 205\n"
"s_mov_b32 s88, 0\ns_mov_b32 s89, 8192\ns_mov_b32 s90, 16384\ns_mov_b32 s91, 24576\nv_readfirstlane_b32 s34, v136\ns_and_b32 s92, s76, 0x700\ns_lshl_b32 s92, s92, 1\ns_add_u32 s92, s92, 0x42a000\n"
"s_add_u32 s34, s34, 0x9e3779b1\ns_and_b32 s93, s76, 0x7c\ns_add_u32 s93, s93, s92\ns_lshr_b32 s86, s87, 3\ns_and_b32 s86, s86, 0x180\ns_add_u32 s93, s93, s86\nlb 21,48,65,3072\ns_add_u32 s65, s65, s60\n"
"v_accvgpr_write_b32 a224, v96\nv_accvgpr_write_b32 a225, v97\nv_accvgpr_write_b32 a226, v98\nv_accvgpr_write_b32 a227, v99\nv_accvgpr_write_b32 a228, v100\nv_accvgpr_write_b32 a229, v101\nv_accvgpr_write_b32 a230, v102\nv_accvgpr_write_b32 a231, v103\n"
"sm 24576\ns_nop 0\nlb 22,52,66,0\ns_add_u32 s66, s66, s61\nv_accvgpr_write_b32 a232, v104\nv_accvgpr_write_b32 a233, v105\nv_accvgpr_write_b32 a234, v106\nv_accvgpr_write_b32 a235, v107\n"
"v_accvgpr_write_b32 a236, v108\nv_accvgpr_write_b32 a237, v109\nv_accvgpr_write_b32 a238, v110\nv_accvgpr_write_b32 a239, v111\nv_accvgpr_write_b32 a0, 0\nv_accvgpr_write_b32 a1, 0\nv_accvgpr_write_b32 a2, 0\nv_accvgpr_write_b32 a3, 0\n"
"v_accvgpr_write_b32 a4, 0\nv_accvgpr_write_b32 a5, 0\nv_accvgpr_write_b32 a6, 0\nv_accvgpr_write_b32 a7, 0\nv_accvgpr_write_b32 a8, 0\nv_accvgpr_write_b32 a9, 0\nv_accvgpr_write_b32 a10, 0\nv_accvgpr_write_b32 a11, 0\n"
"v_accvgpr_write_b32 a12, 0\nv_accvgpr_write_b32 a13, 0\nv_accvgpr_write_b32 a14, 0\nv_accvgpr_write_b32 a15, 0\ns_cmp_lg_u32 s62, 0\ns_cbranch_scc1 48f\ns_waitcnt vmcnt(2)\nv_mov_b32 v57, s34\n"
"buffer_store_dword v57, v30, s[56:59], s93 offen sc1\ns_mov_b32 s93, 0x70000000\n48:\nv_add_u32 v6, 65536, v0\nv_add_u32 v7, 65536, v1\nv_add_u32 v8, 65536, v2\nv_add_u32 v9, 65536, v3\nv_add_u32 v10, 65536, v4\n"
"v_add_u32 v11, 65536, v5\nv_add_u32 v12, 131072, v0\nv_add_u32 v13, 131072, v1\nv_add_u32 v14, 131072, v2\nv_add_u32 v15, 131072, v3\nv_add_u32 v16, 131072, v4\nv_add_u32 v17, 131072, v5\ns_mov_b32 s67, s63\n"
"s_mov_b32 s68, 3\ns_mov_b32 s80, 0x3fb8aa3b\ns_mov_b32 s81, 0x3fb8aa3b\ns_mov_b32 s82, 0xbfb8aa3b\ns_mov_b32 s83, 0xbfb8aa3b\ns_mov_b32 s84, 1.0\ns_mov_b32 s85, 1.0\ns_cmp_eq_u32 s62, 15360\n"
"s_cbranch_scc1 41f\nsm 40960\ns_nop 0\nlb 20,36,64,0\nlb 20,40,64,1024\ns_add_u32 s64, s64, 229376\nlb 21,44,65,2048\nlb 21,48,65,3072\n"
"s_add_u32 s65, s65, s60\nsm 45056\ns_nop 0\nlb 22,52,66,0\ns_add_u32 s66, s66, s61\nsm 61440\ns_nop 0\nlb 20,36,64,0\n"
"lb 20,40,64,1024\ns_add_u32 s64, s64, 229376\nlb 21,44,65,2048\nlb 21,48,65,3072\ns_add_u32 s65, s65, s60\nsm 65536\ns_nop 0\nlb 22,52,66,0\n"
"s_add_u32 s66, s66, s61\n41:\nv_accvgpr_write_b32 a16, 0\nv_accvgpr_write_b32 a17, 0\nv_accvgpr_write_b32 a18, 0\nv_accvgpr_write_b32 a19, 0\nv_accvgpr_write_b32 a20, 0\nv_accvgpr_write_b32 a21, 0\n"
"v_accvgpr_write_b32 a22, 0\nv_accvgpr_write_b32 a23, 0\nv_accvgpr_write_b32 a24, 0\nv_accvgpr_write_b32 a25, 0\nv_accvgpr_write_b32 a26, 0\nv_accvgpr_write_b32 a27, 0\nv_accvgpr_write_b32 a28, 0\nv_accvgpr_write_b32 a29, 0\n"
"v_accvgpr_write_b32 a30, 0\nv_accvgpr_write_b32 a31, 0\nv_accvgpr_write_b32 a32, 0\nv_accvgpr_write_b32 a33, 0\nv_accvgpr_write_b32 a34, 0\nv_accvgpr_write_b32 a35, 0\nv_accvgpr_write_b32 a36, 0\nv_accvgpr_write_b32 a37, 0\n"
"v_accvgpr_write_b32 a38, 0\nv_accvgpr_write_b32 a39, 0\nv_accvgpr_write_b32 a40, 0\nv_accvgpr_write_b32 a41, 0\nv_accvgpr_write_b32 a42, 0\nv_accvgpr_write_b32 a43, 0\nv_accvgpr_write_b32 a44, 0\nv_accvgpr_write_b32 a45, 0\n"
"v_accvgpr_write_b32 a46, 0\nv_accvgpr_write_b32 a47, 0\nv_accvgpr_write_b32 a48, 0\nv_accvgpr_write_b32 a49, 0\nv_accvgpr_write_b32 a50, 0\nv_accvgpr_write_b32 a51, 0\nv_accvgpr_write_b32 a52, 0\nv_accvgpr_write_b32 a53, 0\n"
"v_accvgpr_write_b32 a54, 0\nv_accvgpr_write_b32 a55, 0\nv_accvgpr_write_b32 a56, 0\nv_accvgpr_write_b32 a57, 0\nv_accvgpr_write_b32 a58, 0\nv_accvgpr_write_b32 a59, 0\nv_accvgpr_write_b32 a60, 0\nv_accvgpr_write_b32 a61, 0\n"
"v_accvgpr_write_b32 a62, 0\nv_accvgpr_write_b32 a63, 0\nv_accvgpr_write_b32 a64, 0\nv_accvgpr_write_b32 a65, 0\nv_accvgpr_write_b32 a66, 0\nv_accvgpr_write_b32 a67, 0\nv_accvgpr_write_b32 a68, 0\nv_accvgpr_write_b32 a69, 0\n"
"v_accvgpr_write_b32 a70, 0\nv_accvgpr_write_b32 a71, 0\nv_accvgpr_write_b32 a72, 0\nv_accvgpr_write_b32 a73, 0\nv_accvgpr_write_b32 a74, 0\nv_accvgpr_write_b32 a75, 0\nv_accvgpr_write_b32 a76, 0\nv_accvgpr_write_b32 a77, 0\n"
"v_accvgpr_write_b32 a78, 0\nv_accvgpr_write_b32 a79, 0\nv_accvgpr_write_b32 a80, 0\nv_accvgpr_write_b32 a81, 0\nv_accvgpr_write_b32 a82, 0\nv_accvgpr_write_b32 a83, 0\nv_accvgpr_write_b32 a84, 0\nv_accvgpr_write_b32 a85, 0\n"
"v_accvgpr_write_b32 a86, 0\nv_accvgpr_write_b32 a87, 0\nv_accvgpr_write_b32 a88, 0\nv_accvgpr_write_b32 a89, 0\nv_accvgpr_write_b32 a90, 0\nv_accvgpr_write_b32 a91, 0\nv_accvgpr_write_b32 a92, 0\nv_accvgpr_write_b32 a93, 0\n"
"v_accvgpr_write_b32 a94, 0\nv_accvgpr_write_b32 a95, 0\nv_accvgpr_write_b32 a96, 0\nv_accvgpr_write_b32 a97, 0\nv_accvgpr_write_b32 a98, 0\nv_accvgpr_write_b32 a99, 0\nv_accvgpr_write_b32 a100, 0\nv_accvgpr_write_b32 a101, 0\n"
"v_accvgpr_write_b32 a102, 0\nv_accvgpr_write_b32 a103, 0\nv_accvgpr_write_b32 a104, 0\nv_accvgpr_write_b32 a105, 0\nv_accvgpr_write_b32 a106, 0\nv_accvgpr_write_b32 a107, 0\nv_accvgpr_write_b32 a108, 0\nv_accvgpr_write_b32 a109, 0\n"
"v_accvgpr_write_b32 a110, 0\nv_accvgpr_write_b32 a111, 0\nv_accvgpr_write_b32 a112, 0\nv_accvgpr_write_b32 a113, 0\nv_accvgpr_write_b32 a114, 0\nv_accvgpr_write_b32 a115, 0\nv_accvgpr_write_b32 a116, 0\nv_accvgpr_write_b32 a117, 0\n"
"v_accvgpr_write_b32 a118, 0\nv_accvgpr_write_b32 a119, 0\nv_accvgpr_write_b32 a120, 0\nv_accvgpr_write_b32 a121, 0\nv_accvgpr_write_b32 a122, 0\nv_accvgpr_write_b32 a123, 0\nv_accvgpr_write_b32 a124, 0\nv_accvgpr_write_b32 a125, 0\n"
"v_accvgpr_write_b32 a126, 0\nv_accvgpr_write_b32 a127, 0\nv_mov_b32 v64, 0\nv_mov_b32 v65, 0\nv_mov_b32 v66, 0\nv_mov_b32 v67, 0\nv_mov_b32 v68, 0\nv_mov_b32 v69, 0\n"
"v_mov_b32 v70, 0\nv_mov_b32 v71, 0\nv_mov_b32 v72, 0\nv_mov_b32 v73, 0\nv_mov_b32 v74, 0\nv_mov_b32 v75, 0\nv_mov_b32 v76, 0\nv_mov_b32 v77, 0\n"
"v_mov_b32 v78, 0\nv_mov_b32 v79, 0\nv_mov_b32 v80, 0\nv_mov_b32 v81, 0\nv_mov_b32 v82, 0\nv_mov_b32 v83, 0\nv_mov_b32 v84, 0\nv_mov_b32 v85, 0\n"
"v_mov_b32 v86, 0\nv_mov_b32 v87, 0\nv_mov_b32 v88, 0\nv_mov_b32 v89, 0\nv_mov_b32 v90, 0\nv_mov_b32 v91, 0\nv_mov_b32 v92, 0\nv_mov_b32 v93, 0\n"
"v_mov_b32 v94, 0\nv_mov_b32 v95, 0\nv_mov_b32 v96, 0\nv_mov_b32 v97, 0\nv_mov_b32 v98, 0\nv_mov_b32 v99, 0\nv_mov_b32 v100, 0\nv_mov_b32 v101, 0\n"
"v_mov_b32 v102, 0\nv_mov_b32 v103, 0\nv_mov_b32 v104, 0\nv_mov_b32 v105, 0\nv_mov_b32 v106, 0\nv_mov_b32 v107, 0\nv_mov_b32 v108, 0\nv_mov_b32 v109, 0\n"
"v_mov_b32 v110, 0\nv_mov_b32 v111, 0\nv_mov_b32 v112, 0\nv_mov_b32 v113, 0\nv_mov_b32 v114, 0\nv_mov_b32 v115, 0\nv_mov_b32 v116, 0\nv_mov_b32 v117, 0\n"
"v_mov_b32 v118, 0\nv_mov_b32 v119, 0\nv_mov_b32 v120, 0\nv_mov_b32 v121, 0\nv_mov_b32 v122, 0\nv_mov_b32 v123, 0\nv_mov_b32 v124, 0\nv_mov_b32 v125, 0\n"
"v_mov_b32 v126, 0\nv_mov_b32 v127, 0\nv_mov_b32 v128, 0\nv_mov_b32 v129, 0\nv_mov_b32 v130, 0\nv_mov_b32 v131, 0\nv_mov_b32 v132, 0\nv_mov_b32 v133, 0\n"
"v_mov_b32 v134, 0\nv_mov_b32 v135, 0\nv_mov_b32 v136, 0\nv_mov_b32 v137, 0\nv_mov_b32 v138, 0\nv_mov_b32 v139, 0\ns_waitcnt vmcnt(12)\nv_mov_b32 v140, 0\n"
"s_waitcnt vmcnt(11)\nv_mov_b32 v141, 0\nv_mov_b32 v142, 0\nv_mov_b32 v143, 0\nv_mov_b32 v144, 0\nv_mov_b32 v145, 0\nv_mov_b32 v146, 0\nv_mov_b32 v147, 0\n"
"v_mov_b32 v148, 0\nv_mov_b32 v149, 0\nv_mov_b32 v150, 0\nv_mov_b32 v151, 0\nv_mov_b32 v152, 0\nv_mov_b32 v153, 0\nv_mov_b32 v154, 0\nv_mov_b32 v155, 0\n"
"v_mov_b32 v156, 0\nv_mov_b32 v157, 0\nv_mov_b32 v158, 0\nv_mov_b32 v159, 0\nv_mov_b32 v160, 0\nv_mov_b32 v161, 0\nv_mov_b32 v162, 0\nv_mov_b32 v163, 0\n"
"v_mov_b32 v164, 0\nv_mov_b32 v165, 0\nv_mov_b32 v166, 0\nv_mov_b32 v167, 0\nv_mov_b32 v168, 0\nv_mov_b32 v169, 0\nv_mov_b32 v170, 0\nv_mov_b32 v171, 0\n"
"v_mov_b32 v172, 0\nv_mov_b32 v173, 0\nv_mov_b32 v174, 0\nv_mov_b32 v175, 0\nv_mov_b32 v176, 0\nv_mov_b32 v177, 0\nv_mov_b32 v178, 0\nv_mov_b32 v179, 0\n"
"v_mov_b32 v180, 0\nv_mov_b32 v181, 0\nv_mov_b32 v182, 0\nv_mov_b32 v183, 0\nv_mov_b32 v184, 0\nv_mov_b32 v185, 0\nv_mov_b32 v186, 0\nv_mov_b32 v187, 0\n"
"v_mov_b32 v188, 0\nv_mov_b32 v189, 0\nv_mov_b32 v190, 0\nv_mov_b32 v191, 0\nv_mov_b32 v192, 0\nv_mov_b32 v193, 0\nv_mov_b32 v194, 0\nv_mov_b32 v195, 0\n"
"v_mov_b32 v196, 0\nv_mov_b32 v197, 0\nv_mov_b32 v198, 0\nv_mov_b32 v199, 0\nv_mov_b32 v200, 0\nv_mov_b32 v201, 0\nv_mov_b32 v202, 0\nv_mov_b32 v203, 0\n"
"v_mov_b32 v204, 0\nv_mov_b32 v205, 0\nv_mov_b32 v206, 0\nv_mov_b32 v207, 0\nv_mov_b32 v208, 0\nv_mov_b32 v209, 0\nv_mov_b32 v210, 0\nv_mov_b32 v211, 0\n"
"v_mov_b32 v212, 0\nv_mov_b32 v213, 0\nv_mov_b32 v214, 0\nv_mov_b32 v215, 0\nv_mov_b32 v216, 0\nv_mov_b32 v217, 0\nv_mov_b32 v218, 0\nv_mov_b32 v219, 0\n"
"v_mov_b32 v220, 0\nv_mov_b32 v221, 0\nv_mov_b32 v222, 0\nv_mov_b32 v223, 0\nv_mov_b32 v224, 0\nv_mov_b32 v225, 0\nv_mov_b32 v226, 0\nv_mov_b32 v227, 0\n"
"v_mov_b32 v228, 0\nv_mov_b32 v229, 0\nv_mov_b32 v230, 0\nv_mov_b32 v231, 0\nv_mov_b32 v232, 0\nv_mov_b32 v233, 0\nv_mov_b32 v234, 0\nv_mov_b32 v235, 0\n"
"v_mov_b32 v236, 0\nv_mov_b32 v237, 0\nv_mov_b32 v238, 0\nv_mov_b32 v239, 0\nv_mov_b32 v240, 0\nv_mov_b32 v241, 0\nv_mov_b32 v242, 0\nv_mov_b32 v243, 0\n"
"v_mov_b32 v244, 0\nv_mov_b32 v245, 0\nv_mov_b32 v246, 0\nv_mov_b32 v247, 0\nv_mov_b32 v248, 0\nv_mov_b32 v249, 0\nv_mov_b32 v250, 0\nv_mov_b32 v251, 0\n"
"v_mov_b32 v252, 0\nv_mov_b32 v253, 0\nv_mov_b32 v254, 0\nv_mov_b32 v255, 0\ns_cmp_lg_u32 s62, 15360\ns_cbranch_scc1 49f\nv_lshrrev_b32 v56, 2, v23\nv_and_b32 v56, 124, v56\n"
"buffer_load_dword v31, v56, s[56:59], s92 offen offset:0 sc1\ns_waitcnt vmcnt(0)\nv_cmp_ne_u32 vcc, s34, v31\ns_cbranch_vccz 51f\ns_mov_b32 s69, 0x80000\n50:\ns_sleep 2\nbuffer_load_dword v31, v56, s[56:59], s92 offen offset:0 sc1\n"
"s_waitcnt vmcnt(0)\nv_cmp_ne_u32 vcc, s34, v31\ns_cbranch_vccz 51f\ns_sub_u32 s69, s69, 1\ns_cbranch_scc0 50b\n51:\n49:\ns_waitcnt vmcnt(10)\n"
"s_barrier\nlc 128,23,67,0\nlc 132,23,67,1024\nlc 136,23,67,2048\nlc 140,23,67,3072\ns_add_u32 s67, s67, 4096\nlc 144,23,67,0\nlc 148,23,67,1024\n"
"lc 152,23,67,2048\nlc 156,23,67,3072\ns_add_u32 s67, s67, 4096\ns_cmp_lg_u32 s62, 15360\ns_cbranch_scc1 40f\nsm 40960\ns_nop 0\nlb 20,36,64,0\n"
"lb 20,40,64,1024\ns_add_u32 s64, s64, 229376\nlb 21,44,65,2048\nlb 21,48,65,3072\ns_add_u32 s65, s65, s60\nsm 45056\ns_nop 0\nlb 22,52,66,0\n"
"s_add_u32 s66, s66, s61\nsm 61440\ns_nop 0\nlb 20,36,64,0\nlb 20,40,64,1024\ns_add_u32 s64, s64, 229376\nlb 21,44,65,2048\nlb 21,48,65,3072\n"
"s_add_u32 s65, s65, s60\nsm 65536\ns_nop 0\nlb 22,52,66,0\ns_add_u32 s66, s66, s61\n40:\nv_mov_b32 v31, v33\ns_cmp_eq_u32 s62, 0\n"
"s_cbranch_scc1 46f\nbuffer_store_dwordx4 a[224:227], v31, s[56:59], s88 offen sc1\nbuffer_store_dwordx4 a[228:231], v31, s[56:59], s89 offen sc1\nbuffer_store_dwordx4 a[232:235], v31, s[56:59], s90 offen sc1\nbuffer_store_dwordx4 a[236:239], v31, s[56:59], s91 offen sc1\n46:\nlc 160,23,67,0\nlc 164,23,67,1024\n"
"lc 168,23,67,2048\nlc 172,23,67,3072\ns_add_u32 s67, s67, 4096\nlc 176,23,67,0\nlc 180,23,67,1024\nlc 184,23,67,2048\nlc 188,23,67,3072\ns_add_u32 s67, s67, 4096\n"
"s_sub_u32 s94, s30, 27287552\ns_lshr_b32 s94, s94, 2\ns_and_b32 s95, s32, 0x700\ns_sub_u32 s86, s8, s95\ns_subb_u32 s95, s9, 0\ns_add_u32 s94, s86, s94\ns_addc_u32 s95, s95, 0\ns_lshl_b32 s86, s2, 8\n"
"s_cmp_eq_u32 s62, 15360\ns_cselect_b32 s86, s2, s86\ns_cselect_b32 s87, 2, 99\ndt 32,0,0\ndt 34,0,10240\ndt 36,0,20480\ndt 38,0,30720\ndt 40,1,0\n"
"dt 42,1,10240\ndt 44,1,20480\ndt 46,1,30720\ndt 48,2,0\ndt 50,2,10240\ndt 52,2,20480\ndt 54,2,30720\ns_cmp_eq_u32 s62, 0\n"
"s_cbranch_scc1 45f\ns_cmp_eq_u32 s62, 15360\ns_waitcnt vmcnt(22)\ns_cbranch_scc1 44f\ns_waitcnt vmcnt(12)\ns_branch 44f\n45:\ns_waitcnt vmcnt(8)\n"
"44:\n1:\nwb 19,8\nmvva 64,32,128\ndt 56,3,0\nmvva 64,36,144\ndt 58,3,10240\nmvva 68,36,148\n"
"dt 60,3,20480\nmvva 68,32,132\ndt 62,3,30720\nmvva 72,32,136\nmvva 72,36,152\nmvva 76,36,156\nmvva 76,32,140\nwl 8\n"
"mvva 92,40,140\ndt 32,0,1024\nmvva 92,44,156\ndt 34,0,11264\nmvva 88,44,152\ndt 36,0,21504\nmvva 88,40,136\ndt 38,0,31744\n"
"mvva 84,40,132\nsm 81920\nmvva 84,44,148\nlb 20,36,64,0\nmvva 80,44,144\nmvva 80,40,128\nwl 8\nmvva 96,48,128\n"
"dt 40,1,1024\nmvva 96,52,144\ndt 42,1,11264\nmvva 100,52,148\ndt 44,1,21504\nmvva 100,48,132\ndt 46,1,31744\nmvva 104,48,136\n"
"mvva 104,52,152\nlb 20,40,64,1024\nmvva 108,52,156\ns_add_u32 s64, s64, 229376\nmvva 108,48,140\nwl 8\nmvva 124,56,140\ndt 48,2,1024\n"
"mvva 124,60,156\ndt 50,2,11264\nmvva 120,60,152\ndt 52,2,21504\nmvva 120,56,136\ndt 54,2,31744\nmvva 116,56,132\nmvva 116,60,148\n"
"lb 21,44,65,2048\nmvva 112,60,144\nmvva 112,56,128\nwl 8\nmava 0,32,128\ndt 56,3,1024\nmava 0,36,144\ndt 58,3,11264\n"
"mava 4,36,148\ndt 60,3,21504\nmava 4,32,132\ndt 62,3,31744\nmava 8,32,136\nmava 8,36,152\nlb 21,48,65,3072\nmava 12,36,156\n"
"s_add_u32 s65, s65, s60\nmava 12,32,140\nwl 8\nmava 28,40,140\ndt 32,0,2048\nmava 28,44,156\ndt 34,0,12288\nmava 24,44,152\n"
"dt 36,0,22528\nmava 24,40,136\ndt 38,0,32768\nmava 20,40,132\nsm 86016\nmava 20,44,148\nlb 22,52,66,0\nmava 16,44,144\n"
"v_lshlrev_b32 v31, 2, v23\ns_add_u32 s66, s66, s61\nmava 16,40,128\nbuffer_load_dword a224, v31, s[0:3], 0 offen\ns_add_u32 s0, s0, s86\ns_addc_u32 s1, s1, 0\nwl 8\nmava 32,48,128\n"
"dt 40,1,2048\nmava 32,52,144\ndt 42,1,12288\nmava 36,52,148\ndt 44,1,22528\nmava 36,48,132\ndt 46,1,32768\nmava 40,48,136\n"
"sm 102400\nmava 40,52,152\nlb 20,36,64,0\nmava 44,52,156\nmava 44,48,140\nwl 8\nmava 60,56,140\ndt 48,2,2048\n"
"mava 60,60,156\ndt 50,2,12288\nmava 56,60,152\ndt 52,2,22528\nmava 56,56,136\ndt 54,2,32768\nmava 52,56,132\nmava 52,60,148\n"
"lb 20,40,64,1024\nmava 48,60,144\ns_add_u32 s64, s64, 229376\nmava 48,56,128\nwl 8\nmvva 128,32,128\ndt 56,3,2048\nmvva 128,36,144\n"
"dt 58,3,12288\nmvva 132,36,148\ndt 60,3,22528\nmvva 132,32,132\ndt 62,3,32768\nmvva 136,32,136\nmvva 136,36,152\nlb 21,44,65,2048\n"
"mvva 140,36,156\nmvva 140,32,140\nwl 8\nmvva 156,40,140\ndt 32,0,3072\nmvva 156,44,156\ndt 34,0,13312\nmvva 152,44,152\n"
"dt 36,0,23552\nmvva 152,40,136\ndt 38,0,33792\nmvva 148,40,132\nmvva 148,44,148\nlb 21,48,65,3072\nmvva 144,44,144\ns_add_u32 s65, s65, s60\n"
"mvva 144,40,128\nwl 8\nmvva 160,48,128\ndt 40,1,3072\nmvva 160,52,144\ndt 42,1,13312\nmvva 164,52,148\ndt 44,1,23552\n"
"mvva 164,48,132\ndt 46,1,33792\nmvva 168,48,136\nsm 106496\nmvva 168,52,152\nlb 22,52,66,0\nmvva 172,52,156\ns_add_u32 s66, s66, s61\n"
"mvva 172,48,140\nwl 8\nmvva 188,56,140\ndt 48,2,3072\nmvva 188,60,156\ndt 50,2,13312\nmvva 184,60,152\ndt 52,2,23552\n"
"mvva 184,56,136\ndt 54,2,33792\nmvva 180,56,132\nmvva 180,60,148\nlc 192,23,67,0\nmvva 176,60,144\nmvva 176,56,128\nwl 8\n"
"mvav 192,128,32\ndt 56,3,3072\nmvav 192,144,36\ndt 58,3,13312\nmvav 196,148,36\ndt 60,3,23552\nmvav 196,132,32\ndt 62,3,33792\n"
"mvav 200,136,32\nmvav 200,152,36\nlc 196,23,67,1024\nmvav 204,156,36\nmvav 204,140,32\nwl 8\nmvav 220,140,40\ndt 32,4,4096\n"
"mvav 220,156,44\ndt 34,4,14336\nmvav 216,152,44\ndt 36,4,24576\nmvav 216,136,40\ndt 38,4,34816\nmvav 212,132,40\nmvav 212,148,44\n"
"lc 200,23,67,2048\nmvav 208,144,44\nmvav 208,128,40\nwl 8\nmvav 224,128,48\ndt 40,5,4096\nmvav 224,144,52\ndt 42,5,14336\n"
"mvav 228,148,52\ndt 44,5,24576\nmvav 228,132,48\ndt 46,5,34816\nmvav 232,136,48\nmvav 232,152,52\nlc 204,23,67,3072\nmvav 236,156,52\n"
"s_add_u32 s67, s67, 4096\nmvav 236,140,48\nwl 8\nmvav 252,140,56\ndt 48,4,9216\nmvav 252,156,60\ndt 50,4,19456\nmvav 248,152,60\n"
"dt 52,4,29696\nmvav 248,136,56\ndt 54,4,39936\nmvav 244,132,56\nmvav 244,148,60\nlc 208,23,67,0\nmvav 240,144,60\nmvav 240,128,56\n"
"wl 8\nmava 64,32,128\ndt 56,5,9216\nmava 64,36,144\ndt 58,5,19456\nmava 68,36,148\ndt 60,5,29696\nmava 68,32,132\n"
"dt 62,5,39936\nmava 72,32,136\nmava 72,36,152\nlc 212,23,67,1024\nmava 76,36,156\ns_waitcnt vmcnt(25)\ns_barrier\nmava 76,32,140\n"
"wl 8\ns_mov_b64 exec, 1\nv_mov_b32 v31, s34\nbuffer_store_dword v31, off, s[56:59], s93 sc1\ns_mov_b64 exec, -1\ns_mov_b32 s93, 0x70000000\ns_add_u32 s92, s92, 128\nmava 92,40,140\n"
"dt 32,0,40960\nmava 92,44,156\ndt 34,0,51200\nmava 88,44,152\ndt 36,0,61440\nmava 88,40,136\ndt 38,6,6144\nmava 84,40,132\n"
"mava 84,44,148\nlc 216,23,67,2048\nmava 80,44,144\nmava 80,40,128\nwl 8\nmava 96,48,128\ndt 40,1,40960\nmava 96,52,144\n"
"dt 42,1,51200\nmava 100,52,148\ndt 44,1,61440\nmava 100,48,132\ndt 46,7,6144\nmava 104,48,136\nmava 104,52,152\nlc 220,23,67,3072\n"
"mava 108,52,156\ns_add_u32 s67, s67, 4096\nmava 108,48,140\nwl 8\nmava 124,56,140\ndt 48,2,40960\nmava 124,60,156\ndt 50,2,51200\n"
"mava 120,60,152\ndt 52,2,61440\nmava 120,56,136\ndt 54,8,6144\nmava 116,56,132\nmava 116,60,148\nmava 112,60,144\nmava 112,56,128\n"
"wb 20,8\nmvva 64,32,160\ndt 56,3,40960\nmvva 64,36,176\ndt 58,3,51200\nmvva 68,36,180\ndt 60,3,61440\nmvva 68,32,164\n"
"dt 62,9,6144\nmvva 72,32,168\nmvva 72,36,184\nmvva 76,36,188\nmvva 76,32,172\nwl 8\nmvva 92,40,172\ndt 32,0,41984\n"
"mvva 92,44,188\ndt 34,0,52224\nmvva 88,44,184\ndt 36,0,62464\nmvva 88,40,168\ndt 38,6,7168\nmvva 84,40,164\nsm 122880\n"
"mvva 84,44,180\nlb 20,36,64,0\nmvva 80,44,176\nmvva 80,40,160\nwl 8\nmvva 96,48,160\ndt 40,1,41984\nmvva 96,52,176\n"
"dt 42,1,52224\nmvva 100,52,180\ndt 44,1,62464\nmvva 100,48,164\ndt 46,7,7168\nmvva 104,48,168\nmvva 104,52,184\nlb 20,40,64,1024\n"
"mvva 108,52,188\ns_add_u32 s64, s64, 229376\nmvva 108,48,172\nwl 8\nmvva 124,56,172\ndt 48,2,41984\nmvva 124,60,188\ndt 50,2,52224\n"
"mvva 120,60,184\ndt 52,2,62464\nmvva 120,56,168\ndt 54,8,7168\nmvva 116,56,164\nmvva 116,60,180\nlb 21,44,65,2048\nmvva 112,60,176\n"
"mvva 112,56,160\nwl 8\nmava 0,32,160\ndt 56,3,41984\nmava 0,36,176\ndt 58,3,52224\nmava 4,36,180\ndt 60,3,62464\n"
"mava 4,32,164\ndt 62,9,7168\nmava 8,32,168\nmava 8,36,184\nlb 21,48,65,3072\nmava 12,36,188\ns_add_u32 s65, s65, s60\nmava 12,32,172\n"
"wl 8\nmava 28,40,172\ndt 32,0,43008\nmava 28,44,188\ndt 34,0,53248\nmava 24,44,184\ndt 36,0,63488\nmava 24,40,168\n"
"dt 38,6,8192\nmava 20,40,164\nsm 126976\nmava 20,44,180\nlb 22,52,66,0\nmava 16,44,176\nv_lshlrev_b32 v31, 2, v23\ns_add_u32 s66, s66, s61\n"
"mava 16,40,160\nbuffer_load_dword a128, v31, s[0:3], 0 offen\ns_add_u32 s0, s0, s86\ns_addc_u32 s1, s1, 0\nwl 8\nmava 32,48,160\ndt 40,1,43008\nmava 32,52,176\n"
"dt 42,1,53248\nmava 36,52,180\ndt 44,1,63488\nmava 36,48,164\ndt 46,7,8192\nmava 40,48,168\nsm 143360\nmava 40,52,184\n"
"lb 20,36,64,0\nmava 44,52,188\nmava 44,48,172\nwl 8\nmava 60,56,172\ndt 48,2,43008\nmava 60,60,188\ndt 50,2,53248\n"
"mava 56,60,184\ndt 52,2,63488\nmava 56,56,168\ndt 54,8,8192\nmava 52,56,164\nmava 52,60,180\nlb 20,40,64,1024\nmava 48,60,176\n"
"s_add_u32 s64, s64, 229376\nmava 48,56,160\nwl 8\nmvva 128,32,160\ndt 56,3,43008\nmvva 128,36,176\ndt 58,3,53248\nmvva 132,36,180\n"
"dt 60,3,63488\nmvva 132,32,164\ndt 62,9,8192\nmvva 136,32,168\nmvva 136,36,184\nlb 21,44,65,2048\nmvva 140,36,188\nmvva 140,32,172\n"
"wl 8\nmvva 156,40,172\ndt 32,0,44032\nmvva 156,44,188\ndt 34,0,54272\nmvva 152,44,184\ndt 36,0,64512\nmvva 152,40,168\n"
"dt 38,6,9216\nmvva 148,40,164\nmvva 148,44,180\nlb 21,48,65,3072\nmvva 144,44,176\ns_add_u32 s65, s65, s60\nmvva 144,40,160\nwl 8\n"
"mvva 160,48,160\ndt 40,1,44032\nmvva 160,52,176\ndt 42,1,54272\nmvva 164,52,180\ndt 44,1,64512\nmvva 164,48,164\ndt 46,7,9216\n"
"mvva 168,48,168\nsm 147456\nmvva 168,52,184\nlb 22,52,66,0\nmvva 172,52,188\ns_add_u32 s66, s66, s61\nmvva 172,48,172\nwl 8\n"
"v_lshrrev_b32 v31, 2, v23\nv_and_b32 v31, 124, v31\nbuffer_load_dword v31, v31, s[56:59], s92 offen offset:0 sc1\nmvva 188,56,172\ndt 48,2,44032\nmvva 188,60,188\ndt 50,2,54272\nmvva 184,60,184\n"
"dt 52,2,64512\nmvva 184,56,168\ndt 54,8,9216\nmvva 180,56,164\nmvva 180,60,180\nlc 224,23,67,0\nmvva 176,60,176\nmvva 176,56,160\n"
"wl 8\nmvav 192,160,32\ndt 56,3,44032\nmvav 192,176,36\ndt 58,3,54272\nmvav 196,180,36\ndt 60,3,64512\nmvav 196,164,32\n"
"dt 62,9,9216\nmvav 200,168,32\nmvav 200,184,36\nlc 228,23,67,1024\nmvav 204,188,36\nmvav 204,172,32\nwl 8\nmvav 220,172,40\n"
"dt 32,4,45056\nmvav 220,188,44\ndt 34,4,55296\nmvav 216,184,44\ndt 36,10,0\nmvav 216,168,40\ndt 38,10,10240\nmvav 212,164,40\n"
"mvav 212,180,44\nlc 232,23,67,2048\nmvav 208,176,44\nmvav 208,160,40\nwl 8\nmvav 224,160,48\ndt 40,5,45056\nmvav 224,176,52\n"
"dt 42,5,55296\nmvav 228,180,52\ndt 44,11,0\nmvav 228,164,48\ndt 46,11,10240\nmvav 232,168,48\nmvav 232,184,52\nlc 236,23,67,3072\n"
"mvav 236,188,52\ns_add_u32 s67, s67, 4096\nmvav 236,172,48\nwl 8\nmvav 252,172,56\ndt 48,4,50176\nmvav 252,188,60\ndt 50,4,60416\n"
"mvav 248,184,60\ndt 52,10,5120\nmvav 248,168,56\ndt 54,10,15360\nmvav 244,164,56\nmvav 244,180,60\nlc 240,23,67,0\nmvav 240,176,60\n"
"mvav 240,160,56\nwl 8\nmava 64,32,160\ndt 56,5,50176\nmava 64,36,176\ndt 58,5,60416\nmava 68,36,180\ndt 60,11,5120\n"
"mava 68,32,164\ndt 62,11,15360\nmava 72,32,168\nmava 72,36,184\nlc 244,23,67,1024\nmava 76,36,188\ns_waitcnt vmcnt(27)\ns_barrier\n"
"mava 76,32,172\nwl 8\nmava 92,40,172\ndt 32,6,16384\nmava 92,44,188\ndt 34,6,26624\nmava 88,44,184\ndt 36,6,36864\n"
"mava 88,40,168\ndt 38,6,47104\nmava 84,40,164\nmava 84,44,180\nlc 248,23,67,2048\nmava 80,44,176\nmava 80,40,160\nwl 8\n"
"mava 96,48,160\ndt 40,7,16384\nmava 96,52,176\ndt 42,7,26624\nmava 100,52,180\ndt 44,7,36864\nmava 100,48,164\ndt 46,7,47104\n"
"mava 104,48,168\nmava 104,52,184\nlc 252,23,67,3072\nmava 108,52,188\ns_add_u32 s67, s67, 4096\nmava 108,48,172\nwl 8\nmava 124,56,172\n"
"dt 48,8,16384\nmava 124,60,188\ndt 50,8,26624\nmava 120,60,184\ndt 52,8,36864\nmava 120,56,168\ndt 54,8,47104\nmava 116,56,164\n"
"mava 116,60,180\nmava 112,60,176\nmava 112,56,160\nwb 20,8\nmvva 64,32,192\ndt 56,9,16384\nmvva 64,36,208\ndt 58,9,26624\n"
"mvva 68,36,212\ndt 60,9,36864\nmvva 68,32,196\ndt 62,9,47104\nmvva 72,32,200\nmvva 72,36,216\nmvva 76,36,220\nmvva 76,32,204\n"
"wl 8\nmvva 92,40,204\ndt 32,6,17408\nmvva 92,44,220\ndt 34,6,27648\nmvva 88,44,216\ndt 36,6,37888\nmvva 88,40,200\n"
"dt 38,6,48128\nmvva 84,40,196\nsm 0\nmvva 84,44,212\nlb 20,36,64,0\nmvva 80,44,208\nmvva 80,40,192\nwl 8\n"
"mvva 96,48,192\ndt 40,7,17408\nmvva 96,52,208\ndt 42,7,27648\nmvva 100,52,212\ndt 44,7,37888\nmvva 100,48,196\ndt 46,7,48128\n"
"mvva 104,48,200\nmvva 104,52,216\nlb 20,40,64,1024\nmvva 108,52,220\ns_add_u32 s64, s64, 229376\nmvva 108,48,204\nwl 8\nmvva 124,56,204\n"
"dt 48,8,17408\nmvva 124,60,220\ndt 50,8,27648\nmvva 120,60,216\ndt 52,8,37888\nmvva 120,56,200\ndt 54,8,48128\nmvva 116,56,196\n"
"mvva 116,60,212\nlb 21,44,65,2048\nmvva 112,60,208\nmvva 112,56,192\nwl 8\nmava 0,32,192\ndt 56,9,17408\nmava 0,36,208\n"
"dt 58,9,27648\nmava 4,36,212\ndt 60,9,37888\nmava 4,32,196\ndt 62,9,48128\nmava 8,32,200\nmava 8,36,216\nlb 21,48,65,3072\n"
"mava 12,36,220\ns_add_u32 s65, s65, s60\nmava 12,32,204\nwl 8\nmava 28,40,204\ndt 32,6,18432\nmava 28,44,220\ndt 34,6,28672\n"
"mava 24,44,216\ndt 36,6,38912\nmava 24,40,200\ndt 38,6,49152\nmava 20,40,196\nsm 4096\nmava 20,44,212\nlb 22,52,66,0\n"
"mava 16,44,208\ns_add_u32 s66, s66, s61\nmava 16,40,192\nwl 8\nmava 32,48,192\ndt 40,7,18432\nmava 32,52,208\ndt 42,7,28672\n"
"mava 36,52,212\ndt 44,7,38912\nmava 36,48,196\ndt 46,7,49152\nmava 40,48,200\nsm 20480\nmava 40,52,216\nlb 20,36,64,0\n"
"mava 44,52,220\nmava 44,48,204\nwl 8\nmava 60,56,204\ndt 48,8,18432\nmava 60,60,220\ndt 50,8,28672\nmava 56,60,216\n"
"dt 52,8,38912\nmava 56,56,200\ndt 54,8,49152\nmava 52,56,196\nmava 52,60,212\nlb 20,40,64,1024\nmava 48,60,208\ns_add_u32 s64, s64, 229376\n"
"mava 48,56,192\nwl 8\nmvva 128,32,192\ndt 56,9,18432\nmvva 128,36,208\ndt 58,9,28672\nmvva 132,36,212\ndt 60,9,38912\n"
"mvva 132,32,196\ndt 62,9,49152\nmvva 136,32,200\nmvva 136,36,216\nlb 21,44,65,2048\nmvva 140,36,220\nmvva 140,32,204\nwl 8\n"
"mvva 156,40,204\ndt 32,6,19456\nmvva 156,44,220\ndt 34,6,29696\nmvva 152,44,216\ndt 36,6,39936\nmvva 152,40,200\ndt 38,6,50176\n"
"mvva 148,40,196\nmvva 148,44,212\nlb 21,48,65,3072\nmvva 144,44,208\ns_add_u32 s65, s65, s60\nmvva 144,40,192\nwl 8\ns_waitcnt vmcnt(17)\n"
"v_cmp_ne_u32 vcc, s34, v31\ns_cbranch_vccz 43f\ns_mov_b32 s69, 0x80000\n42:\ns_sleep 1\nv_lshrrev_b32 v31, 2, v23\nv_and_b32 v31, 124, v31\nbuffer_load_dword v31, v31, s[56:59], s92 offen offset:0 sc1\n"
"s_waitcnt vmcnt(0)\nv_cmp_ne_u32 vcc, s34, v31\ns_cbranch_vccz 43f\ns_sub_u32 s69, s69, 1\ns_cbranch_scc0 42b\n43:\nmvva 160,48,192\ndt 40,7,19456\n"
"mvva 160,52,208\ndt 42,7,29696\nmvva 164,52,212\ndt 44,7,39936\nmvva 164,48,196\ndt 46,7,50176\nmvva 168,48,200\nsm 24576\n"
"mvva 168,52,216\nlb 22,52,66,0\nmvva 172,52,220\ns_add_u32 s66, s66, s61\nmvva 172,48,204\nwl 8\nmvva 188,56,204\ndt 48,8,19456\n"
"mvva 188,60,220\ndt 50,8,29696\nmvva 184,60,216\ndt 52,8,39936\nmvva 184,56,200\ndt 54,8,50176\nmvva 180,56,196\nmvva 180,60,212\n"
"lc 128,23,67,0\nmvva 176,60,208\nmvva 176,56,192\nwl 8\nmvav 192,192,32\ndt 56,9,19456\nmvav 192,208,36\ndt 58,9,29696\n"
"mvav 196,212,36\ndt 60,9,39936\nmvav 196,196,32\ndt 62,9,50176\nmvav 200,200,32\nmvav 200,216,36\nlc 132,23,67,1024\nmvav 204,220,36\n"
"mvav 204,204,32\nwl 8\nmvav 220,204,40\ndt 32,10,20480\nmvav 220,220,44\ndt 34,10,30720\nmvav 216,216,44\ndt 36,10,40960\n"
"mvav 216,200,40\ndt 38,10,51200\nmvav 212,196,40\nmvav 212,212,44\nlc 136,23,67,2048\nmvav 208,208,44\nmvav 208,192,40\nwl 8\n"
"mvav 224,192,48\ndt 40,11,20480\nmvav 224,208,52\ndt 42,11,30720\nmvav 228,212,52\ndt 44,11,40960\nmvav 228,196,48\ndt 46,11,51200\n"
"mvav 232,200,48\nmvav 232,216,52\nlc 140,23,67,3072\nmvav 236,220,52\ns_add_u32 s67, s67, 4096\nmvav 236,204,48\nwl 8\nmvav 252,204,56\n"
"dt 48,10,25600\nmvav 252,220,60\ndt 50,10,35840\nmvav 248,216,60\ndt 52,10,46080\nmvav 248,200,56\ndt 54,10,56320\nmvav 244,196,56\n"
"mvav 244,212,60\nlc 144,23,67,0\nmvav 240,208,60\nv_lshlrev_b32 v31, 2, v23\nmvav 240,192,56\nbuffer_load_dword a160, v31, s[0:3], 0 offen\ns_add_u32 s0, s0, s86\ns_addc_u32 s1, s1, 0\n"
"wl 8\nmava 64,32,192\ndt 56,11,25600\nmava 64,36,208\ndt 58,11,35840\nmava 68,36,212\ndt 60,11,46080\nmava 68,32,196\n"
"dt 62,11,56320\nmava 72,32,200\nmava 72,36,216\nlc 148,23,67,1024\nmava 76,36,220\ns_barrier\nmava 76,32,204\nwl 8\n"
"mava 92,40,204\ndt 32,6,57344\nmava 92,44,220\ndt 34,12,2048\nmava 88,44,216\ndt 36,12,12288\nmava 88,40,200\ndt 38,12,22528\n"
"mava 84,40,196\nmava 84,44,212\nlc 152,23,67,2048\nmava 80,44,208\nmava 80,40,192\nwl 8\nmava 96,48,192\ndt 40,7,57344\n"
"mava 96,52,208\ndt 42,13,2048\nmava 100,52,212\ndt 44,13,12288\nmava 100,48,196\ndt 46,13,22528\nmava 104,48,200\nmava 104,52,216\n"
"lc 156,23,67,3072\nmava 108,52,220\ns_add_u32 s67, s67, 4096\nmava 108,48,204\nwl 8\nmava 124,56,204\ndt 48,8,57344\nmava 124,60,220\n"
"dt 50,14,2048\nmava 120,60,216\ndt 52,14,12288\nmava 120,56,200\ndt 54,14,22528\nmava 116,56,196\nmava 116,60,212\nmava 112,60,208\n"
"mava 112,56,192\nwb 19,8\nmvva 64,32,224\ndt 56,9,57344\nmvva 64,36,240\ndt 58,15,2048\nmvva 68,36,244\ndt 60,15,12288\n"
"mvva 68,32,228\ndt 62,15,22528\nmvva 72,32,232\nmvva 72,36,248\nmvva 76,36,252\nmvva 76,32,236\nwl 8\nmvva 92,40,236\n"
"dt 32,6,58368\nmvva 92,44,252\ndt 34,12,3072\nmvva 88,44,248\ndt 36,12,13312\nmvva 88,40,232\ndt 38,12,23552\nmvva 84,40,228\n"
"sm 40960\nmvva 84,44,244\nlb 20,36,64,0\nmvva 80,44,240\nmvva 80,40,224\nwl 8\nmvva 96,48,224\ndt 40,7,58368\n"
"mvva 96,52,240\ndt 42,13,3072\nmvva 100,52,244\ndt 44,13,13312\nmvva 100,48,228\ndt 46,13,23552\nmvva 104,48,232\nmvva 104,52,248\n"
"lb 20,40,64,1024\nmvva 108,52,252\ns_add_u32 s64, s64, 229376\nmvva 108,48,236\nwl 8\nmvva 124,56,236\ndt 48,8,58368\nmvva 124,60,252\n"
"dt 50,14,3072\nmvva 120,60,248\ndt 52,14,13312\nmvva 120,56,232\ndt 54,14,23552\nmvva 116,56,228\nmvva 116,60,244\nlb 21,44,65,2048\n"
"mvva 112,60,240\nmvva 112,56,224\nwl 8\nmava 0,32,224\ndt 56,9,58368\nmava 0,36,240\ndt 58,15,3072\nmava 4,36,244\n"
"dt 60,15,13312\nmava 4,32,228\ndt 62,15,23552\nmava 8,32,232\nmava 8,36,248\nlb 21,48,65,3072\nmava 12,36,252\ns_add_u32 s65, s65, s60\n"
"mava 12,32,236\nwl 8\nmava 28,40,236\ndt 32,6,59392\nmava 28,44,252\ndt 34,12,4096\nmava 24,44,248\ndt 36,12,14336\n"
"mava 24,40,232\ndt 38,12,24576\nmava 20,40,228\nsm 45056\nmava 20,44,244\nlb 22,52,66,0\nmava 16,44,240\nv_lshlrev_b32 v31, 2, v23\n"
"s_add_u32 s66, s66, s61\nmava 16,40,224\nbuffer_load_dword a192, v31, s[0:3], 0 offen\ns_add_u32 s0, s0, s86\ns_addc_u32 s1, s1, 0\ns_cmp_eq_u32 s68, s87\ns_cselect_b64 s[0:1], s[94:95], s[0:1]\ns_cselect_b32 s2, 2048, s2\n"
"s_cselect_b32 s86, 2048, s86\nwl 8\nmava 32,48,224\ndt 40,7,59392\nmava 32,52,240\ndt 42,13,4096\nmava 36,52,244\ndt 44,13,14336\n"
"mava 36,48,228\ndt 46,13,24576\nmava 40,48,232\nsm 61440\nmava 40,52,248\nlb 20,36,64,0\nmava 44,52,252\nmava 44,48,236\n"
"wl 8\nmava 60,56,236\ndt 48,8,59392\nmava 60,60,252\ndt 50,14,4096\nmava 56,60,248\ndt 52,14,14336\nmava 56,56,232\n"
"dt 54,14,24576\nmava 52,56,228\nmava 52,60,244\nlb 20,40,64,1024\nmava 48,60,240\ns_add_u32 s64, s64, 229376\nmava 48,56,224\nwl 8\n"
"mvva 128,32,224\ndt 56,9,59392\nmvva 128,36,240\ndt 58,15,4096\nmvva 132,36,244\ndt 60,15,14336\nmvva 132,32,228\ndt 62,15,24576\n"
"mvva 136,32,232\nmvva 136,36,248\nlb 21,44,65,2048\nmvva 140,36,252\nmvva 140,32,236\nwl 8\nmvva 156,40,236\ndt 32,6,60416\n"
"mvva 156,44,252\ndt 34,12,5120\nmvva 152,44,248\ndt 36,12,15360\nmvva 152,40,232\ndt 38,12,25600\nmvva 148,40,228\nmvva 148,44,244\n"
"lb 21,48,65,3072\nmvva 144,44,240\ns_add_u32 s65, s65, s60\nmvva 144,40,224\nwl 8\nmvva 160,48,224\ndt 40,7,60416\nmvva 160,52,240\n"
"dt 42,13,5120\nmvva 164,52,244\ndt 44,13,15360\nmvva 164,48,228\ndt 46,13,25600\nmvva 168,48,232\nsm 65536\nmvva 168,52,248\n"
"lb 22,52,66,0\nmvva 172,52,252\ns_add_u32 s66, s66, s61\nmvva 172,48,236\nwl 8\nmvva 188,56,236\ndt 48,8,60416\nmvva 188,60,252\n"
"dt 50,14,5120\nmvva 184,60,248\ndt 52,14,15360\nmvva 184,56,232\ndt 54,14,25600\nmvva 180,56,228\nmvva 180,60,244\nlc 160,23,67,0\n"
"mvva 176,60,240\nmvva 176,56,224\nwl 8\nmvav 192,224,32\ndt 56,9,60416\nmvav 192,240,36\ndt 58,15,5120\nmvav 196,244,36\n"
"dt 60,15,15360\nmvav 196,228,32\ndt 62,15,25600\nmvav 200,232,32\nmvav 200,248,36\nlc 164,23,67,1024\nmvav 204,252,36\nmvav 204,236,32\n"
"wl 8\nmvav 220,236,40\ndt 32,10,61440\nmvav 220,252,44\ndt 34,16,6144\nmvav 216,248,44\ndt 36,16,16384\nmvav 216,232,40\n"
"dt 38,16,26624\nmvav 212,228,40\nmvav 212,244,44\nlc 168,23,67,2048\nmvav 208,240,44\nmvav 208,224,40\nwl 8\nmvav 224,224,48\n"
"dt 40,11,61440\nmvav 224,240,52\ndt 42,17,6144\nmvav 228,244,52\ndt 44,17,16384\nmvav 228,228,48\ndt 46,17,26624\nmvav 232,232,48\n"
"mvav 232,248,52\nlc 172,23,67,3072\nmvav 236,252,52\ns_add_u32 s67, s67, 4096\nmvav 236,236,48\nwl 8\nmvav 252,236,56\ndt 48,16,1024\n"
"mvav 252,252,60\ndt 50,16,11264\nmvav 248,248,60\ndt 52,16,21504\nmvav 248,232,56\ndt 54,16,31744\nmvav 244,228,56\nmvav 244,244,60\n"
"lc 176,23,67,0\nmvav 240,240,60\nmvav 240,224,56\nwl 8\nmava 64,32,224\ndt 56,17,1024\nmava 64,36,240\ndt 58,17,11264\n"
"mava 68,36,244\ndt 60,17,21504\nmava 68,32,228\ndt 62,17,31744\nmava 72,32,232\nmava 72,36,248\nlc 180,23,67,1024\nmava 76,36,252\n"
"s_waitcnt vmcnt(26)\ns_barrier\nmava 76,32,236\nwl 8\nmava 92,40,236\ndt 32,0,0\nmava 92,44,252\ndt 34,0,10240\n"
"mava 88,44,248\ndt 36,0,20480\nmava 88,40,232\ndt 38,0,30720\nmava 84,40,228\nmava 84,44,244\nlc 184,23,67,2048\nmava 80,44,240\n"
"mava 80,40,224\nwl 8\nmava 96,48,224\ndt 40,1,0\nmava 96,52,240\ndt 42,1,10240\nmava 100,52,244\ndt 44,1,20480\n"
"mava 100,48,228\ndt 46,1,30720\nmava 104,48,232\nmava 104,52,248\nlc 188,23,67,3072\nmava 108,52,252\ns_add_u32 s67, s67, 4096\nmava 108,48,236\n"
"wl 8\nmava 124,56,236\ndt 48,2,0\nmava 124,60,252\ndt 50,2,10240\nmava 120,60,248\ndt 52,2,20480\nmava 120,56,232\n"
"dt 54,2,30720\nmava 116,56,228\nmava 116,60,244\nmava 112,60,240\nmava 112,56,224\ns_sub_u32 s68, s68, 1\ns_cmp_lg_u32 s68, 0\ns_cbranch_scc1 1b\n"
"wb 19,8\nmvva 64,32,128\ns_getpc_b64 s[88:89]\ndt 56,3,0\nmvva 64,36,144\n31:\ndt 58,3,10240\nmvva 68,36,148\n"
"s_and_b32 s89, s89, 0xffff\ndt 60,3,20480\nmvva 68,32,132\ns_mov_b32 s90, 39f-31b\ndt 62,3,30720\nmvva 72,32,136\ns_mov_b32 s91, 0x20000\nmvva 72,36,152\n"
"buffer_load_dword v31, v23, s[88:91], s35 offen\nmvva 76,36,156\nmvva 76,32,140\nwl 8\nmvva 92,40,140\ndt 32,0,1024\nmvva 92,44,156\ndt 34,0,11264\n"
"mvva 88,44,152\ndt 36,0,21504\nmvva 88,40,136\ndt 38,0,31744\nmvva 84,40,132\nsm 81920\nmvva 84,44,148\nlb 20,36,64,0\n"
"mvva 80,44,144\nmvva 80,40,128\nwl 8\nmvva 96,48,128\ndt 40,1,1024\nmvva 96,52,144\ndt 42,1,11264\nmvva 100,52,148\n"
"dt 44,1,21504\nmvva 100,48,132\ndt 46,1,31744\nmvva 104,48,136\nmvva 104,52,152\nlb 20,40,64,1024\nmvva 108,52,156\ns_add_u32 s64, s64, 229376\n"
"mvva 108,48,140\nwl 8\nmvva 124,56,140\ndt 48,2,1024\nmvva 124,60,156\ndt 50,2,11264\nmvva 120,60,152\ndt 52,2,21504\n"
"mvva 120,56,136\ndt 54,2,31744\nmvva 116,56,132\nmvva 116,60,148\nlb 21,44,65,2048\nmvva 112,60,144\nmvva 112,56,128\nwl 8\n"
"mava 0,32,128\ndt 56,3,1024\nmava 0,36,144\ndt 58,3,11264\nmava 4,36,148\ndt 60,3,21504\nmava 4,32,132\ndt 62,3,31744\n"
"mava 8,32,136\nmava 8,36,152\nlb 21,48,65,3072\nmava 12,36,156\ns_add_u32 s65, s65, s60\nmava 12,32,140\nwl 8\nmava 28,40,140\n"
"dt 32,0,2048\nmava 28,44,156\ndt 34,0,12288\nmava 24,44,152\ndt 36,0,22528\nmava 24,40,136\ndt 38,0,32768\nmava 20,40,132\n"
"sm 86016\nmava 20,44,148\nlb 22,52,66,0\nmava 16,44,144\ns_add_u32 s66, s66, s61\nmava 16,40,128\nwl 8\nmava 32,48,128\n"
"dt 40,1,2048\nmava 32,52,144\ndt 42,1,12288\nmava 36,52,148\ndt 44,1,22528\nmava 36,48,132\ndt 46,1,32768\nmava 40,48,136\n"
"sm 102400\nmava 40,52,152\nlb 20,36,64,0\nmava 44,52,156\nmava 44,48,140\nwl 8\nmava 60,56,140\ndt 48,2,2048\n"
"mava 60,60,156\ndt 50,2,12288\nmava 56,60,152\ndt 52,2,22528\nmava 56,56,136\ndt 54,2,32768\nmava 52,56,132\nmava 52,60,148\n"
"lb 20,40,64,1024\nmava 48,60,144\ns_add_u32 s64, s64, 229376\nmava 48,56,128\nwl 8\nmvva 128,32,128\ndt 56,3,2048\nmvva 128,36,144\n"
"dt 58,3,12288\nmvva 132,36,148\ndt 60,3,22528\nmvva 132,32,132\ndt 62,3,32768\nmvva 136,32,136\nmvva 136,36,152\nlb 21,44,65,2048\n"
"mvva 140,36,156\nmvva 140,32,140\nwl 8\nmvva 156,40,140\ndt 32,0,3072\nmvva 156,44,156\ndt 34,0,13312\nmvva 152,44,152\n"
"dt 36,0,23552\nmvva 152,40,136\ndt 38,0,33792\nmvva 148,40,132\nmvva 148,44,148\nlb 21,48,65,3072\nmvva 144,44,144\ns_add_u32 s65, s65, s60\n"
"mvva 144,40,128\nwl 8\nmvva 160,48,128\ndt 40,1,3072\nmvva 160,52,144\ndt 42,1,13312\nmvva 164,52,148\ndt 44,1,23552\n"
"mvva 164,48,132\ndt 46,1,33792\nmvva 168,48,136\nsm 106496\nmvva 168,52,152\nlb 22,52,66,0\nmvva 172,52,156\ns_add_u32 s66, s66, s61\n"
"mvva 172,48,140\nwl 8\nmvva 188,56,140\ndt 48,2,3072\nmvva 188,60,156\ndt 50,2,13312\nmvva 184,60,152\ndt 52,2,23552\n"
"mvva 184,56,136\ndt 54,2,33792\nmvva 180,56,132\nmvva 180,60,148\nlc 192,23,67,0\nmvva 176,60,144\nmvva 176,56,128\nwl 8\n"
"mvav 192,128,32\ndt 56,3,3072\nmvav 192,144,36\ndt 58,3,13312\nmvav 196,148,36\ndt 60,3,23552\nmvav 196,132,32\ndt 62,3,33792\n"
"mvav 200,136,32\nmvav 200,152,36\nlc 196,23,67,1024\nmvav 204,156,36\nmvav 204,140,32\nwl 8\nmvav 220,140,40\ndt 32,4,4096\n"
"mvav 220,156,44\ndt 34,4,14336\nmvav 216,152,44\ndt 36,4,24576\nmvav 216,136,40\ndt 38,4,34816\nmvav 212,132,40\nmvav 212,148,44\n"
"lc 200,23,67,2048\nmvav 208,144,44\nmvav 208,128,40\nwl 8\nmvav 224,128,48\ndt 40,5,4096\nmvav 224,144,52\ndt 42,5,14336\n"
"mvav 228,148,52\ndt 44,5,24576\nmvav 228,132,48\ndt 46,5,34816\nmvav 232,136,48\nmvav 232,152,52\nlc 204,23,67,3072\nmvav 236,156,52\n"
"s_add_u32 s67, s67, 4096\nmvav 236,140,48\nwl 8\nmvav 252,140,56\ndt 48,4,9216\nmvav 252,156,60\ndt 50,4,19456\nmvav 248,152,60\n"
"dt 52,4,29696\nmvav 248,136,56\ndt 54,4,39936\nmvav 244,132,56\nmvav 244,148,60\nlc 208,23,67,0\nmvav 240,144,60\nmvav 240,128,56\n"
"wl 8\nmava 64,32,128\ndt 56,5,9216\nmava 64,36,144\ndt 58,5,19456\nmava 68,36,148\ndt 60,5,29696\nmava 68,32,132\n"
"dt 62,5,39936\nmava 72,32,136\nmava 72,36,152\nlc 212,23,67,1024\nmava 76,36,156\ns_waitcnt vmcnt(25)\ns_barrier\nmava 76,32,140\n"
"wl 8\nmava 92,40,140\ndt 32,0,40960\nmava 92,44,156\ndt 34,0,51200\nmava 88,44,152\ndt 36,0,61440\nmava 88,40,136\n"
"dt 38,6,6144\nmava 84,40,132\nmava 84,44,148\nlc 216,23,67,2048\nmava 80,44,144\nmava 80,40,128\nwl 8\nmava 96,48,128\n"
"dt 40,1,40960\nmava 96,52,144\ndt 42,1,51200\nmava 100,52,148\ndt 44,1,61440\nmava 100,48,132\ndt 46,7,6144\nmava 104,48,136\n"
"mava 104,52,152\nlc 220,23,67,3072\nmava 108,52,156\ns_add_u32 s67, s67, 4096\nmava 108,48,140\nwl 8\nmava 124,56,140\ndt 48,2,40960\n"
"mava 124,60,156\ndt 50,2,51200\nmava 120,60,152\ndt 52,2,61440\nmava 120,56,136\ndt 54,8,6144\nmava 116,56,132\nmava 116,60,148\n"
"mava 112,60,144\nmava 112,56,128\nwb 19,8\nmvva 64,32,160\ndt 56,3,40960\nmvva 64,36,176\ndt 58,3,51200\nmvva 68,36,180\n"
"dt 60,3,61440\nmvva 68,32,164\ndt 62,9,6144\nmvva 72,32,168\nmvva 72,36,184\nmvva 76,36,188\nmvva 76,32,172\nwl 8\n"
"mvva 92,40,172\ndt 32,0,41984\nmvva 92,44,188\ndt 34,0,52224\nmvva 88,44,184\ndt 36,0,62464\nmvva 88,40,168\ndt 38,6,7168\n"
"mvva 84,40,164\nsm 122880\nmvva 84,44,180\nlb 20,36,64,0\nmvva 80,44,176\nmvva 80,40,160\nwl 8\nmvva 96,48,160\n"
"dt 40,1,41984\nmvva 96,52,176\ndt 42,1,52224\nmvva 100,52,180\ndt 44,1,62464\nmvva 100,48,164\ndt 46,7,7168\nmvva 104,48,168\n"
"mvva 104,52,184\nlb 20,40,64,1024\nmvva 108,52,188\ns_movk_i32 s95, 1920\ns_waitcnt vmcnt(20)\nv_lshrrev_b32 v31, 7, v23\ns_add_u32 s64, s64, 229376\nmvva 108,48,172\n"
"v_mad_u32_u24 v31, v31, s95, v23\nwl 8\nmvva 124,56,172\ns_and_b32 s94, s32, 0x700\ns_sub_u32 s88, s8, s94\ns_subb_u32 s89, s9, 0\ndt 48,2,41984\nmvva 124,60,188\n"
"s_mov_b32 s90, 2097152\ns_mov_b32 s91, s11\ndt 50,2,52224\nmvva 120,60,184\ns_and_b32 s94, s76, 0x7c\ns_lshl_b32 s94, s94, 14\ndt 52,2,62464\nmvva 120,56,168\n"
"s_and_b32 s95, s35, 0xc00\ns_lshl_b32 s95, s95, 4\ns_add_u32 s94, s94, s95\ndt 54,8,7168\nmvva 116,56,164\nmvva 116,60,180\nlb 21,44,65,2048\nmvva 112,60,176\n"
"mvva 112,56,160\nbuffer_load_dwordx4 a[136:139], v31, s[88:91], s94 offen offset:384\nwl 8\nmava 0,32,160\ndt 56,3,41984\nmava 0,36,176\ndt 58,3,52224\nmava 4,36,180\n"
"dt 60,3,62464\nmava 4,32,164\ndt 62,9,7168\nmava 8,32,168\nmava 8,36,184\nlb 21,48,65,3072\nmava 12,36,188\ns_add_u32 s65, s65, s60\n"
"mava 12,32,172\nwl 8\nmava 28,40,172\ndt 32,0,43008\nmava 28,44,188\ndt 34,0,53248\nmava 24,44,184\ndt 36,0,63488\n"
"mava 24,40,168\ndt 38,6,8192\nmava 20,40,164\nsm 126976\nmava 20,44,180\nlb 22,52,66,0\nmava 16,44,176\ns_add_u32 s66, s66, s61\n"
"mava 16,40,160\nbuffer_load_dwordx4 a[140:143], v31, s[88:91], s94 offen offset:1408\nwl 8\nmava 32,48,160\ndt 40,1,43008\nmava 32,52,176\ndt 42,1,53248\nmava 36,52,180\n"
"dt 44,1,63488\nmava 36,48,164\ndt 46,7,8192\nmava 40,48,168\nsm 143360\nmava 40,52,184\nlb 20,36,64,0\nmava 44,52,188\n"
"mava 44,48,172\nwl 8\nmava 60,56,172\ndt 48,2,43008\nmava 60,60,188\ndt 50,2,53248\nmava 56,60,184\ndt 52,2,63488\n"
"mava 56,56,168\ndt 54,8,8192\nmava 52,56,164\nmava 52,60,180\nlb 20,40,64,1024\nmava 48,60,176\ns_add_u32 s64, s64, 229376\nmava 48,56,160\n"
"wl 8\nmvva 128,32,160\ndt 56,3,43008\nmvva 128,36,176\ndt 58,3,53248\nmvva 132,36,180\ndt 60,3,63488\nmvva 132,32,164\n"
"dt 62,9,8192\nmvva 136,32,168\nmvva 136,36,184\nlb 21,44,65,2048\nmvva 140,36,188\nmvva 140,32,172\nwl 8\nmvva 156,40,172\n"
"dt 32,0,44032\nmvva 156,44,188\ndt 34,0,54272\nmvva 152,44,184\ndt 36,0,64512\nmvva 152,40,168\ndt 38,6,9216\nmvva 148,40,164\n"
"mvva 148,44,180\nlb 21,48,65,3072\nmvva 144,44,176\ns_add_u32 s65, s65, s60\nmvva 144,40,160\nwl 8\nmvva 160,48,160\ndt 40,1,44032\n"
"mvva 160,52,176\ndt 42,1,54272\nmvva 164,52,180\ndt 44,1,64512\nmvva 164,48,164\ndt 46,7,9216\nmvva 168,48,168\nsm 147456\n"
"mvva 168,52,184\nlb 22,52,66,0\nmvva 172,52,188\ns_add_u32 s66, s66, s61\nmvva 172,48,172\nwl 8\nmvva 188,56,172\ndt 48,2,44032\n"
"mvva 188,60,188\ndt 50,2,54272\nmvva 184,60,184\ndt 52,2,64512\nmvva 184,56,168\ndt 54,8,9216\nmvva 180,56,164\nmvva 180,60,180\n"
"lc 224,23,67,0\nmvva 176,60,176\nmvva 176,56,160\nwl 8\nmvav 192,160,32\ndt 56,3,44032\nmvav 192,176,36\ndt 58,3,54272\n"
"mvav 196,180,36\ndt 60,3,64512\nmvav 196,164,32\ndt 62,9,9216\nmvav 200,168,32\nmvav 200,184,36\nlc 228,23,67,1024\nmvav 204,188,36\n"
"mvav 204,172,32\nwl 8\nmvav 220,172,40\ndt 32,4,45056\nmvav 220,188,44\ndt 34,4,55296\nmvav 216,184,44\ndt 36,10,0\n"
"mvav 216,168,40\ndt 38,10,10240\nmvav 212,164,40\nmvav 212,180,44\nlc 232,23,67,2048\nmvav 208,176,44\nmvav 208,160,40\nwl 8\n"
"mvav 224,160,48\ndt 40,5,45056\nmvav 224,176,52\ndt 42,5,55296\nmvav 228,180,52\ndt 44,11,0\nmvav 228,164,48\ndt 46,11,10240\n"
"mvav 232,168,48\nmvav 232,184,52\nlc 236,23,67,3072\nmvav 236,188,52\ns_add_u32 s67, s67, 4096\nmvav 236,172,48\nwl 8\nmvav 252,172,56\n"
"dt 48,4,50176\nmvav 252,188,60\ndt 50,4,60416\nmvav 248,184,60\ndt 52,10,5120\nmvav 248,168,56\ndt 54,10,15360\nmvav 244,164,56\n"
"mvav 244,180,60\nlc 240,23,67,0\nmvav 240,176,60\nmvav 240,160,56\nwl 8\nmava 64,32,160\ndt 56,5,50176\nmava 64,36,176\n"
"dt 58,5,60416\nmava 68,36,180\ndt 60,11,5120\nmava 68,32,164\ndt 62,11,15360\nmava 72,32,168\nmava 72,36,184\nlc 244,23,67,1024\n"
"mava 76,36,188\ns_waitcnt vmcnt(26)\ns_barrier\nmava 76,32,172\nwl 8\nmava 92,40,172\ndt 32,6,16384\nmava 92,44,188\n"
"dt 34,6,26624\nmava 88,44,184\ndt 36,6,36864\nmava 88,40,168\ndt 38,6,47104\nmava 84,40,164\nmava 84,44,180\nlc 248,23,67,2048\n"
"mava 80,44,176\nmava 80,40,160\nwl 8\nmava 96,48,160\ndt 40,7,16384\nmava 96,52,176\ndt 42,7,26624\nmava 100,52,180\n"
"dt 44,7,36864\nmava 100,48,164\ndt 46,7,47104\nmava 104,48,168\nmava 104,52,184\nlc 252,23,67,3072\nmava 108,52,188\ns_add_u32 s67, s67, 4096\n"
"mava 108,48,172\nwl 8\nmava 124,56,172\ndt 48,8,16384\nmava 124,60,188\ndt 50,8,26624\nmava 120,60,184\ndt 52,8,36864\n"
"mava 120,56,168\ndt 54,8,47104\nmava 116,56,164\nmava 116,60,180\nmava 112,60,176\nmava 112,56,160\nwb 20,8\nmvva 64,32,192\n"
"dt 56,9,16384\nmvva 64,36,208\ndt 58,9,26624\nmvva 68,36,212\ndt 60,9,36864\nmvva 68,32,196\ndt 62,9,47104\nmvva 72,32,200\n"
"mvva 72,36,216\nmvva 76,36,220\nmvva 76,32,204\nwl 8\nmvva 92,40,204\ndt 32,6,17408\nmvva 92,44,220\ndt 34,6,27648\n"
"mvva 88,44,216\ndt 36,6,37888\nmvva 88,40,200\ndt 38,6,48128\nmvva 84,40,196\nmvva 84,44,212\ns_lshr_b32 s86, s35, 14\ns_mul_i32 s86, s86, 131072\n"
"s_and_b32 s87, s35, 16383\ns_lshl_b32 s87, s87, 2\ns_add_u32 s86, s86, s87\nlc 176,23,33,0\nmvva 80,44,208\ns_and_b32 s87, s32, 0x700\ns_sub_u32 s87, 4194304, s87\ns_add_u32 s86, s86, s87\n"
"s_max_i32 s86, s86, 0\nv_lshlrev_b32 v31, 2, v23\nmvva 80,40,192\nbuffer_load_dword a128, v31, s[4:7], s86 offen\nwl 8\nmvva 96,48,192\ndt 40,7,17408\nmvva 96,52,208\n"
"dt 42,7,27648\nmvva 100,52,212\ndt 44,7,37888\nmvva 100,48,196\ndt 46,7,48128\nmvva 104,48,200\nmvva 104,52,216\nlc 180,23,33,1024\n"
"mvva 108,52,220\nmvva 108,48,204\nwl 8\nmvva 124,56,204\ndt 48,8,17408\nmvva 124,60,220\ndt 50,8,27648\nmvva 120,60,216\n"
"dt 52,8,37888\nmvva 120,56,200\ndt 54,8,48128\nmvva 116,56,196\nmvva 116,60,212\nlc 184,23,33,2048\nmvva 112,60,208\nmvva 112,56,192\n"
"wl 8\nmava 0,32,192\ndt 56,9,17408\nmava 0,36,208\ndt 58,9,27648\nmava 4,36,212\ndt 60,9,37888\nmava 4,32,196\n"
"dt 62,9,48128\nmava 8,32,200\nmava 8,36,216\nlc 188,23,33,3072\nmava 12,36,220\nmava 12,32,204\nwl 8\nmava 28,40,204\n"
"dt 32,6,18432\nmava 28,44,220\ndt 34,6,28672\nmava 24,44,216\ndt 36,6,38912\nmava 24,40,200\ns_mov_b32 s65, 4194304\ndt 38,6,49152\n"
"mava 20,40,196\nsm 0\nmava 20,44,212\nlb 18,4,65,0\nmava 16,44,208\nmava 16,40,192\nwl 8\nmava 32,48,192\n"
"dt 40,7,18432\nmava 32,52,208\ndt 42,7,28672\nmava 36,52,212\ndt 44,7,38912\nmava 36,48,196\ndt 46,7,49152\nmava 40,48,200\n"
"sm 1024\nmava 40,52,216\nlb 19,4,65,0\nmava 44,52,220\nmava 44,48,204\nwl 8\nmava 60,56,204\ndt 48,8,18432\n"
"mava 60,60,220\ndt 50,8,28672\nmava 56,60,216\ndt 52,8,38912\nmava 56,56,200\ns_mov_b32 s64, 4325376\ndt 54,8,49152\nmava 52,56,196\n"
"sm 2048\nmava 52,60,212\nlb 18,4,64,0\nmava 48,60,208\nmava 48,56,192\nwl 8\nmvva 128,32,192\ndt 56,9,18432\n"
"mvva 128,36,208\ndt 58,9,28672\nmvva 132,36,212\ndt 60,9,38912\nmvva 132,32,196\ndt 62,9,49152\nmvva 136,32,200\nsm 3072\n"
"mvva 136,36,216\nlb 19,4,64,0\nmvva 140,36,220\nmvva 140,32,204\nwl 8\nmvva 156,40,204\ndt 32,6,19456\nmvva 156,44,220\n"
"dt 34,6,29696\nmvva 152,44,216\ndt 36,6,39936\nmvva 152,40,200\ns_mov_b32 s65, 4456448\ndt 38,6,50176\nmvva 148,40,196\nsm 4096\n"
"mvva 148,44,212\nlb 18,4,65,0\nmvva 144,44,208\nmvva 144,40,192\nwl 8\nmvva 160,48,192\ndt 40,7,19456\nmvva 160,52,208\n"
"dt 42,7,29696\nmvva 164,52,212\ndt 44,7,39936\nmvva 164,48,196\ndt 46,7,50176\nmvva 168,48,200\nsm 20480\nmvva 168,52,216\n"
"lb 19,4,65,0\nmvva 172,52,220\nmvva 172,48,204\nwl 8\nmvva 188,56,204\ndt 48,8,19456\nmvva 188,60,220\ndt 50,8,29696\n"
"mvva 184,60,216\ndt 52,8,39936\nmvva 184,56,200\ns_mov_b32 s64, 4587520\ndt 54,8,50176\nmvva 180,56,196\nsm 21504\nmvva 180,60,212\n"
"s_lshr_b32 s86, s35, 14\ns_mul_i32 s86, s86, 131072\ns_and_b32 s87, s35, 16383\ns_lshl_b32 s87, s87, 2\ns_add_u32 s86, s86, s87\nlb 18,4,64,0\nmvva 176,60,208\ns_and_b32 s87, s32, 0x700\n"
"s_sub_u32 s87, 5242880, s87\ns_add_u32 s86, s86, s87\ns_max_i32 s86, s86, 0\nv_lshlrev_b32 v31, 2, v23\nmvva 176,56,192\nbuffer_load_dword a129, v31, s[4:7], s86 offen\nwl 8\nmvav 192,192,32\n"
"dt 56,9,19456\nmvav 192,208,36\ndt 58,9,29696\nmvav 196,212,36\ndt 60,9,39936\nmvav 196,196,32\ndt 62,9,50176\nmvav 200,200,32\n"
"sm 22528\nmvav 200,216,36\nlb 19,4,64,0\nmvav 204,220,36\nmvav 204,204,32\nwl 8\nmvav 220,204,40\ndt 32,10,20480\n"
"mvav 220,220,44\ndt 34,10,30720\nmvav 216,216,44\ndt 36,10,40960\nmvav 216,200,40\ns_mov_b32 s65, 4718592\ndt 38,10,51200\nmvav 212,196,40\n"
"sm 23552\nmvav 212,212,44\nlb 18,4,65,0\nmvav 208,208,44\nmvav 208,192,40\nwl 8\nmvav 224,192,48\ndt 40,11,20480\n"
"mvav 224,208,52\ndt 42,11,30720\nmvav 228,212,52\ndt 44,11,40960\nmvav 228,196,48\ndt 46,11,51200\nmvav 232,200,48\nsm 24576\n"
"mvav 232,216,52\nlb 19,4,65,0\nmvav 236,220,52\nmvav 236,204,48\nwl 8\nmvav 252,204,56\ndt 48,10,25600\nmvav 252,220,60\n"
"dt 50,10,35840\nmvav 248,216,60\ndt 52,10,46080\nmvav 248,200,56\ndt 54,10,56320\nmvav 244,196,56\nmvav 244,212,60\nmvav 240,208,60\n"
"mvav 240,192,56\nwl 8\nmava 64,32,192\ndt 56,11,25600\nmava 64,36,208\ndt 58,11,35840\nmava 68,36,212\ndt 60,11,46080\n"
"mava 68,32,196\ndt 62,11,56320\nmava 72,32,200\nmava 72,36,216\nmava 76,36,220\ns_waitcnt vmcnt(24)\ns_barrier\nmava 76,32,204\n"
"wl 8\nmava 92,40,204\ndt 32,6,57344\nmava 92,44,220\ndt 34,12,2048\nmava 88,44,216\ndt 36,12,12288\nmava 88,40,200\n"
"dt 38,12,22528\nmava 84,40,196\nmava 84,44,212\nmava 80,44,208\nmava 80,40,192\nwl 8\nmava 96,48,192\ndt 40,7,57344\n"
"mava 96,52,208\ndt 42,13,2048\nmava 100,52,212\ndt 44,13,12288\nmava 100,48,196\ndt 46,13,22528\nmava 104,48,200\nmava 104,52,216\n"
"mava 108,52,220\nmava 108,48,204\nwl 8\nmava 124,56,204\ndt 48,8,57344\nmava 124,60,220\ndt 50,14,2048\nmava 120,60,216\n"
"dt 52,14,12288\nmava 120,56,200\ndt 54,14,22528\nmava 116,56,196\nmava 116,60,212\nmava 112,60,208\nmava 112,56,192\nwb 16,8\n"
"mvva 64,32,224\ndt 56,9,57344\nmvva 64,36,240\ndt 58,15,2048\nmvva 68,36,244\ndt 60,15,12288\nmvva 68,32,228\ndt 62,15,22528\n"
"mvva 72,32,232\nmvva 72,36,248\nmvva 76,36,252\nmvva 76,32,236\nwl 8\nmvva 92,40,236\ndt 32,6,58368\nmvva 92,44,252\n"
"dt 34,12,3072\nmvva 88,44,248\ndt 36,12,13312\nmvva 88,40,232\ns_mov_b32 s64, 4849664\ndt 38,12,23552\nmvva 84,40,228\nsm 40960\n"
"mvva 84,44,244\ns_lshr_b32 s86, s35, 14\ns_mul_i32 s86, s86, 131072\ns_and_b32 s87, s35, 16383\ns_lshl_b32 s87, s87, 2\ns_add_u32 s86, s86, s87\nlb 18,4,64,0\nmvva 80,44,240\n"
"s_and_b32 s87, s32, 0x700\ns_sub_u32 s87, 4259840, s87\ns_add_u32 s86, s86, s87\ns_max_i32 s86, s86, 0\nv_lshlrev_b32 v31, 2, v23\nmvva 80,40,224\nbuffer_load_dword a130, v31, s[4:7], s86 offen\nwl 8\n"
"mvva 96,48,224\ndt 40,7,58368\nmvva 96,52,240\ndt 42,13,3072\nmvva 100,52,244\ndt 44,13,13312\nmvva 100,48,228\ndt 46,13,23552\n"
"mvva 104,48,232\nsm 41984\nmvva 104,52,248\nlb 19,4,64,0\nmvva 108,52,252\nmvva 108,48,236\nwl 8\nmvva 124,56,236\n"
"dt 48,8,58368\nmvva 124,60,252\ndt 50,14,3072\nmvva 120,60,248\ndt 52,14,13312\nmvva 120,56,232\ns_mov_b32 s65, 4980736\ndt 54,14,23552\n"
"mvva 116,56,228\nsm 43008\nmvva 116,60,244\nlb 18,4,65,0\nmvva 112,60,240\nmvva 112,56,224\nwl 8\nmava 0,32,224\n"
"dt 56,9,58368\nmava 0,36,240\ndt 58,15,3072\nmava 4,36,244\ndt 60,15,13312\nmava 4,32,228\ndt 62,15,23552\nmava 8,32,232\n"
"sm 44032\nmava 8,36,248\nlb 19,4,65,0\nmava 12,36,252\nmava 12,32,236\nwl 8\nmava 28,40,236\ndt 32,6,59392\n"
"mava 28,44,252\ndt 34,12,4096\nmava 24,44,248\ndt 36,12,14336\nmava 24,40,232\ns_mov_b32 s64, 5111808\ndt 38,12,24576\nmava 20,40,228\n"
"sm 45056\nmava 20,44,244\nlb 18,4,64,0\nmava 16,44,240\nmava 16,40,224\nwl 8\nmava 32,48,224\ndt 40,7,59392\n"
"mava 32,52,240\ndt 42,13,4096\nmava 36,52,244\ndt 44,13,14336\nmava 36,48,228\ndt 46,13,24576\nmava 40,48,232\nmava 40,52,248\n"
"mava 44,52,252\nmava 44,48,236\nwl 8\nmava 60,56,236\ndt 48,8,59392\nmava 60,60,252\ndt 50,14,4096\nmava 56,60,248\n"
"dt 52,14,14336\nmava 56,56,232\ndt 54,14,24576\nmava 52,56,228\nmava 52,60,244\nmava 48,60,240\nmava 48,56,224\nwl 8\n"
"mvva 128,32,224\ndt 56,9,59392\nmvva 128,36,240\ns_and_b32 s94, s76, 0x7c\ns_lshl_b32 s94, s94, 14\ndt 58,15,4096\nmvva 132,36,244\ns_and_b32 s95, s76, 0x700\n"
"s_lshl_b32 s95, s95, 5\ns_add_u32 s94, s94, s95\ndt 60,15,14336\nmvva 132,32,228\ns_and_b32 s95, s35, 0xc00\ns_add_u32 s94, s94, s95\ns_add_u32 s94, s94, 37748736\ndt 62,15,24576\n"
"mvva 136,32,232\nmvva 136,36,248\nmvva 140,36,252\nmvva 140,32,236\nbuffer_store_dwordx4 a[136:139], v23, s[56:59], s94 offen sc1\nwl 8\nmvva 156,40,236\ndt 32,6,60416\n"
"mvva 156,44,252\ndt 34,12,5120\nmvva 152,44,248\ndt 36,12,15360\nmvva 152,40,232\ndt 38,12,25600\nmvva 148,40,228\nmvva 148,44,244\n"
"mvva 144,44,240\nmvva 144,40,224\nwl 8\nmvva 160,48,224\ndt 40,7,60416\nmvva 160,52,240\ndt 42,13,5120\nmvva 164,52,244\n"
"dt 44,13,15360\nmvva 164,48,228\ns_add_u32 s94, s94, 4096\ndt 46,13,25600\nmvva 168,48,232\nmvva 168,52,248\nmvva 172,52,252\nmvva 172,48,236\n"
"buffer_store_dwordx4 a[140:143], v23, s[56:59], s94 offen sc1\nwl 8\nmvva 188,56,236\ndt 48,8,60416\nmvva 188,60,252\ndt 50,14,5120\nmvva 184,60,248\ndt 52,14,15360\n"
"mvva 184,56,232\ndt 54,14,25600\nmvva 180,56,228\nmvva 180,60,244\ns_lshr_b32 s86, s35, 14\ns_mul_i32 s86, s86, 131072\ns_and_b32 s87, s35, 16383\ns_lshl_b32 s87, s87, 2\n"
"s_add_u32 s86, s86, s87\nmvva 176,60,240\ns_and_b32 s87, s32, 0x700\ns_sub_u32 s87, 5308416, s87\ns_add_u32 s86, s86, s87\ns_max_i32 s86, s86, 0\nv_lshlrev_b32 v31, 2, v23\nmvva 176,56,224\n"
"buffer_load_dword a131, v31, s[4:7], s86 offen\nwl 8\nmvav 192,224,32\ndt 56,9,60416\nmvav 192,240,36\ndt 58,15,5120\nmvav 196,244,36\ndt 60,15,15360\n"
"mvav 196,228,32\ndt 62,15,25600\nmvav 200,232,32\nmvav 200,248,36\nmvav 204,252,36\nmvav 204,236,32\nwl 8\nmvav 220,236,40\n"
"dt 32,10,61440\nmvav 220,252,44\ndt 34,16,6144\nmvav 216,248,44\ndt 36,16,16384\nmvav 216,232,40\ndt 38,16,26624\nmvav 212,228,40\n"
"mvav 212,244,44\nmvav 208,240,44\nmvav 208,224,40\nwl 8\nmvav 224,224,48\ndt 40,11,61440\nmvav 224,240,52\ndt 42,17,6144\n"
"mvav 228,244,52\ndt 44,17,16384\nmvav 228,228,48\ndt 46,17,26624\nmvav 232,232,48\nmvav 232,248,52\nmvav 236,252,52\nmvav 236,236,48\n"
"wl 8\nmvav 252,236,56\ndt 48,16,1024\nmvav 252,252,60\ndt 50,16,11264\nmvav 248,248,60\ndt 52,16,21504\nmvav 248,232,56\n"
"dt 54,16,31744\nmvav 244,228,56\nmvav 244,244,60\nmvav 240,240,60\nmvav 240,224,56\nwl 8\nmava 64,32,224\ndt 56,17,1024\n"
"mava 64,36,240\ndt 58,17,11264\nmava 68,36,244\ndt 60,17,21504\nmava 68,32,228\ndt 62,17,31744\nmava 72,32,232\nmava 72,36,248\n"
"mava 76,36,252\nmava 76,32,236\nwl 8\nmava 92,40,236\nmava 92,44,252\nmava 88,44,248\nmava 88,40,232\nmava 84,40,228\n"
"mava 84,44,244\nmava 80,44,240\nmava 80,40,224\nwl 4\nmava 96,48,224\nmava 96,52,240\nmava 100,52,244\nmava 100,48,228\n"
"mava 104,48,232\nmava 104,52,248\nmava 108,52,252\nmava 108,48,236\nwl 0\nmava 124,56,236\nmava 124,60,252\nmava 120,60,248\n"
"mava 120,56,232\nmava 116,56,228\nmava 116,60,244\nmava 112,60,240\nmava 112,56,224\nv_lshrrev_b32 v21, 7, v23\nv_cmp_eq_u32 vcc, 3, v21\nv_add_u32 v21, 37748864, v23\n"
"s_nop 0\nv_cndmask_b32 v21, v23, v21, vcc\ns_add_u32 s86, s72, 4096\nv_mov_b32 v31, 0x3f803f80\nv_accvgpr_write_b32 a240, v31\nv_accvgpr_write_b32 a241, v31\nv_accvgpr_write_b32 a242, v31\nv_accvgpr_write_b32 a243, v31\n"
"v_pk_mul_f32 v[64:65], v[64:65], s[82:83]\nv_pk_mul_f32 v[66:67], v[66:67], s[82:83]\nv_pk_mul_f32 v[80:81], v[80:81], s[82:83]\nv_pk_mul_f32 v[82:83], v[82:83], s[82:83]\nve 64\nve 65\nve 66\nve 67\n"
"ve 80\nve 81\nve 82\nve 83\nv_pk_add_f32 v[64:65], v[64:65], s[84:85]\nv_pk_add_f32 v[66:67], v[66:67], s[84:85]\nv_pk_add_f32 v[80:81], v[80:81], s[84:85]\nv_pk_add_f32 v[82:83], v[82:83], s[84:85]\n"
"vr 64\nvr 65\nvr 66\nvr 67\nvr 80\nvr 81\nvr 82\nvr 83\n"
"ar 32,0\nar 33,1\nar 34,2\nar 35,3\nar 36,16\nar 37,17\nar 38,18\nar 39,19\n"
"v_pk_mul_f32 v[64:65], v[64:65], v[32:33]\nv_pk_mul_f32 v[66:67], v[66:67], v[34:35]\nv_pk_mul_f32 v[80:81], v[80:81], v[36:37]\nv_pk_mul_f32 v[82:83], v[82:83], v[38:39]\ncv 64,64,65\ncv 65,66,67\ncv 66,80,81\ncv 67,82,83\n"
"buffer_store_dwordx4 v[64:67], v21, s[56:59], s72 offen offset:0 sc1\nv_pk_mul_f32 v[68:69], v[68:69], s[82:83]\nv_pk_mul_f32 v[70:71], v[70:71], s[82:83]\nv_pk_mul_f32 v[84:85], v[84:85], s[82:83]\nv_pk_mul_f32 v[86:87], v[86:87], s[82:83]\nve 68\nve 69\nve 70\n"
"ve 71\nve 84\nve 85\nve 86\nve 87\nv_pk_add_f32 v[68:69], v[68:69], s[84:85]\nv_pk_add_f32 v[70:71], v[70:71], s[84:85]\nv_pk_add_f32 v[84:85], v[84:85], s[84:85]\n"
"v_pk_add_f32 v[86:87], v[86:87], s[84:85]\nvr 68\nvr 69\nvr 70\nvr 71\nvr 84\nvr 85\nvr 86\n"
"vr 87\nar 40,4\nar 41,5\nar 42,6\nar 43,7\nar 44,20\nar 45,21\nar 46,22\n"
"ar 47,23\nv_pk_mul_f32 v[68:69], v[68:69], v[40:41]\nv_pk_mul_f32 v[70:71], v[70:71], v[42:43]\nv_pk_mul_f32 v[84:85], v[84:85], v[44:45]\nv_pk_mul_f32 v[86:87], v[86:87], v[46:47]\ncv 68,68,69\ncv 69,70,71\ncv 70,84,85\n"
"cv 71,86,87\nbuffer_store_dwordx4 v[68:71], v21, s[56:59], s72 offen offset:1024 sc1\nv_pk_mul_f32 v[72:73], v[72:73], s[82:83]\nv_pk_mul_f32 v[74:75], v[74:75], s[82:83]\nv_pk_mul_f32 v[88:89], v[88:89], s[82:83]\nv_pk_mul_f32 v[90:91], v[90:91], s[82:83]\nve 72\nve 73\n"
"ve 74\nve 75\nve 88\nve 89\nve 90\nve 91\nv_pk_add_f32 v[72:73], v[72:73], s[84:85]\nv_pk_add_f32 v[74:75], v[74:75], s[84:85]\n"
"v_pk_add_f32 v[88:89], v[88:89], s[84:85]\nv_pk_add_f32 v[90:91], v[90:91], s[84:85]\nvr 72\nvr 73\nvr 74\nvr 75\nvr 88\nvr 89\n"
"vr 90\nvr 91\nar 48,8\nar 49,9\nar 50,10\nar 51,11\nar 52,24\nar 53,25\n"
"ar 54,26\nar 55,27\nv_pk_mul_f32 v[72:73], v[72:73], v[48:49]\nv_pk_mul_f32 v[74:75], v[74:75], v[50:51]\nv_pk_mul_f32 v[88:89], v[88:89], v[52:53]\nv_pk_mul_f32 v[90:91], v[90:91], v[54:55]\ncv 72,72,73\ncv 73,74,75\n"
"cv 74,88,89\ncv 75,90,91\nbuffer_store_dwordx4 v[72:75], v21, s[56:59], s72 offen offset:2048 sc1\nv_pk_mul_f32 v[76:77], v[76:77], s[82:83]\nv_pk_mul_f32 v[78:79], v[78:79], s[82:83]\nv_pk_mul_f32 v[92:93], v[92:93], s[82:83]\nv_pk_mul_f32 v[94:95], v[94:95], s[82:83]\nve 76\n"
"ve 77\nve 78\nve 79\nve 92\nve 93\nve 94\nve 95\nv_pk_add_f32 v[76:77], v[76:77], s[84:85]\n"
"v_pk_add_f32 v[78:79], v[78:79], s[84:85]\nv_pk_add_f32 v[92:93], v[92:93], s[84:85]\nv_pk_add_f32 v[94:95], v[94:95], s[84:85]\nvr 76\nvr 77\nvr 78\nvr 79\nvr 92\n"
"vr 93\nvr 94\nvr 95\nar 56,12\nar 57,13\nar 58,14\nar 59,15\nar 60,28\n"
"ar 61,29\nar 62,30\nar 63,31\nv_pk_mul_f32 v[76:77], v[76:77], v[56:57]\nv_pk_mul_f32 v[78:79], v[78:79], v[58:59]\nv_pk_mul_f32 v[92:93], v[92:93], v[60:61]\nv_pk_mul_f32 v[94:95], v[94:95], v[62:63]\ncv 76,76,77\n"
"cv 77,78,79\ncv 78,92,93\ncv 79,94,95\nbuffer_store_dwordx4 v[76:79], v21, s[56:59], s72 offen offset:3072 sc1\ns_barrier\nv_pk_mul_f32 v[96:97], v[96:97], s[82:83]\nv_pk_mul_f32 v[98:99], v[98:99], s[82:83]\nv_pk_mul_f32 v[112:113], v[112:113], s[82:83]\n"
"v_pk_mul_f32 v[114:115], v[114:115], s[82:83]\nve 96\nve 97\nve 98\nve 99\nve 112\nve 113\nve 114\n"
"ve 115\nv_pk_add_f32 v[96:97], v[96:97], s[84:85]\nv_pk_add_f32 v[98:99], v[98:99], s[84:85]\nv_pk_add_f32 v[112:113], v[112:113], s[84:85]\nv_pk_add_f32 v[114:115], v[114:115], s[84:85]\nvr 96\nvr 97\nvr 98\n"
"vr 99\nvr 112\nvr 113\nvr 114\nvr 115\nar 32,32\nar 33,33\nar 34,34\n"
"ar 35,35\nar 36,48\nar 37,49\nar 38,50\nar 39,51\nv_pk_mul_f32 v[96:97], v[96:97], v[32:33]\nv_pk_mul_f32 v[98:99], v[98:99], v[34:35]\nv_pk_mul_f32 v[112:113], v[112:113], v[36:37]\n"
"v_pk_mul_f32 v[114:115], v[114:115], v[38:39]\ncv 96,96,97\ncv 97,98,99\ncv 98,112,113\ncv 99,114,115\nbuffer_store_dwordx4 v[96:99], v21, s[56:59], s86 offen offset:0 sc1\nv_pk_mul_f32 v[100:101], v[100:101], s[82:83]\nv_pk_mul_f32 v[102:103], v[102:103], s[82:83]\n"
"v_pk_mul_f32 v[116:117], v[116:117], s[82:83]\nv_pk_mul_f32 v[118:119], v[118:119], s[82:83]\nve 100\nve 101\nve 102\nve 103\nve 116\nve 117\n"
"ve 118\nve 119\nv_pk_add_f32 v[100:101], v[100:101], s[84:85]\nv_pk_add_f32 v[102:103], v[102:103], s[84:85]\nv_pk_add_f32 v[116:117], v[116:117], s[84:85]\nv_pk_add_f32 v[118:119], v[118:119], s[84:85]\nvr 100\nvr 101\n"
"vr 102\nvr 103\nvr 116\nvr 117\nvr 118\nvr 119\nar 40,36\nar 41,37\n"
"ar 42,38\nar 43,39\nar 44,52\nar 45,53\nar 46,54\nar 47,55\nv_pk_mul_f32 v[100:101], v[100:101], v[40:41]\nv_pk_mul_f32 v[102:103], v[102:103], v[42:43]\n"
"v_pk_mul_f32 v[116:117], v[116:117], v[44:45]\nv_pk_mul_f32 v[118:119], v[118:119], v[46:47]\ncv 100,100,101\ncv 101,102,103\ncv 102,116,117\ncv 103,118,119\nbuffer_store_dwordx4 v[100:103], v21, s[56:59], s86 offen offset:1024 sc1\nv_pk_mul_f32 v[104:105], v[104:105], s[82:83]\n"
"v_pk_mul_f32 v[106:107], v[106:107], s[82:83]\nv_pk_mul_f32 v[120:121], v[120:121], s[82:83]\nv_pk_mul_f32 v[122:123], v[122:123], s[82:83]\nve 104\nve 105\nve 106\nve 107\nve 120\n"
"ve 121\nve 122\nve 123\nv_pk_add_f32 v[104:105], v[104:105], s[84:85]\nv_pk_add_f32 v[106:107], v[106:107], s[84:85]\nv_pk_add_f32 v[120:121], v[120:121], s[84:85]\nv_pk_add_f32 v[122:123], v[122:123], s[84:85]\nvr 104\n"
"vr 105\nvr 106\nvr 107\nvr 120\nvr 121\nvr 122\nvr 123\nar 48,40\n"
"ar 49,41\nar 50,42\nar 51,43\nar 52,56\nar 53,57\nar 54,58\nar 55,59\nv_pk_mul_f32 v[104:105], v[104:105], v[48:49]\n"
"v_pk_mul_f32 v[106:107], v[106:107], v[50:51]\nv_pk_mul_f32 v[120:121], v[120:121], v[52:53]\nv_pk_mul_f32 v[122:123], v[122:123], v[54:55]\ncv 104,104,105\ncv 105,106,107\ncv 106,120,121\ncv 107,122,123\nbuffer_store_dwordx4 v[104:107], v21, s[56:59], s86 offen offset:2048 sc1\n"
"v_pk_mul_f32 v[108:109], v[108:109], s[82:83]\nv_pk_mul_f32 v[110:111], v[110:111], s[82:83]\nv_pk_mul_f32 v[124:125], v[124:125], s[82:83]\nv_pk_mul_f32 v[126:127], v[126:127], s[82:83]\nve 108\nve 109\nve 110\nve 111\n"
"ve 124\nve 125\nve 126\nve 127\nv_pk_add_f32 v[108:109], v[108:109], s[84:85]\nv_pk_add_f32 v[110:111], v[110:111], s[84:85]\nv_pk_add_f32 v[124:125], v[124:125], s[84:85]\nv_pk_add_f32 v[126:127], v[126:127], s[84:85]\n"
"vr 108\nvr 109\nvr 110\nvr 111\nvr 124\nvr 125\nvr 126\nvr 127\n"
"ar 56,44\nar 57,45\nar 58,46\nar 59,47\nar 60,60\nar 61,61\nar 62,62\nar 63,63\n"
"v_pk_mul_f32 v[108:109], v[108:109], v[56:57]\nv_pk_mul_f32 v[110:111], v[110:111], v[58:59]\nv_pk_mul_f32 v[124:125], v[124:125], v[60:61]\nv_pk_mul_f32 v[126:127], v[126:127], v[62:63]\ncv 108,108,109\ncv 109,110,111\ncv 110,124,125\ncv 111,126,127\n"
"buffer_store_dwordx4 v[108:111], v21, s[56:59], s86 offen offset:3072 sc1\nar 64,64\nar 65,65\nar 66,66\nar 67,67\nar 68,80\nar 69,81\nar 70,82\n"
"ar 71,83\nv_pk_mul_f32 v[64:65], v[64:65], s[82:83]\nv_pk_mul_f32 v[66:67], v[66:67], s[82:83]\nv_pk_mul_f32 v[68:69], v[68:69], s[82:83]\nv_pk_mul_f32 v[70:71], v[70:71], s[82:83]\nve 64\nve 65\nve 66\n"
"ve 67\nve 68\nve 69\nve 70\nve 71\nv_pk_add_f32 v[64:65], v[64:65], s[84:85]\nv_pk_add_f32 v[66:67], v[66:67], s[84:85]\nv_pk_add_f32 v[68:69], v[68:69], s[84:85]\n"
"v_pk_add_f32 v[70:71], v[70:71], s[84:85]\nvr 64\nvr 65\nvr 66\nvr 67\nvr 68\nvr 69\nvr 70\n"
"vr 71\nar 32,96\nar 33,97\nar 34,98\nar 35,99\nar 36,112\nar 37,113\nar 38,114\n"
"ar 39,115\nv_pk_mul_f32 v[64:65], v[64:65], v[32:33]\nv_pk_mul_f32 v[66:67], v[66:67], v[34:35]\nv_pk_mul_f32 v[68:69], v[68:69], v[36:37]\nv_pk_mul_f32 v[70:71], v[70:71], v[38:39]\ncv 64,64,65\ncv 65,66,67\ncv 66,68,69\n"
"cv 67,70,71\nbuffer_store_dwordx4 v[64:67], v21, s[56:59], s73 offen offset:0 sc1\nar 72,68\nar 73,69\nar 74,70\nar 75,71\nar 76,84\nar 77,85\n"
"ar 78,86\nar 79,87\nv_pk_mul_f32 v[72:73], v[72:73], s[82:83]\nv_pk_mul_f32 v[74:75], v[74:75], s[82:83]\nv_pk_mul_f32 v[76:77], v[76:77], s[82:83]\nv_pk_mul_f32 v[78:79], v[78:79], s[82:83]\nve 72\nve 73\n"
"ve 74\nve 75\nve 76\nve 77\nve 78\nve 79\nv_pk_add_f32 v[72:73], v[72:73], s[84:85]\nv_pk_add_f32 v[74:75], v[74:75], s[84:85]\n"
"v_pk_add_f32 v[76:77], v[76:77], s[84:85]\nv_pk_add_f32 v[78:79], v[78:79], s[84:85]\nvr 72\nvr 73\nvr 74\nvr 75\nvr 76\nvr 77\n"
"vr 78\nvr 79\nar 40,100\nar 41,101\nar 42,102\nar 43,103\nar 44,116\nar 45,117\n"
"ar 46,118\nar 47,119\nv_pk_mul_f32 v[72:73], v[72:73], v[40:41]\nv_pk_mul_f32 v[74:75], v[74:75], v[42:43]\nv_pk_mul_f32 v[76:77], v[76:77], v[44:45]\nv_pk_mul_f32 v[78:79], v[78:79], v[46:47]\ncv 72,72,73\ncv 73,74,75\n"
"cv 74,76,77\ncv 75,78,79\nbuffer_store_dwordx4 v[72:75], v21, s[56:59], s73 offen offset:1024 sc1\nar 80,72\nar 81,73\nar 82,74\nar 83,75\nar 84,88\n"
"ar 85,89\nar 86,90\nar 87,91\nv_pk_mul_f32 v[80:81], v[80:81], s[82:83]\nv_pk_mul_f32 v[82:83], v[82:83], s[82:83]\nv_pk_mul_f32 v[84:85], v[84:85], s[82:83]\nv_pk_mul_f32 v[86:87], v[86:87], s[82:83]\nve 80\n"
"ve 81\nve 82\nve 83\nve 84\nve 85\nve 86\nve 87\nv_pk_add_f32 v[80:81], v[80:81], s[84:85]\n"
"v_pk_add_f32 v[82:83], v[82:83], s[84:85]\nv_pk_add_f32 v[84:85], v[84:85], s[84:85]\nv_pk_add_f32 v[86:87], v[86:87], s[84:85]\nvr 80\nvr 81\nvr 82\nvr 83\nvr 84\n"
"vr 85\nvr 86\nvr 87\nar 48,104\nar 49,105\nar 50,106\nar 51,107\nar 52,120\n"
"ar 53,121\nar 54,122\nar 55,123\nv_pk_mul_f32 v[80:81], v[80:81], v[48:49]\nv_pk_mul_f32 v[82:83], v[82:83], v[50:51]\nv_pk_mul_f32 v[84:85], v[84:85], v[52:53]\nv_pk_mul_f32 v[86:87], v[86:87], v[54:55]\ncv 80,80,81\n"
"cv 81,82,83\ncv 82,84,85\ncv 83,86,87\nbuffer_store_dwordx4 v[80:83], v21, s[56:59], s73 offen offset:2048 sc1\nar 88,76\nar 89,77\nar 90,78\nar 91,79\n"
"ar 92,92\nar 93,93\nar 94,94\nar 95,95\nv_pk_mul_f32 v[88:89], v[88:89], s[82:83]\nv_pk_mul_f32 v[90:91], v[90:91], s[82:83]\nv_pk_mul_f32 v[92:93], v[92:93], s[82:83]\nv_pk_mul_f32 v[94:95], v[94:95], s[82:83]\n"
"ve 88\nve 89\nve 90\nve 91\nve 92\nve 93\nve 94\nve 95\n"
"v_pk_add_f32 v[88:89], v[88:89], s[84:85]\nv_pk_add_f32 v[90:91], v[90:91], s[84:85]\nv_pk_add_f32 v[92:93], v[92:93], s[84:85]\nv_pk_add_f32 v[94:95], v[94:95], s[84:85]\nvr 88\nvr 89\nvr 90\nvr 91\n"
"vr 92\nvr 93\nvr 94\nvr 95\nar 56,108\nar 57,109\nar 58,110\nar 59,111\n"
"ar 60,124\nar 61,125\nar 62,126\nar 63,127\nv_pk_mul_f32 v[88:89], v[88:89], v[56:57]\nv_pk_mul_f32 v[90:91], v[90:91], v[58:59]\nv_pk_mul_f32 v[92:93], v[92:93], v[60:61]\nv_pk_mul_f32 v[94:95], v[94:95], v[62:63]\n"
"cv 88,88,89\ncv 89,90,91\ncv 90,92,93\ncv 91,94,95\nbuffer_store_dwordx4 v[88:91], v21, s[56:59], s73 offen offset:3072 sc1\ncv 192,192,193\ncv 193,194,195\ncv 194,196,197\n"
"cv 195,198,199\nds_write_b128 v26, v[192:195] offset:0\ncv 200,200,201\ncv 201,202,203\ncv 202,204,205\ncv 203,206,207\nds_write_b128 v26, v[200:203] offset:1024\ncv 208,208,209\n"
"cv 209,210,211\ncv 210,212,213\ncv 211,214,215\nds_write_b128 v26, v[208:211] offset:8192\ncv 216,216,217\ncv 217,218,219\ncv 218,220,221\ncv 219,222,223\n"
"ds_write_b128 v26, v[216:219] offset:9216\ncv 224,224,225\ncv 225,226,227\ncv 226,228,229\ncv 227,230,231\nds_write_b128 v26, v[224:227] offset:16384\ncv 232,232,233\ncv 233,234,235\n"
"cv 234,236,237\ncv 235,238,239\nds_write_b128 v26, v[232:235] offset:17408\ncv 240,240,241\ncv 241,242,243\ncv 242,244,245\ncv 243,246,247\nds_write_b128 v26, v[240:243] offset:24576\n"
"cv 248,248,249\ncv 249,250,251\ncv 250,252,253\ncv 251,254,255\nds_write_b128 v26, v[248:251] offset:25600\ncv 96,128,129\ncv 97,130,131\ncv 98,144,145\n"
"cv 99,146,147\ncv 112,132,133\ncv 113,134,135\ns_waitcnt vmcnt(36)\nzvav 128,176,96\ncv 114,148,149\ncv 115,150,151\ns_waitcnt vmcnt(34)\n"
"zvav 144,180,96\nzvav 132,176,112\nzvav 148,180,112\ns_nop 1\nv_max3_f32 v104, v128, v129, v130\nv_max3_f32 v105, -v128, -v129, -v130\ns_nop 1\nv_max3_f32 v104, v104, v131, v144\n"
"v_max3_f32 v104, v104, v145, v146\nv_max_f32 v104, v104, v147\nv_max3_f32 v105, v105, -v131, -v144\nv_max3_f32 v105, v105, -v145, -v146\nv_max_f32 v105, v105, -v147\nv_max3_f32 v120, v132, v133, v134\nv_max3_f32 v120, v120, v135, v148\nv_permlane32_swap_b32 v104, v105\n"
"v_max_f32 v104, v104, v105\nv_mov_b32 v105, v104\nv_max3_f32 v120, v120, v149, v150\nv_max_f32 v120, v120, v151\nv_permlane16_swap_b32 v104, v105\nv_max_f32 v104, v104, v105\nv_mul_f32 v105, s82, v104\nv_mul_f32 v104, s82, v104\n"
"v_max3_f32 v121, -v132, -v133, -v134\nv_max3_f32 v121, v121, -v135, -v148\nv_permlane32_swap_b32 v104, v105\nv_pk_fma_f32 v[96:97], v[128:129], s[82:83], v[104:105] op_sel:[0,0,1] op_sel_hi:[1,1,1]\nv_pk_fma_f32 v[98:99], v[130:131], s[82:83], v[104:105] op_sel:[0,0,1] op_sel_hi:[1,1,1]\nv_pk_fma_f32 v[100:101], v[144:145], s[82:83], v[104:105] op_sel:[0,0,1] op_sel_hi:[1,1,1]\nv_pk_fma_f32 v[102:103], v[146:147], s[82:83], v[104:105] op_sel:[0,0,1] op_sel_hi:[1,1,1]\nv_pk_fma_f32 v[128:129], v[128:129], s[80:81], v[104:105] op_sel:[0,0,0] op_sel_hi:[1,1,0]\n"
"v_pk_fma_f32 v[130:131], v[130:131], s[80:81], v[104:105] op_sel:[0,0,0] op_sel_hi:[1,1,0]\nv_pk_fma_f32 v[144:145], v[144:145], s[80:81], v[104:105] op_sel:[0,0,0] op_sel_hi:[1,1,0]\nv_pk_fma_f32 v[146:147], v[146:147], s[80:81], v[104:105] op_sel:[0,0,0] op_sel_hi:[1,1,0]\nve 128\nve 129\nve 130\nve 131\nve 144\n"
"ve 145\nve 146\nve 147\nve 96\nve 97\nve 98\nve 99\nve 100\n"
"ve 101\nve 102\nve 103\nv_pk_add_f32 v[106:107], v[128:129], v[130:131]\nv_pk_add_f32 v[108:109], v[144:145], v[146:147]\nv_pk_add_f32 v[106:107], v[106:107], v[108:109]\nv_add_f32 v104, v106, v107\nv_pk_add_f32 v[106:107], v[96:97], v[98:99]\n"
"v_pk_add_f32 v[108:109], v[100:101], v[102:103]\nv_pk_add_f32 v[106:107], v[106:107], v[108:109]\nv_add_f32 v105, v106, v107\nv_max3_f32 v121, v121, -v149, -v150\nv_max_f32 v121, v121, -v151\nv_permlane32_swap_b32 v104, v105\nv_add_f32 v104, v104, v105\nv_mov_b32 v105, v104\n"
"v_permlane32_swap_b32 v120, v121\nv_max_f32 v120, v120, v121\nv_permlane16_swap_b32 v104, v105\nv_add_f32 v104, v104, v105\nvr 104\nv_mov_b32 v121, v120\nv_mov_b32 v105, v104\ns_nop 0\n"
"v_permlane16_swap_b32 v120, v121\nv_permlane32_swap_b32 v104, v105\nv_pk_mul_f32 v[128:129], v[128:129], v[104:105] op_sel:[0,0] op_sel_hi:[1,0]\nv_pk_mul_f32 v[130:131], v[130:131], v[104:105] op_sel:[0,0] op_sel_hi:[1,0]\nv_pk_mul_f32 v[144:145], v[144:145], v[104:105] op_sel:[0,0] op_sel_hi:[1,0]\nv_pk_mul_f32 v[146:147], v[146:147], v[104:105] op_sel:[0,0] op_sel_hi:[1,0]\nv_pk_mul_f32 v[96:97], v[96:97], v[104:105] op_sel:[0,1] op_sel_hi:[1,1]\nv_pk_mul_f32 v[98:99], v[98:99], v[104:105] op_sel:[0,1] op_sel_hi:[1,1]\n"
"v_pk_mul_f32 v[100:101], v[100:101], v[104:105] op_sel:[0,1] op_sel_hi:[1,1]\nv_pk_mul_f32 v[102:103], v[102:103], v[104:105] op_sel:[0,1] op_sel_hi:[1,1]\ncv 128,128,129\ncv 129,130,131\nds_write_b64 v24, v[128:129] offset:0\ncv 144,144,145\ncv 145,146,147\nds_write_b64 v24, v[144:145] offset:512\n"
"cv 96,96,97\ncv 97,98,99\nds_write_b64 v24, v[96:97] offset:1024\ncv 100,100,101\ncv 101,102,103\nds_write_b64 v24, v[100:101] offset:1536\nv_max_f32 v120, v120, v121\nv_mul_f32 v121, s82, v120\n"
"v_mul_f32 v120, s82, v120\ns_nop 1\nv_permlane32_swap_b32 v120, v121\nv_pk_fma_f32 v[112:113], v[132:133], s[82:83], v[120:121] op_sel:[0,0,1] op_sel_hi:[1,1,1]\nv_pk_fma_f32 v[114:115], v[134:135], s[82:83], v[120:121] op_sel:[0,0,1] op_sel_hi:[1,1,1]\nv_pk_fma_f32 v[116:117], v[148:149], s[82:83], v[120:121] op_sel:[0,0,1] op_sel_hi:[1,1,1]\nv_pk_fma_f32 v[118:119], v[150:151], s[82:83], v[120:121] op_sel:[0,0,1] op_sel_hi:[1,1,1]\nv_pk_fma_f32 v[132:133], v[132:133], s[80:81], v[120:121] op_sel:[0,0,0] op_sel_hi:[1,1,0]\n"
"v_pk_fma_f32 v[134:135], v[134:135], s[80:81], v[120:121] op_sel:[0,0,0] op_sel_hi:[1,1,0]\nv_pk_fma_f32 v[148:149], v[148:149], s[80:81], v[120:121] op_sel:[0,0,0] op_sel_hi:[1,1,0]\nv_pk_fma_f32 v[150:151], v[150:151], s[80:81], v[120:121] op_sel:[0,0,0] op_sel_hi:[1,1,0]\nve 132\nve 133\nve 134\nve 135\nve 148\n"
"ve 149\nve 150\nve 151\nve 112\nve 113\nve 114\nve 115\nve 116\n"
"ve 117\nve 118\nve 119\nv_pk_add_f32 v[122:123], v[132:133], v[134:135]\nv_pk_add_f32 v[124:125], v[148:149], v[150:151]\nv_pk_add_f32 v[122:123], v[122:123], v[124:125]\nv_add_f32 v120, v122, v123\nv_pk_add_f32 v[122:123], v[112:113], v[114:115]\n"
"v_pk_add_f32 v[124:125], v[116:117], v[118:119]\nv_pk_add_f32 v[122:123], v[122:123], v[124:125]\nv_add_f32 v121, v122, v123\ns_nop 1\nv_permlane32_swap_b32 v120, v121\nv_add_f32 v120, v120, v121\nv_mov_b32 v121, v120\ns_nop 1\n"
"v_permlane16_swap_b32 v120, v121\nv_add_f32 v120, v120, v121\nvr 120\ns_nop 0\nv_mov_b32 v121, v120\ns_nop 1\nv_permlane32_swap_b32 v120, v121\nv_pk_mul_f32 v[132:133], v[132:133], v[120:121] op_sel:[0,0] op_sel_hi:[1,0]\n"
"v_pk_mul_f32 v[134:135], v[134:135], v[120:121] op_sel:[0,0] op_sel_hi:[1,0]\nv_pk_mul_f32 v[148:149], v[148:149], v[120:121] op_sel:[0,0] op_sel_hi:[1,0]\nv_pk_mul_f32 v[150:151], v[150:151], v[120:121] op_sel:[0,0] op_sel_hi:[1,0]\nv_pk_mul_f32 v[112:113], v[112:113], v[120:121] op_sel:[0,1] op_sel_hi:[1,1]\nv_pk_mul_f32 v[114:115], v[114:115], v[120:121] op_sel:[0,1] op_sel_hi:[1,1]\nv_pk_mul_f32 v[116:117], v[116:117], v[120:121] op_sel:[0,1] op_sel_hi:[1,1]\nv_pk_mul_f32 v[118:119], v[118:119], v[120:121] op_sel:[0,1] op_sel_hi:[1,1]\ncv 132,132,133\n"
"cv 133,134,135\nds_write_b64 v24, v[132:133] offset:2048\ncv 148,148,149\ncv 149,150,151\nds_write_b64 v24, v[148:149] offset:2560\ncv 112,112,113\ncv 113,114,115\nds_write_b64 v24, v[112:113] offset:3072\n"
"cv 116,116,117\ncv 117,118,119\nds_write_b64 v24, v[116:117] offset:3584\ns_waitcnt vmcnt(0)\ns_barrier\nv_mov_b32 v22, s34\nbuffer_store_dword v22, v30, s[56:59], s76 offen offset:0 sc1\ns_add_u32 s95, s32, 32768\n"
"buffer_load_dwordx4 a[172:175], v29, s[12:15], s32 offen offset:0\ncv 96,136,137\ncv 97,138,139\ncv 98,152,153\ncv 99,154,155\ncv 112,140,141\ncv 113,142,143\nzvav 136,176,96\n"
"cv 114,156,157\ncv 115,158,159\nzvav 152,180,96\nbuffer_load_dwordx4 a[244:247], v29, s[12:15], s32 offen offset:64\nzvav 140,176,112\ncv 32,160,161\ncv 33,162,163\nzvav 156,180,112\n"
"v_max3_f32 v104, v136, v137, v138\nv_max3_f32 v105, -v136, -v137, -v138\ncv 34,176,177\nv_max3_f32 v104, v104, v139, v152\nv_max3_f32 v104, v104, v153, v154\nv_max_f32 v104, v104, v155\nv_max3_f32 v105, v105, -v139, -v152\nv_max3_f32 v105, v105, -v153, -v154\n"
"v_max_f32 v105, v105, -v155\nv_max3_f32 v120, v140, v141, v142\nv_max3_f32 v120, v120, v143, v156\nv_permlane32_swap_b32 v104, v105\nv_max_f32 v104, v104, v105\nv_mov_b32 v105, v104\nv_max3_f32 v120, v120, v157, v158\nv_max_f32 v120, v120, v159\n"
"v_permlane16_swap_b32 v104, v105\nv_max_f32 v104, v104, v105\nv_mul_f32 v105, s82, v104\nv_mul_f32 v104, s82, v104\nv_max3_f32 v121, -v140, -v141, -v142\nv_max3_f32 v121, v121, -v143, -v156\nv_permlane32_swap_b32 v104, v105\nv_pk_fma_f32 v[96:97], v[136:137], s[82:83], v[104:105] op_sel:[0,0,1] op_sel_hi:[1,1,1]\n"
"v_pk_fma_f32 v[98:99], v[138:139], s[82:83], v[104:105] op_sel:[0,0,1] op_sel_hi:[1,1,1]\nv_pk_fma_f32 v[100:101], v[152:153], s[82:83], v[104:105] op_sel:[0,0,1] op_sel_hi:[1,1,1]\nv_pk_fma_f32 v[102:103], v[154:155], s[82:83], v[104:105] op_sel:[0,0,1] op_sel_hi:[1,1,1]\nv_pk_fma_f32 v[136:137], v[136:137], s[80:81], v[104:105] op_sel:[0,0,0] op_sel_hi:[1,1,0]\nv_pk_fma_f32 v[138:139], v[138:139], s[80:81], v[104:105] op_sel:[0,0,0] op_sel_hi:[1,1,0]\nv_pk_fma_f32 v[152:153], v[152:153], s[80:81], v[104:105] op_sel:[0,0,0] op_sel_hi:[1,1,0]\nv_pk_fma_f32 v[154:155], v[154:155], s[80:81], v[104:105] op_sel:[0,0,0] op_sel_hi:[1,1,0]\nve 136\n"
"ve 137\nve 138\nve 139\nve 152\nve 153\nve 154\nve 155\nve 96\n"
"ve 97\nve 98\nve 99\nve 100\nve 101\nve 102\nve 103\nv_pk_add_f32 v[106:107], v[136:137], v[138:139]\n"
"v_pk_add_f32 v[108:109], v[152:153], v[154:155]\nv_pk_add_f32 v[106:107], v[106:107], v[108:109]\nv_add_f32 v104, v106, v107\nv_pk_add_f32 v[106:107], v[96:97], v[98:99]\nv_pk_add_f32 v[108:109], v[100:101], v[102:103]\nv_pk_add_f32 v[106:107], v[106:107], v[108:109]\nv_add_f32 v105, v106, v107\nv_max3_f32 v121, v121, -v157, -v158\n"
"v_max_f32 v121, v121, -v159\nv_permlane32_swap_b32 v104, v105\nv_add_f32 v104, v104, v105\nv_mov_b32 v105, v104\nv_permlane32_swap_b32 v120, v121\nv_max_f32 v120, v120, v121\nv_permlane16_swap_b32 v104, v105\nv_add_f32 v104, v104, v105\n"
"vr 104\nv_mov_b32 v121, v120\nv_mov_b32 v105, v104\nbuffer_load_dwordx4 a[248:251], v29, s[12:15], s95 offen offset:0\nv_permlane16_swap_b32 v120, v121\nv_permlane32_swap_b32 v104, v105\nv_pk_mul_f32 v[136:137], v[136:137], v[104:105] op_sel:[0,0] op_sel_hi:[1,0]\nv_pk_mul_f32 v[138:139], v[138:139], v[104:105] op_sel:[0,0] op_sel_hi:[1,0]\n"
"v_pk_mul_f32 v[152:153], v[152:153], v[104:105] op_sel:[0,0] op_sel_hi:[1,0]\nv_pk_mul_f32 v[154:155], v[154:155], v[104:105] op_sel:[0,0] op_sel_hi:[1,0]\nv_pk_mul_f32 v[96:97], v[96:97], v[104:105] op_sel:[0,1] op_sel_hi:[1,1]\nv_pk_mul_f32 v[98:99], v[98:99], v[104:105] op_sel:[0,1] op_sel_hi:[1,1]\nv_pk_mul_f32 v[100:101], v[100:101], v[104:105] op_sel:[0,1] op_sel_hi:[1,1]\nv_pk_mul_f32 v[102:103], v[102:103], v[104:105] op_sel:[0,1] op_sel_hi:[1,1]\ncv 136,136,137\ncv 137,138,139\n"
"ds_write_b64 v24, v[136:137] offset:4096\ncv 152,152,153\ncv 153,154,155\nds_write_b64 v24, v[152:153] offset:4608\ncv 96,96,97\ncv 97,98,99\nds_write_b64 v24, v[96:97] offset:5120\ncv 100,100,101\n"
"cv 101,102,103\nds_write_b64 v24, v[100:101] offset:5632\nv_max_f32 v120, v120, v121\nv_mul_f32 v121, s82, v120\nv_mul_f32 v120, s82, v120\ncv 35,178,179\nbuffer_load_dwordx4 a[252:255], v29, s[12:15], s95 offen offset:64\nv_permlane32_swap_b32 v120, v121\n"
"v_pk_fma_f32 v[112:113], v[140:141], s[82:83], v[120:121] op_sel:[0,0,1] op_sel_hi:[1,1,1]\nv_pk_fma_f32 v[114:115], v[142:143], s[82:83], v[120:121] op_sel:[0,0,1] op_sel_hi:[1,1,1]\nv_pk_fma_f32 v[116:117], v[156:157], s[82:83], v[120:121] op_sel:[0,0,1] op_sel_hi:[1,1,1]\nv_pk_fma_f32 v[118:119], v[158:159], s[82:83], v[120:121] op_sel:[0,0,1] op_sel_hi:[1,1,1]\nv_pk_fma_f32 v[140:141], v[140:141], s[80:81], v[120:121] op_sel:[0,0,0] op_sel_hi:[1,1,0]\nv_pk_fma_f32 v[142:143], v[142:143], s[80:81], v[120:121] op_sel:[0,0,0] op_sel_hi:[1,1,0]\nv_pk_fma_f32 v[156:157], v[156:157], s[80:81], v[120:121] op_sel:[0,0,0] op_sel_hi:[1,1,0]\nv_pk_fma_f32 v[158:159], v[158:159], s[80:81], v[120:121] op_sel:[0,0,0] op_sel_hi:[1,1,0]\n"
"ve 140\nve 141\nve 142\nve 143\nve 156\nve 157\nve 158\nve 159\n"
"ve 112\nve 113\nve 114\nve 115\nve 116\nve 117\nve 118\nve 119\n"
"v_pk_add_f32 v[122:123], v[140:141], v[142:143]\nv_pk_add_f32 v[124:125], v[156:157], v[158:159]\nv_pk_add_f32 v[122:123], v[122:123], v[124:125]\nv_add_f32 v120, v122, v123\nv_pk_add_f32 v[122:123], v[112:113], v[114:115]\nv_pk_add_f32 v[124:125], v[116:117], v[118:119]\nv_pk_add_f32 v[122:123], v[122:123], v[124:125]\nv_add_f32 v121, v122, v123\n"
"zvav 160,184,32\ncv 48,164,165\nv_permlane32_swap_b32 v120, v121\nv_add_f32 v120, v120, v121\nv_mov_b32 v121, v120\nzvav 176,188,32\ncv 49,166,167\nv_permlane16_swap_b32 v120, v121\n"
"v_add_f32 v120, v120, v121\nvr 120\nv_max3_f32 v40, v160, v161, v162\nv_mov_b32 v121, v120\nv_max3_f32 v41, -v160, -v161, -v162\ncv 50,180,181\nv_permlane32_swap_b32 v120, v121\nv_pk_mul_f32 v[140:141], v[140:141], v[120:121] op_sel:[0,0] op_sel_hi:[1,0]\n"
"v_pk_mul_f32 v[142:143], v[142:143], v[120:121] op_sel:[0,0] op_sel_hi:[1,0]\nv_pk_mul_f32 v[156:157], v[156:157], v[120:121] op_sel:[0,0] op_sel_hi:[1,0]\nv_pk_mul_f32 v[158:159], v[158:159], v[120:121] op_sel:[0,0] op_sel_hi:[1,0]\nv_pk_mul_f32 v[112:113], v[112:113], v[120:121] op_sel:[0,1] op_sel_hi:[1,1]\nv_pk_mul_f32 v[114:115], v[114:115], v[120:121] op_sel:[0,1] op_sel_hi:[1,1]\nv_pk_mul_f32 v[116:117], v[116:117], v[120:121] op_sel:[0,1] op_sel_hi:[1,1]\nv_pk_mul_f32 v[118:119], v[118:119], v[120:121] op_sel:[0,1] op_sel_hi:[1,1]\ncv 140,140,141\n"
"cv 141,142,143\nds_write_b64 v24, v[140:141] offset:6144\ncv 156,156,157\ncv 157,158,159\nds_write_b64 v24, v[156:157] offset:6656\ncv 112,112,113\ncv 113,114,115\nds_write_b64 v24, v[112:113] offset:7168\n"
"cv 116,116,117\ncv 117,118,119\nds_write_b64 v24, v[116:117] offset:7680\nv_max3_f32 v40, v40, v163, v176\nv_max3_f32 v40, v40, v177, v178\nv_max_f32 v40, v40, v179\nv_max3_f32 v41, v41, -v163, -v176\nv_max3_f32 v41, v41, -v177, -v178\n"
"v_max_f32 v41, v41, -v179\ncv 51,182,183\ns_nop 0\nv_permlane32_swap_b32 v40, v41\nv_max_f32 v40, v40, v41\nv_mov_b32 v41, v40\nzvav 164,184,48\ns_nop 0\n"
"v_permlane16_swap_b32 v40, v41\nv_max_f32 v40, v40, v41\nv_mul_f32 v41, s82, v40\nv_mul_f32 v40, s82, v40\nzvav 180,188,48\ns_nop 0\nv_permlane32_swap_b32 v40, v41\nv_max3_f32 v56, v164, v165, v166\n"
"v_max3_f32 v57, -v164, -v165, -v166\nv_pk_fma_f32 v[32:33], v[160:161], s[82:83], v[40:41] op_sel:[0,0,1] op_sel_hi:[1,1,1]\nv_pk_fma_f32 v[34:35], v[162:163], s[82:83], v[40:41] op_sel:[0,0,1] op_sel_hi:[1,1,1]\nv_pk_fma_f32 v[36:37], v[176:177], s[82:83], v[40:41] op_sel:[0,0,1] op_sel_hi:[1,1,1]\nv_pk_fma_f32 v[38:39], v[178:179], s[82:83], v[40:41] op_sel:[0,0,1] op_sel_hi:[1,1,1]\nv_pk_fma_f32 v[160:161], v[160:161], s[80:81], v[40:41] op_sel:[0,0,0] op_sel_hi:[1,1,0]\nv_pk_fma_f32 v[162:163], v[162:163], s[80:81], v[40:41] op_sel:[0,0,0] op_sel_hi:[1,1,0]\nv_pk_fma_f32 v[176:177], v[176:177], s[80:81], v[40:41] op_sel:[0,0,0] op_sel_hi:[1,1,0]\n"
"v_pk_fma_f32 v[178:179], v[178:179], s[80:81], v[40:41] op_sel:[0,0,0] op_sel_hi:[1,1,0]\nve 160\nve 161\nve 162\nve 163\nve 176\nve 177\nve 178\n"
"ve 179\nve 32\nve 33\nve 34\nve 35\nve 36\nve 37\nve 38\n"
"ve 39\nv_pk_add_f32 v[42:43], v[160:161], v[162:163]\nv_pk_add_f32 v[44:45], v[176:177], v[178:179]\nv_pk_add_f32 v[42:43], v[42:43], v[44:45]\nv_add_f32 v40, v42, v43\nv_pk_add_f32 v[42:43], v[32:33], v[34:35]\nv_pk_add_f32 v[44:45], v[36:37], v[38:39]\nv_pk_add_f32 v[42:43], v[42:43], v[44:45]\n"
"v_add_f32 v41, v42, v43\nv_max3_f32 v56, v56, v167, v180\nv_max3_f32 v56, v56, v181, v182\nv_permlane32_swap_b32 v40, v41\nv_add_f32 v40, v40, v41\nv_mov_b32 v41, v40\nv_max_f32 v56, v56, v183\nv_max3_f32 v57, v57, -v167, -v180\n"
"v_permlane16_swap_b32 v40, v41\nv_add_f32 v40, v40, v41\nvr 40\nv_max3_f32 v57, v57, -v181, -v182\nv_mov_b32 v41, v40\nv_max_f32 v57, v57, -v183\ns_nop 0\nv_permlane32_swap_b32 v40, v41\n"
"v_pk_mul_f32 v[160:161], v[160:161], v[40:41] op_sel:[0,0] op_sel_hi:[1,0]\nv_pk_mul_f32 v[162:163], v[162:163], v[40:41] op_sel:[0,0] op_sel_hi:[1,0]\nv_pk_mul_f32 v[176:177], v[176:177], v[40:41] op_sel:[0,0] op_sel_hi:[1,0]\nv_pk_mul_f32 v[178:179], v[178:179], v[40:41] op_sel:[0,0] op_sel_hi:[1,0]\nv_pk_mul_f32 v[32:33], v[32:33], v[40:41] op_sel:[0,1] op_sel_hi:[1,1]\nv_pk_mul_f32 v[34:35], v[34:35], v[40:41] op_sel:[0,1] op_sel_hi:[1,1]\nv_pk_mul_f32 v[36:37], v[36:37], v[40:41] op_sel:[0,1] op_sel_hi:[1,1]\nv_pk_mul_f32 v[38:39], v[38:39], v[40:41] op_sel:[0,1] op_sel_hi:[1,1]\n"
"cv 160,160,161\ncv 161,162,163\nds_write_b64 v24, v[160:161] offset:32768\ncv 176,176,177\ncv 177,178,179\nds_write_b64 v24, v[176:177] offset:33280\ncv 32,32,33\ncv 33,34,35\n"
"ds_write_b64 v24, v[32:33] offset:33792\ncv 36,36,37\ncv 37,38,39\nds_write_b64 v24, v[36:37] offset:34304\nv_permlane32_swap_b32 v56, v57\nv_max_f32 v56, v56, v57\nv_mov_b32 v57, v56\ns_nop 1\n"
"v_permlane16_swap_b32 v56, v57\nv_max_f32 v56, v56, v57\nv_mul_f32 v57, s82, v56\nv_mul_f32 v56, s82, v56\ns_nop 1\nv_permlane32_swap_b32 v56, v57\nv_pk_fma_f32 v[48:49], v[164:165], s[82:83], v[56:57] op_sel:[0,0,1] op_sel_hi:[1,1,1]\nv_pk_fma_f32 v[50:51], v[166:167], s[82:83], v[56:57] op_sel:[0,0,1] op_sel_hi:[1,1,1]\n"
"v_pk_fma_f32 v[52:53], v[180:181], s[82:83], v[56:57] op_sel:[0,0,1] op_sel_hi:[1,1,1]\nv_pk_fma_f32 v[54:55], v[182:183], s[82:83], v[56:57] op_sel:[0,0,1] op_sel_hi:[1,1,1]\nv_pk_fma_f32 v[164:165], v[164:165], s[80:81], v[56:57] op_sel:[0,0,0] op_sel_hi:[1,1,0]\nv_pk_fma_f32 v[166:167], v[166:167], s[80:81], v[56:57] op_sel:[0,0,0] op_sel_hi:[1,1,0]\nv_pk_fma_f32 v[180:181], v[180:181], s[80:81], v[56:57] op_sel:[0,0,0] op_sel_hi:[1,1,0]\nv_pk_fma_f32 v[182:183], v[182:183], s[80:81], v[56:57] op_sel:[0,0,0] op_sel_hi:[1,1,0]\nve 164\nve 165\n"
"ve 166\nve 167\nve 180\nve 181\nve 182\nve 183\nve 48\nve 49\n"
"ve 50\nve 51\nve 52\nve 53\nve 54\nve 55\nv_pk_add_f32 v[58:59], v[164:165], v[166:167]\nv_pk_add_f32 v[60:61], v[180:181], v[182:183]\n"
"v_pk_add_f32 v[58:59], v[58:59], v[60:61]\nv_add_f32 v56, v58, v59\nv_pk_add_f32 v[58:59], v[48:49], v[50:51]\nv_pk_add_f32 v[60:61], v[52:53], v[54:55]\nv_pk_add_f32 v[58:59], v[58:59], v[60:61]\nv_add_f32 v57, v58, v59\ns_nop 1\nv_permlane32_swap_b32 v56, v57\n"
"v_add_f32 v56, v56, v57\nv_mov_b32 v57, v56\ns_nop 1\nv_permlane16_swap_b32 v56, v57\nv_add_f32 v56, v56, v57\nvr 56\ns_nop 0\nv_mov_b32 v57, v56\n"
"s_nop 1\nv_permlane32_swap_b32 v56, v57\nv_pk_mul_f32 v[164:165], v[164:165], v[56:57] op_sel:[0,0] op_sel_hi:[1,0]\nv_pk_mul_f32 v[166:167], v[166:167], v[56:57] op_sel:[0,0] op_sel_hi:[1,0]\nv_pk_mul_f32 v[180:181], v[180:181], v[56:57] op_sel:[0,0] op_sel_hi:[1,0]\nv_pk_mul_f32 v[182:183], v[182:183], v[56:57] op_sel:[0,0] op_sel_hi:[1,0]\nv_pk_mul_f32 v[48:49], v[48:49], v[56:57] op_sel:[0,1] op_sel_hi:[1,1]\nv_pk_mul_f32 v[50:51], v[50:51], v[56:57] op_sel:[0,1] op_sel_hi:[1,1]\n"
"v_pk_mul_f32 v[52:53], v[52:53], v[56:57] op_sel:[0,1] op_sel_hi:[1,1]\nv_pk_mul_f32 v[54:55], v[54:55], v[56:57] op_sel:[0,1] op_sel_hi:[1,1]\ncv 164,164,165\ncv 165,166,167\nds_write_b64 v24, v[164:165] offset:34816\ncv 180,180,181\ncv 181,182,183\nds_write_b64 v24, v[180:181] offset:35328\n"
"cv 48,48,49\ncv 49,50,51\nds_write_b64 v24, v[48:49] offset:35840\ncv 52,52,53\ncv 53,54,55\nds_write_b64 v24, v[52:53] offset:36352\nv_lshrrev_b32 v20, 2, v23\nv_and_b32 v20, 124, v20\n"
"buffer_load_dword v31, v20, s[56:59], s77 offen offset:0 sc1\ncv 64,168,169\ncv 65,170,171\ncv 66,184,185\ncv 67,186,187\ns_nop 1\nzvav 168,184,64\nzvav 184,188,64\n"
"s_nop 6\nv_max3_f32 v72, v168, v169, v170\nv_max3_f32 v72, v72, v171, v184\nv_max3_f32 v72, v72, v185, v186\nv_max_f32 v72, v72, v187\nv_max3_f32 v73, -v168, -v169, -v170\nv_max3_f32 v73, v73, -v171, -v184\nv_max3_f32 v73, v73, -v185, -v186\n"
"v_max_f32 v73, v73, -v187\ns_nop 1\nv_permlane32_swap_b32 v72, v73\nv_max_f32 v72, v72, v73\nv_mov_b32 v73, v72\ns_nop 1\nv_permlane16_swap_b32 v72, v73\nv_max_f32 v72, v72, v73\n"
"v_mul_f32 v73, s82, v72\nv_mul_f32 v72, s82, v72\ns_nop 1\nv_permlane32_swap_b32 v72, v73\nv_pk_fma_f32 v[64:65], v[168:169], s[82:83], v[72:73] op_sel:[0,0,1] op_sel_hi:[1,1,1]\nv_pk_fma_f32 v[66:67], v[170:171], s[82:83], v[72:73] op_sel:[0,0,1] op_sel_hi:[1,1,1]\nv_pk_fma_f32 v[68:69], v[184:185], s[82:83], v[72:73] op_sel:[0,0,1] op_sel_hi:[1,1,1]\nv_pk_fma_f32 v[70:71], v[186:187], s[82:83], v[72:73] op_sel:[0,0,1] op_sel_hi:[1,1,1]\n"
"v_pk_fma_f32 v[168:169], v[168:169], s[80:81], v[72:73] op_sel:[0,0,0] op_sel_hi:[1,1,0]\nv_pk_fma_f32 v[170:171], v[170:171], s[80:81], v[72:73] op_sel:[0,0,0] op_sel_hi:[1,1,0]\nv_pk_fma_f32 v[184:185], v[184:185], s[80:81], v[72:73] op_sel:[0,0,0] op_sel_hi:[1,1,0]\nv_pk_fma_f32 v[186:187], v[186:187], s[80:81], v[72:73] op_sel:[0,0,0] op_sel_hi:[1,1,0]\nve 168\nve 169\nve 170\nve 171\n"
"ve 184\nve 185\nve 186\nve 187\nve 64\nve 65\nve 66\nve 67\n"
"ve 68\nve 69\nve 70\nve 71\nv_pk_add_f32 v[74:75], v[168:169], v[170:171]\nv_pk_add_f32 v[76:77], v[184:185], v[186:187]\nv_pk_add_f32 v[74:75], v[74:75], v[76:77]\nv_add_f32 v72, v74, v75\n"
"v_pk_add_f32 v[74:75], v[64:65], v[66:67]\nv_pk_add_f32 v[76:77], v[68:69], v[70:71]\nv_pk_add_f32 v[74:75], v[74:75], v[76:77]\nv_add_f32 v73, v74, v75\ns_nop 1\nv_permlane32_swap_b32 v72, v73\nv_add_f32 v72, v72, v73\nv_mov_b32 v73, v72\n"
"s_nop 1\nv_permlane16_swap_b32 v72, v73\nv_add_f32 v72, v72, v73\nvr 72\ns_nop 0\nv_mov_b32 v73, v72\ns_nop 1\nv_permlane32_swap_b32 v72, v73\n"
"v_pk_mul_f32 v[168:169], v[168:169], v[72:73] op_sel:[0,0] op_sel_hi:[1,0]\nv_pk_mul_f32 v[170:171], v[170:171], v[72:73] op_sel:[0,0] op_sel_hi:[1,0]\nv_pk_mul_f32 v[184:185], v[184:185], v[72:73] op_sel:[0,0] op_sel_hi:[1,0]\nv_pk_mul_f32 v[186:187], v[186:187], v[72:73] op_sel:[0,0] op_sel_hi:[1,0]\nv_pk_mul_f32 v[64:65], v[64:65], v[72:73] op_sel:[0,1] op_sel_hi:[1,1]\nv_pk_mul_f32 v[66:67], v[66:67], v[72:73] op_sel:[0,1] op_sel_hi:[1,1]\nv_pk_mul_f32 v[68:69], v[68:69], v[72:73] op_sel:[0,1] op_sel_hi:[1,1]\nv_pk_mul_f32 v[70:71], v[70:71], v[72:73] op_sel:[0,1] op_sel_hi:[1,1]\n"
"cv 168,168,169\ncv 169,170,171\nds_write_b64 v24, v[168:169] offset:36864\ncv 184,184,185\ncv 185,186,187\nds_write_b64 v24, v[184:185] offset:37376\ncv 64,64,65\ncv 65,66,67\n"
"ds_write_b64 v24, v[64:65] offset:37888\ncv 68,68,69\ncv 69,70,71\nds_write_b64 v24, v[68:69] offset:38400\ns_waitcnt vmcnt(0)\nv_cmp_ne_u32 vcc, s34, v31\ns_cbranch_vccz 53f\ns_mov_b32 s69, 0x80000\n"
"52:\ns_sleep 1\nbuffer_load_dword v31, v20, s[56:59], s77 offen offset:0 sc1\ns_waitcnt vmcnt(0)\nv_cmp_ne_u32 vcc, s34, v31\ns_cbranch_vccz 53f\ns_sub_u32 s69, s69, 1\ns_cbranch_scc0 52b\n"
"53:\ns_add_u32 s66, s28, 262144\nlc 0,21,66,0\ncv 80,172,173\ncv 81,174,175\ncv 82,188,189\ncv 83,190,191\ns_add_u32 s67, s28, 270336\n"
"s_nop 0\nzvav 172,184,80\nzvav 188,188,80\nlc 4,21,66,1024\ns_add_u32 s66, s28, 278528\ns_nop 4\nv_max3_f32 v88, v172, v173, v174\nv_max3_f32 v88, v88, v175, v188\n"
"v_max3_f32 v88, v88, v189, v190\nv_max_f32 v88, v88, v191\nv_max3_f32 v89, -v172, -v173, -v174\nv_max3_f32 v89, v89, -v175, -v188\nv_max3_f32 v89, v89, -v189, -v190\nlc 8,21,67,0\nv_max_f32 v89, v89, -v191\ns_nop 1\n"
"v_permlane32_swap_b32 v88, v89\nv_max_f32 v88, v88, v89\nv_mov_b32 v89, v88\ns_nop 1\nv_permlane16_swap_b32 v88, v89\nv_max_f32 v88, v88, v89\nlc 12,21,67,1024\nv_mul_f32 v89, s82, v88\n"
"v_mul_f32 v88, s82, v88\ns_add_u32 s67, s28, 286720\ns_nop 0\nv_permlane32_swap_b32 v88, v89\nv_pk_fma_f32 v[80:81], v[172:173], s[82:83], v[88:89] op_sel:[0,0,1] op_sel_hi:[1,1,1]\nv_pk_fma_f32 v[82:83], v[174:175], s[82:83], v[88:89] op_sel:[0,0,1] op_sel_hi:[1,1,1]\nv_pk_fma_f32 v[84:85], v[188:189], s[82:83], v[88:89] op_sel:[0,0,1] op_sel_hi:[1,1,1]\nv_pk_fma_f32 v[86:87], v[190:191], s[82:83], v[88:89] op_sel:[0,0,1] op_sel_hi:[1,1,1]\n"
"v_pk_fma_f32 v[172:173], v[172:173], s[80:81], v[88:89] op_sel:[0,0,0] op_sel_hi:[1,1,0]\nv_pk_fma_f32 v[174:175], v[174:175], s[80:81], v[88:89] op_sel:[0,0,0] op_sel_hi:[1,1,0]\nv_pk_fma_f32 v[188:189], v[188:189], s[80:81], v[88:89] op_sel:[0,0,0] op_sel_hi:[1,1,0]\nlc 16,21,66,0\nv_pk_fma_f32 v[190:191], v[190:191], s[80:81], v[88:89] op_sel:[0,0,0] op_sel_hi:[1,1,0]\nve 172\nve 173\nve 174\n"
"ve 175\nve 188\nve 189\nlc 20,21,66,1024\nve 190\nve 191\nve 80\nve 81\n"
"ve 82\nve 83\nlc 24,21,67,0\nve 84\nve 85\nve 86\nve 87\ns_add_u32 s66, s28, 294912\n"
"v_pk_add_f32 v[90:91], v[172:173], v[174:175]\nv_pk_add_f32 v[92:93], v[188:189], v[190:191]\nv_pk_add_f32 v[90:91], v[90:91], v[92:93]\nv_add_f32 v88, v90, v91\nlc 28,21,67,1024\nv_pk_add_f32 v[90:91], v[80:81], v[82:83]\nv_pk_add_f32 v[92:93], v[84:85], v[86:87]\nv_pk_add_f32 v[90:91], v[90:91], v[92:93]\n"
"v_add_f32 v89, v90, v91\ns_add_u32 s67, s28, 303104\ns_nop 0\nv_permlane32_swap_b32 v88, v89\nv_add_f32 v88, v88, v89\nv_mov_b32 v89, v88\ns_nop 1\nv_permlane16_swap_b32 v88, v89\n"
"lc 32,21,66,0\nv_add_f32 v88, v88, v89\nvr 88\ns_nop 0\nv_mov_b32 v89, v88\ns_nop 1\nv_permlane32_swap_b32 v88, v89\nv_pk_mul_f32 v[172:173], v[172:173], v[88:89] op_sel:[0,0] op_sel_hi:[1,0]\n"
"v_pk_mul_f32 v[174:175], v[174:175], v[88:89] op_sel:[0,0] op_sel_hi:[1,0]\nv_pk_mul_f32 v[188:189], v[188:189], v[88:89] op_sel:[0,0] op_sel_hi:[1,0]\nlc 36,21,66,1024\nv_pk_mul_f32 v[190:191], v[190:191], v[88:89] op_sel:[0,0] op_sel_hi:[1,0]\nv_pk_mul_f32 v[80:81], v[80:81], v[88:89] op_sel:[0,1] op_sel_hi:[1,1]\nv_pk_mul_f32 v[82:83], v[82:83], v[88:89] op_sel:[0,1] op_sel_hi:[1,1]\nv_pk_mul_f32 v[84:85], v[84:85], v[88:89] op_sel:[0,1] op_sel_hi:[1,1]\nv_pk_mul_f32 v[86:87], v[86:87], v[88:89] op_sel:[0,1] op_sel_hi:[1,1]\n"
"s_add_u32 s66, s28, 311296\ncv 172,172,173\ncv 173,174,175\nds_write_b64 v24, v[172:173] offset:38912\nlc 40,21,67,0\ncv 188,188,189\ncv 189,190,191\nds_write_b64 v24, v[188:189] offset:39424\n"
"cv 80,80,81\ncv 81,82,83\nds_write_b64 v24, v[80:81] offset:39936\nlc 44,21,67,1024\ncv 84,84,85\ncv 85,86,87\nds_write_b64 v24, v[84:85] offset:40448\nlc 48,21,66,0\n"
"lc 52,21,66,1024\nwl 0\ns_barrier\ns_add_u32 s67, s28, 319488\nlc 56,21,67,0\ndt 160,25,0\ndt 162,25,2048\ndt 164,25,512\n"
"dt 166,25,2560\nds_read_b128 v[168:171], v27 offset:0\nds_read_b128 v[172:175], v27 offset:8192\ndt 176,25,4096\ndt 178,25,6144\ndt 180,25,4608\ndt 182,25,6656\nds_read_b128 v[184:187], v27 offset:1024\n"
"ds_read_b128 v[188:191], v27 offset:9216\nlc 60,21,67,1024\ndt 192,25,8192\ns_add_u32 s66, s28, 327680\ndt 194,25,10240\ndt 196,25,8704\ndt 198,25,10752\nds_read_b128 v[200:203], v27 offset:2048\n"
"ds_read_b128 v[204:207], v27 offset:10240\ndt 208,25,12288\ndt 210,25,14336\ndt 212,25,12800\ndt 214,25,14848\nds_read_b128 v[216:219], v27 offset:3072\nds_read_b128 v[220:223], v27 offset:11264\nlc 64,21,66,0\n"
"wl 14\nzvva 144,160,240\ns_add_u32 s67, s28, 335872\nwl 14\nzvvv 128,160,168\nwl 14\nzvvv 132,160,172\ndt 160,25,16384\n"
"dt 162,25,18432\nzvvv 136,164,168\nlc 68,21,66,1024\nzvvv 140,164,172\nds_read_b128 v[168:171], v27 offset:4096\nds_read_b128 v[172:175], v27 offset:12288\nzvva 148,164,240\ndt 164,25,16896\n"
"dt 166,25,18944\nwl 14\nmvvv 128,176,184\ns_add_u32 s66, s28, 344064\nwl 14\nmvvv 132,176,188\nlc 72,21,67,0\nmvva 144,176,240\n"
"dt 176,25,20480\ndt 178,25,22528\nmvvv 136,180,184\nds_read_b128 v[184:187], v27 offset:5120\nmvvv 140,180,188\nds_read_b128 v[188:191], v27 offset:13312\nmvva 148,180,240\ndt 180,25,20992\n"
"lc 76,21,67,1024\nwl 14\nmvvv 128,192,200\ndt 182,25,23040\nwl 14\nmvvv 132,192,204\nmvva 144,192,240\ndt 192,25,24576\n"
"dt 194,25,26624\nmvvv 136,196,200\nlc 80,21,66,0\nmvvv 140,196,204\nds_read_b128 v[200:203], v27 offset:6144\nds_read_b128 v[204:207], v27 offset:14336\nmvva 148,196,240\ndt 196,25,25088\n"
"dt 198,25,27136\nwl 14\nmvvv 128,208,216\nwl 14\nmvvv 132,208,220\nlc 84,21,66,1024\nmvva 144,208,240\ndt 208,25,28672\n"
"dt 210,25,30720\nmvvv 136,212,216\nds_read_b128 v[216:219], v27 offset:7168\nmvvv 140,212,220\nds_read_b128 v[220:223], v27 offset:15360\nmvva 148,212,240\ndt 212,25,29184\ndt 214,25,31232\n"
"wl 14\nmvvv 128,160,168\nwl 14\nmvvv 132,160,172\nmvva 144,160,240\nwl 14\nmvvv 136,164,168\nmvvv 140,164,172\n"
"mvva 148,164,240\nwl 14\nmvvv 128,176,184\nwl 14\nmvvv 132,176,188\nmvva 144,176,240\nwl 12\nmvvv 136,180,184\n"
"mvvv 140,180,188\nmvva 148,180,240\nwl 9\nmvvv 128,192,200\nwl 8\nmvvv 132,192,204\nmvva 144,192,240\nwl 6\n"
"mvvv 136,196,200\nmvvv 140,196,204\nmvva 148,196,240\nwl 3\nmvvv 128,208,216\nwl 2\nmvvv 132,208,220\nmvva 144,208,240\n"
"wl 0\nmvvv 136,212,216\nmvvv 140,212,220\nmvva 148,212,240\ns_nop 0\nbuffer_store_dwordx4 v[128:131], v23, s[56:59], s74 offen offset:0 sc1\ns_nop 0\nbuffer_store_dwordx4 v[132:135], v23, s[56:59], s74 offen offset:1024 sc1\n"
"buffer_store_dwordx4 v[144:147], v28, s[56:59], s75 offen offset:0 sc1\ns_nop 0\nbuffer_store_dwordx4 v[136:139], v23, s[56:59], s74 offen offset:2048 sc1\nbuffer_store_dwordx4 v[140:143], v23, s[56:59], s74 offen offset:3072 sc1\nbuffer_store_dwordx4 v[148:151], v28, s[56:59], s75 offen offset:64 sc1\ns_barrier\ndt 32,0,0\ndt 34,0,10240\n"
"dt 36,0,2048\ndt 38,0,12288\ndt 40,1,0\ndt 42,1,10240\ndt 44,1,2048\ndt 46,1,12288\ndt 48,2,0\ndt 50,2,10240\n"
"dt 52,2,2048\ndt 54,2,12288\nwb 24,8\nzvva 64,32,0\ndt 56,3,0\ndt 58,3,10240\nmvva 64,36,8\nsm 61440\n"
"dt 60,3,2048\ndt 62,3,12288\nzvva 68,36,12\ns_mov_b32 s65, 5242880\nlb 19,4,64,0\nsm 62464\nmvva 68,32,4\nwl 8\n"
"lb 18,4,65,0\nzvva 76,40,4\ndt 32,0,1024\ndt 34,0,11264\nmvva 76,44,12\nsm 63488\ndt 36,0,3072\ndt 38,0,13312\n"
"zvva 72,44,8\nlb 19,4,65,0\nmvva 72,40,0\nwl 8\nzvva 80,48,0\ns_mov_b32 s64, 5373952\ndt 40,1,1024\ndt 42,1,11264\n"
"mvva 80,52,8\nsm 64512\ndt 44,1,3072\ndt 46,1,13312\nzvva 84,52,12\nlb 18,4,64,0\nmvva 84,48,4\nwl 8\n"
"zvva 92,56,4\ndt 48,2,1024\ndt 50,2,11264\nmvva 92,60,12\nsm 65536\ndt 52,2,3072\ndt 54,2,13312\nzvva 88,60,8\n"
"lb 19,4,64,0\nmvva 88,56,0\nwl 8\nzvva 96,32,0\ns_mov_b32 s65, 5505024\ndt 56,3,1024\ndt 58,3,11264\nmvva 96,36,8\n"
"sm 81920\ndt 60,3,3072\ndt 62,3,13312\nzvva 100,36,12\nlb 18,4,65,0\nmvva 100,32,4\nwl 8\nzvva 108,40,4\n"
"dt 32,0,4096\ndt 34,0,14336\nmvva 108,44,12\nsm 82944\ndt 36,0,21504\ndt 38,0,31744\nzvva 104,44,8\nlb 19,4,65,0\n"
"mvva 104,40,0\nwl 8\nzvva 112,48,0\ns_mov_b32 s64, 5636096\ndt 40,1,4096\ndt 42,1,14336\nmvva 112,52,8\nsm 83968\n"
"dt 44,1,21504\ndt 46,1,31744\nzvva 116,52,12\nlb 18,4,64,0\nmvva 116,48,4\nwb 8,8\ns_barrier\nv_mov_b32 v22, s34\n"
"buffer_store_dword v22, v30, s[56:59], s70 offen offset:0 sc1\nzvva 124,56,4\ndt 48,2,4096\ndt 50,2,14336\nmvva 124,60,12\nsm 84992\ndt 52,2,21504\ndt 54,2,31744\n"
"zvva 120,60,8\ns_add_u32 s67, s28, 352256\nlb 19,4,64,0\nmvva 120,56,0\nwl 8\nlc 88,21,67,0\nmvva 64,32,16\ndt 56,3,4096\n"
"dt 58,3,14336\nmvva 64,36,24\ndt 60,3,21504\ndt 62,3,31744\nmvva 68,36,28\nmvva 68,32,20\nwl 8\nlc 92,21,67,1024\n"
"mvva 76,40,20\ns_mov_b32 s65, 5767168\ndt 32,0,20480\ndt 34,0,30720\nmvva 76,44,28\nsm 86016\ndt 36,0,22528\ndt 38,0,32768\n"
"mvva 72,44,24\nlb 18,4,65,0\nmvva 72,40,16\nwl 8\nmvva 80,48,16\ndt 40,1,20480\ndt 42,1,30720\nmvva 80,52,24\n"
"sm 102400\ndt 44,1,22528\ndt 46,1,32768\nmvva 84,52,28\nlb 19,4,65,0\nmvva 84,48,20\nwl 8\nmvva 92,56,20\n"
"s_mov_b32 s64, 5898240\ndt 48,2,20480\ndt 50,2,30720\nmvva 92,60,28\nsm 103424\ndt 52,2,22528\ndt 54,2,32768\nmvva 88,60,24\n"
"lb 18,4,64,0\nmvva 88,56,16\nwb 14,8\ns_barrier\nmvva 96,32,16\ndt 56,3,20480\ndt 58,3,30720\nmvva 96,36,24\n"
"sm 104448\ndt 60,3,22528\ndt 62,3,32768\nmvva 100,36,28\nlb 19,4,64,0\nmvva 100,32,20\nwl 8\nmvva 108,40,20\n"
"s_mov_b32 s65, 6029312\ndt 32,0,23552\ndt 34,0,33792\nmvva 108,44,28\nsm 105472\ndt 36,0,40960\ndt 38,0,51200\nmvva 104,44,24\n"
"lb 18,4,65,0\nmvva 104,40,16\nwl 8\nmvva 112,48,16\ndt 40,1,23552\ndt 42,1,33792\nmvva 112,52,24\ns_add_u32 s66, s28, 360448\n"
"dt 44,1,40960\ndt 46,1,51200\nmvva 116,52,28\nlc 96,21,66,0\nmvva 116,48,20\nwl 8\nlc 100,21,66,1024\nmvva 124,56,20\n"
"dt 48,2,23552\ndt 50,2,33792\nmvva 124,60,28\ndt 52,2,40960\ndt 54,2,51200\nmvva 120,60,24\ns_add_u32 s67, s28, 368640\nmvva 120,56,16\n"
"wl 8\nlc 104,21,67,0\nmvva 64,32,32\ndt 56,3,23552\ndt 58,3,33792\nmvva 64,36,40\ndt 60,3,40960\ndt 62,3,51200\n"
"mvva 68,36,44\nmvva 68,32,36\nwl 8\nlc 108,21,67,1024\nmvva 76,40,36\ndt 32,0,24576\ndt 34,0,34816\nmvva 76,44,44\n"
"dt 36,0,41984\ndt 38,0,52224\nmvva 72,44,40\ns_add_u32 s66, s28, 376832\nmvva 72,40,32\nwl 8\nlc 112,21,66,0\nmvva 80,48,32\n"
"dt 40,1,24576\ndt 42,1,34816\nmvva 80,52,40\ndt 44,1,41984\ndt 46,1,52224\nmvva 84,52,44\nmvva 84,48,36\nwl 8\n"
"lc 116,21,66,1024\nmvva 92,56,36\ndt 48,2,24576\ndt 50,2,34816\nmvva 92,60,44\ndt 52,2,41984\ndt 54,2,52224\nmvva 88,60,40\n"
"s_add_u32 s67, s28, 385024\nmvva 88,56,32\nwl 8\nlc 120,21,67,0\nmvva 96,32,32\ndt 56,3,24576\ndt 58,3,34816\nmvva 96,36,40\n"
"dt 60,3,41984\ndt 62,3,52224\nmvva 100,36,44\nmvva 100,32,36\nwl 8\nlc 124,21,67,1024\nmvva 108,40,36\ndt 32,0,43008\n"
"dt 34,0,53248\nmvva 108,44,44\nsm 106496\ndt 36,0,45056\ndt 38,0,55296\nmvva 104,44,40\ns_mov_b32 s64, 6160384\nlb 19,4,65,0\n"
"sm 122880\nmvva 104,40,32\nwl 8\nlb 18,4,64,0\nmvva 112,48,32\ndt 40,1,43008\ndt 42,1,53248\nmvva 112,52,40\n"
"sm 123904\ndt 44,1,45056\ndt 46,1,55296\nmvva 116,52,44\nlb 19,4,64,0\nmvva 116,48,36\nwl 8\nv_lshrrev_b32 v20, 2, v23\n"
"v_and_b32 v20, 28, v20\nbuffer_load_dword v31, v20, s[56:59], s78 offen offset:0 sc1\nmvva 124,56,36\ns_mov_b32 s65, 4259840\ndt 48,2,43008\ndt 50,2,53248\nmvva 124,60,44\nsm 124928\n"
"dt 52,2,45056\ndt 54,2,55296\nmvva 120,60,40\nlb 18,4,65,0\nmvva 120,56,32\nwl 8\nmvva 64,32,48\ndt 56,3,43008\n"
"dt 58,3,53248\nmvva 64,36,56\nsm 125952\ndt 60,3,45056\ndt 62,3,55296\nmvva 68,36,60\nlb 19,4,65,0\nmvva 68,32,52\n"
"wl 8\nmvva 76,40,52\ns_mov_b32 s64, 4390912\ndt 32,0,44032\ndt 34,0,54272\nmvva 76,44,60\nsm 126976\ndt 36,0,61440\n"
"dt 38,6,6144\nmvva 72,44,56\nlb 18,4,64,0\nmvva 72,40,48\nwl 8\nmvva 80,48,48\ndt 40,1,44032\ndt 42,1,54272\n"
"mvva 80,52,56\nsm 143360\ndt 44,1,61440\ndt 46,7,6144\nmvva 84,52,60\nlb 19,4,64,0\nmvva 84,48,52\nwl 8\n"
"mvva 92,56,52\ns_mov_b32 s65, 4521984\ndt 48,2,44032\ndt 50,2,54272\nmvva 92,60,60\nsm 144384\ndt 52,2,61440\ndt 54,8,6144\n"
"mvva 88,60,56\nlb 18,4,65,0\nmvva 88,56,48\nwb 24,8\ns_barrier\nmvva 96,32,48\ndt 56,3,44032\ndt 58,3,54272\n"
"mvva 96,36,56\nsm 145408\ndt 60,3,61440\ndt 62,9,6144\nmvva 100,36,60\ns_mov_b32 s64, 4653056\nlb 19,4,65,0\nsm 146432\n"
"mvva 100,32,52\nwl 8\nlb 18,4,64,0\nmvva 108,40,52\ndt 32,0,62464\ndt 34,6,7168\nmvva 108,44,60\nsm 147456\n"
"dt 36,0,64512\ndt 38,6,9216\nmvva 104,44,56\nlb 19,4,64,0\nmvva 104,40,48\nwl 8\nmvva 112,48,48\ndt 40,1,62464\n"
"dt 42,7,7168\nmvva 112,52,56\ndt 44,1,64512\ndt 46,7,9216\nmvva 116,52,60\ns_add_u32 s66, s28, 266240\nmvva 116,48,52\nwl 8\n"
"lc 128,21,66,0\nmvva 124,56,52\ndt 48,2,62464\ndt 50,8,7168\nmvva 124,60,60\ndt 52,2,64512\ndt 54,8,9216\nmvva 120,60,56\n"
"mvva 120,56,48\nwl 8\nlc 132,21,66,1024\nmvva 64,32,64\ndt 56,3,62464\ndt 58,9,7168\nmvva 64,36,72\ndt 60,3,64512\n"
"dt 62,9,9216\nmvva 68,36,76\ns_add_u32 s67, s28, 274432\nmvva 68,32,68\nwl 8\nlc 136,21,67,0\nmvva 76,40,68\ndt 32,0,63488\n"
"dt 34,6,8192\nmvva 76,44,76\ndt 36,6,0\ndt 38,6,10240\nmvva 72,44,72\nmvva 72,40,64\nwl 8\nlc 140,21,67,1024\n"
"mvva 80,48,64\ndt 40,1,63488\ndt 42,7,8192\nmvva 80,52,72\ndt 44,7,0\ndt 46,7,10240\nmvva 84,52,76\ns_add_u32 s66, s28, 282624\n"
"mvva 84,48,68\nwl 8\nlc 144,21,66,0\ns_waitcnt vmcnt(13)\nv_cmp_ne_u32 vcc, s34, v31\ns_cbranch_vccz 55f\ns_mov_b32 s69, 0x80000\n54:\n"
"s_sleep 1\nbuffer_load_dword v31, v20, s[56:59], s78 offen offset:0 sc1\ns_waitcnt vmcnt(0)\nv_cmp_ne_u32 vcc, s34, v31\ns_cbranch_vccz 55f\ns_sub_u32 s69, s69, 1\ns_cbranch_scc0 54b\n55:\n"
"v_lshrrev_b32 v24, 1, v23\nv_lshrrev_b32 v27, 2, v23\nv_and_b32 v27, 4, v27\nv_and_b32 v26, 0x1f0, v24\nv_or_b32 v26, v26, v27\nv_mov_b32 v27, 0x70000000\nv_cmp_gt_u32 vcc, 128, v23\ns_nop 1\n"
"v_cndmask_b32 v25, v27, v24, vcc\nv_cndmask_b32 v27, v27, v26, vcc\nmvva 92,56,68\ndt 48,2,63488\ndt 50,8,8192\nmvva 92,60,76\ns_add_u32 s88, s96, 0\ndt 52,8,0\n"
"dt 54,8,10240\nmvva 88,60,72\ns_add_u32 s89, s96, 16384\nbuffer_load_dwordx2 v[160:161], v24, s[56:59], s88 offen sc1\nmvva 88,56,64\nwl 8\nbuffer_load_dwordx2 v[162:163], v24, s[56:59], s89 offen sc1\nmvva 96,32,64\n"
"dt 56,3,63488\ndt 58,9,8192\nmvva 96,36,72\ns_add_u32 s88, s96, 32768\ndt 60,9,0\ndt 62,9,10240\nmvva 100,36,76\ns_add_u32 s89, s96, 49152\n"
"buffer_load_dwordx2 v[164:165], v24, s[56:59], s88 offen sc1\nmvva 100,32,68\nwl 8\nbuffer_load_dwordx2 v[166:167], v24, s[56:59], s89 offen sc1\nmvva 108,40,68\ndt 32,6,16384\ndt 34,6,26624\nmvva 108,44,76\n"
"s_add_u32 s88, s96, 65536\ndt 36,6,18432\ndt 38,6,28672\nmvva 104,44,72\ns_add_u32 s89, s96, 81920\nbuffer_load_dwordx2 v[168:169], v24, s[56:59], s88 offen sc1\nmvva 104,40,64\nwl 8\n"
"buffer_load_dwordx2 v[170:171], v24, s[56:59], s89 offen sc1\nmvva 112,48,64\ndt 40,7,16384\ndt 42,7,26624\nmvva 112,52,72\ns_add_u32 s88, s96, 98304\ndt 44,7,18432\ndt 46,7,28672\n"
"mvva 116,52,76\ns_add_u32 s89, s96, 114688\nbuffer_load_dwordx2 v[172:173], v24, s[56:59], s88 offen sc1\nmvva 116,48,68\nwl 8\nbuffer_load_dwordx2 v[174:175], v24, s[56:59], s89 offen sc1\nmvva 124,56,68\ndt 48,8,16384\n"
"dt 50,8,26624\nmvva 124,60,76\ndt 52,8,18432\ndt 54,8,28672\nmvva 120,60,72\nbuffer_load_dwordx2 v[176:177], v25, s[56:59], s98 offen offset:0 sc1\nmvva 120,56,64\nwl 8\n"
"buffer_load_dwordx2 v[178:179], v25, s[56:59], s98 offen offset:512 sc1\nmvva 64,32,80\ndt 56,9,16384\ndt 58,9,26624\nmvva 64,36,88\ndt 60,9,18432\ndt 62,9,28672\nmvva 68,36,92\n"
"buffer_load_dwordx2 v[180:181], v25, s[56:59], s98 offen offset:1024 sc1\nmvva 68,32,84\nwl 8\nbuffer_load_dwordx2 v[182:183], v25, s[56:59], s98 offen offset:1536 sc1\nmvva 76,40,84\ndt 32,6,17408\ndt 34,6,27648\nmvva 76,44,92\n"
"dt 36,6,19456\ndt 38,6,29696\nmvva 72,44,88\nbuffer_load_dwordx2 v[184:185], v25, s[56:59], s98 offen offset:2048 sc1\nmvva 72,40,80\nwl 8\nbuffer_load_dwordx2 v[186:187], v25, s[56:59], s98 offen offset:2560 sc1\nmvva 80,48,80\n"
"dt 40,7,17408\ndt 42,7,27648\nmvva 80,52,88\ndt 44,7,19456\ndt 46,7,29696\nmvva 84,52,92\nbuffer_load_dwordx2 v[188:189], v25, s[56:59], s98 offen offset:3072 sc1\nmvva 84,48,84\n"
"wl 8\nbuffer_load_dwordx2 v[190:191], v25, s[56:59], s98 offen offset:3584 sc1\nmvva 92,56,84\ndt 48,8,17408\ndt 50,8,27648\nmvva 92,60,92\ndt 52,8,19456\ndt 54,8,29696\n"
"mvva 88,60,88\ns_add_u32 s67, s28, 290816\nlc 148,21,66,1024\nmvva 88,56,80\nwl 8\nlc 152,21,67,0\ns_barrier\nmvva 96,32,80\n"
"dt 56,9,17408\ndt 58,9,27648\nmvva 96,36,88\ndt 60,9,19456\ndt 62,9,29696\nmvva 100,36,92\nmvva 100,32,84\nwl 8\n"
"lc 156,21,67,1024\nmvva 108,40,84\ns_mov_b32 s65, 4784128\ndt 32,6,20480\ndt 34,6,30720\nmvva 108,44,92\nsm 0\ndt 36,6,37888\n"
"dt 38,6,48128\nmvva 104,44,88\nlb 18,4,65,0\nmvva 104,40,80\nwl 8\nmvva 112,48,80\ndt 40,7,20480\ndt 42,7,30720\n"
"mvva 112,52,88\nsm 1024\ndt 44,7,37888\ndt 46,7,48128\nmvva 116,52,92\nlb 19,4,65,0\nmvva 116,48,84\nwl 8\n"
"s_mov_b32 s64, 4915200\nsm 2048\ns_waitcnt vmcnt(19)\nv_pk_add_f32 v[160:161], v[160:161], v[162:163]\nlb 18,4,64,0\ns_waitcnt vmcnt(18)\nv_pk_add_f32 v[164:165], v[164:165], v[166:167]\ns_waitcnt vmcnt(16)\n"
"v_pk_add_f32 v[168:169], v[168:169], v[170:171]\ns_waitcnt vmcnt(14)\nv_pk_add_f32 v[172:173], v[172:173], v[174:175]\nv_pk_add_f32 v[160:161], v[160:161], v[164:165]\nv_pk_add_f32 v[168:169], v[168:169], v[172:173]\nv_pk_add_f32 v[160:161], v[160:161], v[168:169]\ncv 160,160,161\nbuffer_store_dword v160, v26, s[56:59], s97 offen sc1\n"
"sm 3072\ns_waitcnt vmcnt(13)\nv_pk_add_f32 v[176:177], v[176:177], v[178:179]\nlb 19,4,64,0\ns_waitcnt vmcnt(12)\nv_pk_add_f32 v[180:181], v[180:181], v[182:183]\ns_waitcnt vmcnt(10)\nv_pk_add_f32 v[184:185], v[184:185], v[186:187]\n"
"s_waitcnt vmcnt(8)\nv_pk_add_f32 v[188:189], v[188:189], v[190:191]\nv_pk_add_f32 v[176:177], v[176:177], v[180:181]\nv_pk_add_f32 v[184:185], v[184:185], v[188:189]\nv_pk_add_f32 v[176:177], v[176:177], v[184:185]\ncv 176,176,177\nbuffer_store_dword v176, v27, s[56:59], s99 offen sc1\nmvva 124,56,84\n"
"s_mov_b32 s65, 5046272\ndt 48,8,20480\ndt 50,8,30720\nmvva 124,60,92\nsm 4096\ndt 52,8,37888\ndt 54,8,48128\nmvva 120,60,88\n"
"lb 18,4,65,0\nmvva 120,56,80\nwl 8\nmvva 64,32,96\ndt 56,9,20480\ndt 58,9,30720\nmvva 64,36,104\nsm 20480\n"
"dt 60,9,37888\ndt 62,9,48128\nmvva 68,36,108\nlb 19,4,65,0\nmvva 68,32,100\nwl 8\nmvva 76,40,100\ns_mov_b32 s64, 5177344\n"
"dt 32,6,36864\ndt 34,6,47104\nmvva 76,44,108\nsm 21504\ndt 36,6,38912\ndt 38,6,49152\nmvva 72,44,104\nlb 18,4,64,0\n"
"mvva 72,40,96\nwl 8\nmvva 80,48,96\ndt 40,7,36864\ndt 42,7,47104\nmvva 80,52,104\nsm 22528\ndt 44,7,38912\n"
"dt 46,7,49152\nmvva 84,52,108\nlb 19,4,64,0\nmvva 84,48,100\nwl 8\nmvva 92,56,100\ndt 48,8,36864\ndt 50,8,47104\n"
"mvva 92,60,108\ndt 52,8,38912\ndt 54,8,49152\nmvva 88,60,104\ns_add_u32 s66, s28, 299008\nmvva 88,56,96\nwl 8\nlc 160,21,66,0\n"
"mvva 96,32,96\ndt 56,9,36864\ndt 58,9,47104\nmvva 96,36,104\ndt 60,9,38912\ndt 62,9,49152\nmvva 100,36,108\nmvva 100,32,100\n"
"wl 8\nlc 164,21,66,1024\nmvva 108,40,100\ndt 32,6,39936\ndt 34,6,50176\nmvva 108,44,108\ns_add_u32 s67, s28, 307200\ndt 36,6,57344\n"
"dt 38,12,2048\nmvva 104,44,104\nlc 168,21,67,0\nmvva 104,40,96\nwl 8\nlc 0,21,67,1024\nmvva 112,48,96\ndt 40,7,39936\n"
"dt 42,7,50176\nmvva 112,52,104\ndt 44,7,57344\ndt 46,13,2048\nmvva 116,52,108\ns_add_u32 s66, s28, 315392\nmvva 116,48,100\nwl 8\n"
"lc 4,21,66,0\nmvva 124,56,100\ndt 48,8,39936\ndt 50,8,50176\nmvva 124,60,108\ndt 52,8,57344\ndt 54,14,2048\nmvva 120,60,104\n"
"mvva 120,56,96\nwl 8\nlc 8,21,66,1024\nmvva 64,32,112\ndt 56,9,39936\ndt 58,9,50176\nmvva 64,36,120\ndt 60,9,57344\n"
"dt 62,15,2048\nmvva 68,36,124\ns_add_u32 s67, s28, 323584\nmvva 68,32,116\nwb 10,8\nlc 12,21,67,0\ns_barrier\nv_mov_b32 v22, s34\n"
"buffer_store_dword v22, v30, s[56:59], s70 offen offset:64 sc1\nmvva 76,40,116\ndt 32,6,40960\ndt 34,6,51200\nmvva 76,44,124\ndt 36,6,58368\ndt 38,12,3072\nmvva 72,44,120\n"
"mvva 72,40,112\nwl 8\nlc 16,21,67,1024\nmvva 80,48,112\ns_mov_b32 s65, 5308416\ndt 40,7,40960\ndt 42,7,51200\nmvva 80,52,120\n"
"sm 23552\ndt 44,7,58368\ndt 46,13,3072\nmvva 84,52,124\nlb 18,4,65,0\nmvva 84,48,116\nwl 8\nmvva 92,56,116\n"
"dt 48,8,40960\ndt 50,8,51200\nmvva 92,60,124\nsm 24576\ndt 52,8,58368\ndt 54,14,3072\nmvva 88,60,120\nlb 19,4,65,0\n"
"mvva 88,56,112\nwl 8\ns_barrier\nmvva 96,32,112\ns_mov_b32 s64, 5439488\ndt 56,9,40960\ndt 58,9,51200\nmvva 96,36,120\n"
"sm 40960\ndt 60,9,58368\ndt 62,15,3072\nmvva 100,36,124\nlb 18,4,64,0\nsm 41984\nmvva 100,32,116\nwl 8\n"
"lb 19,4,64,0\nmvva 108,40,116\ns_mov_b32 s65, 5570560\ndt 32,6,59392\ndt 34,12,4096\nmvva 108,44,124\nsm 43008\ndt 36,6,61440\n"
"dt 38,12,6144\nmvva 104,44,120\nlb 18,4,65,0\nmvva 104,40,112\nwl 8\nmvva 112,48,112\ndt 40,7,59392\ndt 42,13,4096\n"
"mvva 112,52,120\nsm 44032\ndt 44,7,61440\ndt 46,13,6144\nmvva 116,52,124\nlb 19,4,65,0\nmvva 116,48,116\nwl 8\n"
"mvva 124,56,116\ns_mov_b32 s64, 5701632\ndt 48,8,59392\ndt 50,14,4096\nmvva 124,60,124\nsm 45056\ndt 52,8,61440\ndt 54,14,6144\n"
"mvva 120,60,120\nlb 18,4,64,0\nmvva 120,56,112\nwl 8\nmvva 64,32,128\ndt 56,9,59392\ndt 58,15,4096\nmvva 64,36,136\n"
"sm 61440\ndt 60,9,61440\ndt 62,15,6144\nmvva 68,36,140\nlb 19,4,64,0\nmvva 68,32,132\nwl 8\nmvva 76,40,132\n"
"dt 32,6,60416\ndt 34,12,5120\nmvva 76,44,140\ndt 36,12,12288\ndt 38,12,22528\nmvva 72,44,136\ns_add_u32 s66, s28, 331776\nmvva 72,40,128\n"
"wl 8\nlc 20,21,66,0\nmvva 80,48,128\ndt 40,7,60416\ndt 42,13,5120\nmvva 80,52,136\ndt 44,13,12288\ndt 46,13,22528\n"
"mvva 84,52,140\nmvva 84,48,132\nwl 8\nlc 24,21,66,1024\nmvva 92,56,132\ndt 48,8,60416\ndt 50,14,5120\nmvva 92,60,140\n"
"s_add_u32 s67, s28, 339968\ndt 52,14,12288\ndt 54,14,22528\nmvva 88,60,136\nlc 28,21,67,0\nmvva 88,56,128\nwl 8\nlc 32,21,67,1024\n"
"mvva 96,32,128\ndt 56,9,60416\ndt 58,15,5120\nmvva 96,36,136\ndt 60,15,12288\ndt 62,15,22528\nmvva 100,36,140\ns_add_u32 s66, s28, 348160\n"
"mvva 100,32,132\nwl 8\nlc 36,21,66,0\nmvva 108,40,132\ndt 32,12,13312\ndt 34,12,23552\nmvva 108,44,140\ndt 36,12,15360\n"
"dt 38,12,25600\nmvva 104,44,136\nmvva 104,40,128\nwl 8\nlc 40,21,66,1024\nmvva 112,48,128\ndt 40,13,13312\ndt 42,13,23552\n"
"mvva 112,52,136\ndt 44,13,15360\ndt 46,13,25600\nmvva 116,52,140\ns_add_u32 s67, s28, 356352\nmvva 116,48,132\nwl 8\nlc 44,21,67,0\n"
"s_and_b32 s94, s32, 0x400\ns_lshl_b32 s94, s94, 2\nmvva 124,56,132\ndt 48,14,13312\ndt 50,14,23552\nmvva 124,60,140\ndt 52,14,15360\ndt 54,14,25600\n"
"mvva 120,60,136\nmvva 120,56,128\nwl 8\nlc 48,21,67,1024\ns_and_b32 s88, s77, 0x700\ns_lshl_b32 s88, s88, 5\nmvva 64,32,144\ns_mov_b32 s65, 5832704\n"
"dt 56,15,13312\ndt 58,15,23552\nmvva 64,36,152\nsm 62464\ndt 60,15,15360\ndt 62,15,25600\nmvva 68,36,156\nlb 18,4,65,0\n"
"mvva 68,32,148\nwl 8\ns_add_u32 s94, s94, s88\ns_sub_u32 s94, s94, 128\nmvva 76,40,148\ndt 32,12,14336\ndt 34,12,24576\nmvva 76,44,156\n"
"sm 63488\ndt 36,12,16384\ndt 38,12,26624\nmvva 72,44,152\nlb 19,4,65,0\nmvva 72,40,144\nwl 8\nv_lshrrev_b32 v240, 11, v18\n"
"v_mul_u32_u24 v240, 1920, v240\nmvva 80,48,144\ns_mov_b32 s64, 5963776\ndt 40,13,14336\ndt 42,13,24576\nmvva 80,52,152\nsm 64512\ndt 44,13,16384\n"
"dt 46,13,26624\nmvva 84,52,156\nlb 18,4,64,0\nsm 65536\nmvva 84,48,148\nwl 8\nlb 19,4,64,0\nv_sub_u32 v240, v18, v240\n"
"v_add_u32 v240, s94, v240\nmvva 92,56,148\ns_mov_b32 s65, 6094848\ndt 48,14,14336\ndt 50,14,24576\nmvva 92,60,156\nsm 81920\ndt 52,14,16384\n"
"dt 54,14,26624\nmvva 88,60,152\nlb 18,4,65,0\nmvva 88,56,144\nwb 30,8\ns_barrier\ns_add_u32 s88, s56, 37748736\ns_addc_u32 s89, s57, 0\n"
"mvva 96,32,144\ndt 56,15,14336\ndt 58,15,24576\nmvva 96,36,152\nsm 82944\ndt 60,15,16384\ndt 62,15,26624\nmvva 100,36,156\n"
"lb 19,4,65,0\nmvva 100,32,148\nwl 8\ns_add_u32 s0, s56, 73400320\ns_addc_u32 s1, s57, 0\nmvva 108,40,148\ns_mov_b32 s64, 6225920\ndt 32,0,0\n"
"dt 34,0,10240\nmvva 108,44,156\nsm 83968\ndt 36,0,2048\ndt 38,0,12288\nmvva 104,44,152\nlb 18,4,64,0\nmvva 104,40,144\n"
"wl 8\ns_and_b32 s94, s32, 0x300\ns_mov_b32 s3, s7\nmvva 112,48,144\ndt 40,1,0\ndt 42,1,10240\nmvva 112,52,152\nsm 84992\n"
"dt 44,1,2048\ndt 46,1,12288\nmvva 116,52,156\nlb 19,4,64,0\nmvva 116,48,148\nwl 8\ns_cmp_eq_u32 s94, 0x100\ns_cselect_b64 s[8:9], s[88:89], s[8:9]\n"
"s_cselect_b32 s10, 0x200000, s10\ns_cselect_b64 s[0:1], s[0:1], s[4:5]\ns_cselect_b32 s2, 0x700000, s6\ns_cselect_b64 vcc, -1, 0\nv_cndmask_b32 v240, v18, v240, vcc\nmvva 124,56,148\ndt 48,2,0\ndt 50,2,10240\n"
"mvva 124,60,156\ndt 52,2,2048\ndt 54,2,12288\nmvva 120,60,152\ns_add_u32 s66, s28, 364544\nmvva 120,56,144\nwb 29,8\nlc 52,21,66,0\n"
"mvva 64,32,160\ndt 56,3,0\ndt 58,3,10240\nmvva 64,36,168\ndt 60,3,2048\ndt 62,3,12288\nmvva 68,36,0\nmvva 68,32,164\n"
"wl 8\nlc 56,21,66,1024\nmvva 76,40,164\ndt 32,0,1024\ndt 34,0,11264\nmvva 76,44,0\ns_add_u32 s67, s28, 372736\ndt 36,0,3072\n"
"dt 38,0,13312\nmvva 72,44,168\nlc 60,21,67,0\nmvva 72,40,160\nwl 8\nlc 64,21,67,1024\nmvva 80,48,160\ndt 40,1,1024\n"
"dt 42,1,11264\nmvva 80,52,168\ndt 44,1,3072\ndt 46,1,13312\nmvva 84,52,0\ns_add_u32 s66, s28, 380928\nmvva 84,48,164\nwl 8\n"
"lc 68,21,66,0\nmvva 92,56,164\ndt 48,2,1024\ndt 50,2,11264\nmvva 92,60,0\ndt 52,2,3072\ndt 54,2,13312\nmvva 88,60,168\n"
"mvva 88,56,160\nwl 8\nlc 72,21,66,1024\nmvva 96,32,160\ndt 56,3,1024\ndt 58,3,11264\nmvva 96,36,168\ndt 60,3,3072\n"
"dt 62,3,13312\nmvva 100,36,0\ns_add_u32 s67, s28, 389120\nmvva 100,32,164\nwl 8\nlc 76,21,67,0\nmvva 108,40,164\ndt 32,0,4096\n"
"dt 34,0,14336\nmvva 108,44,0\ndt 36,0,21504\ndt 38,0,31744\nmvva 104,44,168\nmvva 104,40,160\nwl 8\nlc 80,21,67,1024\n"
"mvva 112,48,160\ns_mov_b32 s65, 6291456\ndt 40,1,4096\ndt 42,1,14336\nmvva 112,52,168\nsm 86016\ndt 44,1,21504\ndt 46,1,31744\n"
"mvva 116,52,0\nlb 18,4,65,0\nmvva 116,48,164\nwl 8\nmvva 124,56,164\ndt 48,2,4096\ndt 50,2,14336\nmvva 124,60,0\n"
"sm 102400\ndt 52,2,21504\ndt 54,2,31744\nmvva 120,60,168\nlb 19,4,65,0\nmvva 120,56,160\nwb 34,8\nmvva 64,32,4\n"
"s_mov_b32 s64, 6356992\ndt 56,3,4096\ndt 58,3,14336\nmvva 64,36,12\nsm 103424\ndt 60,3,21504\ndt 62,3,31744\nmvva 68,36,16\n"
"lb 18,4,64,0\nsm 104448\nmvva 68,32,8\nwl 8\nlb 19,4,64,0\nmvva 76,40,8\ns_mov_b32 s65, 6422528\ndt 32,0,20480\n"
"dt 34,0,30720\nmvva 76,44,16\nsm 105472\ndt 36,0,22528\ndt 38,0,32768\nmvva 72,44,12\nlb 18,4,65,0\nmvva 72,40,4\n"
"wl 8\nmvva 80,48,4\ndt 40,1,20480\ndt 42,1,30720\nmvva 80,52,12\nsm 106496\ndt 44,1,22528\ndt 46,1,32768\n"
"mvva 84,52,16\nlb 19,4,65,0\nmvva 84,48,8\nwl 8\nmvva 92,56,8\ns_mov_b32 s64, 6488064\ndt 48,2,20480\ndt 50,2,30720\n"
"mvva 92,60,16\nsm 122880\ndt 52,2,22528\ndt 54,2,32768\nmvva 88,60,12\nlb 18,4,64,0\nmvva 88,56,4\nwb 31,8\n"
"s_barrier\nmvva 96,32,4\ndt 56,3,20480\ndt 58,3,30720\nmvva 96,36,12\nsm 123904\ndt 60,3,22528\ndt 62,3,32768\n"
"mvva 100,36,16\nlb 19,4,64,0\nmvva 100,32,8\nwl 8\nmvva 108,40,8\ndt 32,0,23552\ndt 34,0,33792\nmvva 108,44,16\n"
"dt 36,0,40960\ndt 38,0,51200\nmvva 104,44,12\ns_add_u32 s66, s28, 393216\nmvva 104,40,4\nwl 8\nlc 84,21,66,0\nmvva 112,48,4\n"
"dt 40,1,23552\ndt 42,1,33792\nmvva 112,52,12\ndt 44,1,40960\ndt 46,1,51200\nmvva 116,52,16\nmvva 116,48,8\nwl 8\n"
"lc 88,21,66,1024\nmvva 124,56,8\ndt 48,2,23552\ndt 50,2,33792\nmvva 124,60,16\ns_add_u32 s67, s28, 397312\ndt 52,2,40960\ndt 54,2,51200\n"
"mvva 120,60,12\nlc 92,21,67,0\nmvva 120,56,4\nwb 31,8\nlc 96,21,67,1024\nmvva 64,32,20\ndt 56,3,23552\ndt 58,3,33792\n"
"mvva 64,36,28\ndt 60,3,40960\ndt 62,3,51200\nmvva 68,36,32\ns_add_u32 s66, s28, 401408\nmvva 68,32,24\nwl 8\nlc 100,21,66,0\n"
"mvva 76,40,24\ndt 32,0,24576\ndt 34,0,34816\nmvva 76,44,32\ndt 36,0,41984\ndt 38,0,52224\nmvva 72,44,28\nmvva 72,40,20\n"
"wl 8\nlc 104,21,66,1024\nmvva 80,48,20\ndt 40,1,24576\ndt 42,1,34816\nmvva 80,52,28\ndt 44,1,41984\ndt 46,1,52224\n"
"mvva 84,52,32\ns_add_u32 s67, s28, 405504\nmvva 84,48,24\nwl 8\nlc 108,21,67,0\nmvva 92,56,24\ndt 48,2,24576\ndt 50,2,34816\n"
"mvva 92,60,32\ndt 52,2,41984\ndt 54,2,52224\nmvva 88,60,28\nmvva 88,56,20\nwl 8\nlc 112,21,67,1024\nmvva 96,32,20\n"
"s_mov_b32 s65, 6553600\ndt 56,3,24576\ndt 58,3,34816\nmvva 96,36,28\nsm 124928\ndt 60,3,41984\ndt 62,3,52224\nmvva 100,36,32\n"
"lb 18,4,65,0\nmvva 100,32,24\nwl 8\nmvva 108,40,24\ndt 32,0,43008\ndt 34,0,53248\nmvva 108,44,32\nsm 125952\n"
"dt 36,0,45056\ndt 38,0,55296\nmvva 104,44,28\nlb 19,4,65,0\nmvva 104,40,20\nwl 8\nmvva 112,48,20\ns_mov_b32 s64, 6619136\n"
"dt 40,1,43008\ndt 42,1,53248\nmvva 112,52,28\nsm 126976\ndt 44,1,45056\ndt 46,1,55296\nmvva 116,52,32\nlb 18,4,64,0\n"
"sm 143360\nmvva 116,48,24\nwl 8\nlb 19,4,64,0\nmvva 124,56,24\ns_mov_b32 s65, 6684672\ndt 48,2,43008\ndt 50,2,53248\n"
"mvva 124,60,32\nsm 144384\ndt 52,2,45056\ndt 54,2,55296\nmvva 120,60,28\nlb 18,4,65,0\nmvva 120,56,20\nwb 37,8\n"
"mvva 64,32,36\ndt 56,3,43008\ndt 58,3,53248\nmvva 64,36,44\nsm 145408\ndt 60,3,45056\ndt 62,3,55296\nmvva 68,36,48\n"
"lb 19,4,65,0\nmvva 68,32,40\nwl 8\nmvva 76,40,40\ns_mov_b32 s64, 6750208\ndt 32,0,44032\ndt 34,0,54272\nmvva 76,44,48\n"
"sm 146432\ndt 36,0,61440\ndt 38,6,6144\nmvva 72,44,44\nlb 18,4,64,0\nmvva 72,40,36\nwl 8\nmvva 80,48,36\n"
"dt 40,1,44032\ndt 42,1,54272\nmvva 80,52,44\nsm 147456\ndt 44,1,61440\ndt 46,7,6144\nmvva 84,52,48\nlb 19,4,64,0\n"
"mvva 84,48,40\nwl 8\nmvva 92,56,40\ndt 48,2,44032\ndt 50,2,54272\nmvva 92,60,48\ndt 52,2,61440\ndt 54,8,6144\n"
"mvva 88,60,44\ns_add_u32 s66, s28, 409600\nmvva 88,56,36\nwb 32,8\nlc 116,21,66,0\ns_barrier\nmvva 96,32,36\ndt 56,3,44032\n"
"dt 58,3,54272\nmvva 96,36,44\ndt 60,3,61440\ndt 62,9,6144\nmvva 100,36,48\nmvva 100,32,40\nwl 8\nlc 120,21,66,1024\n"
"mvva 108,40,40\ndt 32,0,62464\ndt 34,6,7168\nmvva 108,44,48\ns_add_u32 s67, s28, 413696\ndt 36,0,64512\ndt 38,6,9216\nmvva 104,44,44\n"
"lc 124,21,67,0\nmvva 104,40,36\nwl 8\nlc 128,21,67,1024\nmvva 112,48,36\ndt 40,1,62464\ndt 42,7,7168\nmvva 112,52,44\n"
"dt 44,1,64512\ndt 46,7,9216\nmvva 116,52,48\ns_add_u32 s66, s28, 417792\nmvva 116,48,40\nwl 8\nlc 132,21,66,0\nmvva 124,56,40\n"
"dt 48,2,62464\ndt 50,8,7168\nmvva 124,60,48\ndt 52,2,64512\ndt 54,8,9216\nmvva 120,60,44\nmvva 120,56,36\nwb 33,8\n"
"lc 136,21,66,1024\nmvva 64,32,52\ndt 56,3,62464\ndt 58,9,7168\nmvva 64,36,60\ndt 60,3,64512\ndt 62,9,9216\nmvva 68,36,64\n"
"s_add_u32 s67, s28, 421888\nmvva 68,32,56\nwl 8\nlc 140,21,67,0\nmvva 76,40,56\ndt 32,0,63488\ndt 34,6,8192\nmvva 76,44,64\n"
"dt 36,6,0\ndt 38,6,10240\nmvva 72,44,60\nmvva 72,40,52\nwl 8\nlc 144,21,67,1024\nmvva 80,48,52\ns_mov_b32 s65, 6815744\n"
"dt 40,1,63488\ndt 42,7,8192\nmvva 80,52,60\nsm 0\ndt 44,7,0\ndt 46,7,10240\nmvva 84,52,64\nlb 18,4,65,0\n"
"mvva 84,48,56\nwl 8\nmvva 92,56,56\ndt 48,2,63488\ndt 50,8,8192\nmvva 92,60,64\nsm 1024\ndt 52,8,0\n"
"dt 54,8,10240\nmvva 88,60,60\nlb 19,4,65,0\nmvva 88,56,52\nwl 8\nmvva 96,32,52\ns_mov_b32 s64, 6881280\ndt 56,3,63488\n"
"dt 58,9,8192\nmvva 96,36,60\nsm 2048\ndt 60,9,0\ndt 62,9,10240\nmvva 100,36,64\nlb 18,4,64,0\nsm 3072\n"
"mvva 100,32,56\nwl 8\nlb 19,4,64,0\nmvva 108,40,56\ns_mov_b32 s65, 6946816\ndt 32,6,16384\ndt 34,6,26624\nmvva 108,44,64\n"
"sm 4096\ndt 36,6,18432\ndt 38,6,28672\nmvva 104,44,60\nlb 18,4,65,0\nmvva 104,40,52\nwl 8\nmvva 112,48,52\n"
"dt 40,7,16384\ndt 42,7,26624\nmvva 112,52,60\nsm 20480\ndt 44,7,18432\ndt 46,7,28672\nmvva 116,52,64\nlb 19,4,65,0\n"
"mvva 116,48,56\nwl 8\nmvva 124,56,56\ns_mov_b32 s64, 7012352\ndt 48,8,16384\ndt 50,8,26624\nmvva 124,60,64\nsm 21504\n"
"dt 52,8,18432\ndt 54,8,28672\nmvva 120,60,60\nlb 18,4,64,0\nmvva 120,56,52\nwb 39,8\nmvva 64,32,68\ndt 56,9,16384\n"
"dt 58,9,26624\nmvva 64,36,76\nsm 22528\ndt 60,9,18432\ndt 62,9,28672\nmvva 68,36,80\nlb 19,4,64,0\nmvva 68,32,72\n"
"wl 8\nmvva 76,40,72\ndt 32,6,17408\ndt 34,6,27648\nmvva 76,44,80\ndt 36,6,19456\ndt 38,6,29696\nmvva 72,44,76\n"
"s_add_u32 s66, s28, 425984\nmvva 72,40,68\nwl 8\nlc 148,21,66,0\nmvva 80,48,68\ndt 40,7,17408\ndt 42,7,27648\nmvva 80,52,76\n"
"dt 44,7,19456\ndt 46,7,29696\nmvva 84,52,80\nmvva 84,48,72\nwl 8\nlc 152,21,66,1024\nmvva 92,56,72\ndt 48,8,17408\n"
"dt 50,8,27648\nmvva 92,60,80\ns_add_u32 s67, s28, 430080\ndt 52,8,19456\ndt 54,8,29696\nmvva 88,60,76\nlc 156,21,67,0\nmvva 88,56,68\n"
"wb 35,8\nlc 160,21,67,1024\ns_barrier\nmvva 96,32,68\ndt 56,9,17408\ndt 58,9,27648\nmvva 96,36,76\ndt 60,9,19456\n"
"dt 62,9,29696\nmvva 100,36,80\ns_add_u32 s66, s28, 434176\nmvva 100,32,72\nwl 8\nlc 164,21,66,0\nmvva 108,40,72\ndt 32,6,20480\n"
"dt 34,6,30720\nmvva 108,44,80\ndt 36,6,37888\ndt 38,6,48128\nmvva 104,44,76\nmvva 104,40,68\nwl 8\nlc 168,21,66,1024\n"
"mvva 112,48,68\ndt 40,7,20480\ndt 42,7,30720\nmvva 112,52,76\ndt 44,7,37888\ndt 46,7,48128\nmvva 116,52,80\ns_add_u32 s67, s28, 438272\n"
"mvva 116,48,72\nwl 8\nlc 0,21,67,0\nmvva 124,56,72\ndt 48,8,20480\ndt 50,8,30720\nmvva 124,60,80\ndt 52,8,37888\n"
"dt 54,8,48128\nmvva 120,60,76\nmvva 120,56,68\nwb 35,8\nlc 4,21,67,1024\nmvva 64,32,84\ns_mov_b32 s65, 7077888\ndt 56,9,20480\n"
"dt 58,9,30720\nmvva 64,36,92\nsm 23552\ndt 60,9,37888\ndt 62,9,48128\nmvva 68,36,96\nlb 18,4,65,0\nmvva 68,32,88\n"
"wl 8\nmvva 76,40,88\ndt 32,6,36864\ndt 34,6,47104\nmvva 76,44,96\nsm 24576\ndt 36,6,38912\ndt 38,6,49152\n"
"mvva 72,44,92\nlb 19,4,65,0\nmvva 72,40,84\nwl 8\nmvva 80,48,84\ns_mov_b32 s64, 7143424\ndt 40,7,36864\ndt 42,7,47104\n"
"mvva 80,52,92\nsm 40960\ndt 44,7,38912\ndt 46,7,49152\nmvva 84,52,96\nlb 18,4,64,0\nsm 41984\nmvva 84,48,88\n"
"wl 8\nlb 19,4,64,0\nmvva 92,56,88\ns_mov_b32 s65, 7208960\ndt 48,8,36864\ndt 50,8,47104\nmvva 92,60,96\nsm 43008\n"
"dt 52,8,38912\ndt 54,8,49152\nmvva 88,60,92\nlb 19,4,65,0\nmvva 88,56,84\nwl 8\nmvva 96,32,84\ns_mov_b32 s64, 7274496\n"
"dt 56,9,36864\ndt 58,9,47104\nmvva 96,36,92\nsm 44032\ndt 60,9,38912\ndt 62,9,49152\nmvva 100,36,96\nlb 19,4,64,0\n"
"mvva 100,32,88\nwl 8\nmvva 108,40,88\ns_mov_b32 s65, 2818048\ndt 32,6,39936\ndt 34,6,50176\nmvva 108,44,96\nsm 45056\n"
"dt 36,6,57344\ndt 38,12,2048\nmvva 104,44,92\nlb 19,4,65,0\nmvva 104,40,84\nwl 8\nmvva 112,48,84\ns_mov_b32 s64, 3080192\n"
"dt 40,7,39936\ndt 42,7,50176\nmvva 112,52,92\nsm 61440\ndt 44,7,57344\ndt 46,13,2048\nmvva 116,52,96\nlb 19,4,64,0\n"
"mvva 116,48,88\nwl 8\nmvva 124,56,88\ndt 48,8,39936\ndt 50,8,50176\nmvva 124,60,96\ndt 52,8,57344\ndt 54,14,2048\n"
"mvva 120,60,92\ns_add_u32 s66, s28, 442368\nmvva 120,56,84\nwb 40,8\nlc 8,21,66,0\nmvva 64,32,100\ndt 56,9,39936\ndt 58,9,50176\n"
"mvva 64,36,108\ndt 60,9,57344\ndt 62,15,2048\nmvva 68,36,112\nmvva 68,32,104\nwl 8\nlc 12,21,66,1024\nmvva 76,40,104\n"
"dt 32,6,40960\ndt 34,6,51200\nmvva 76,44,112\ns_add_u32 s67, s28, 446464\ndt 36,6,58368\ndt 38,12,3072\nmvva 72,44,108\nlc 16,21,67,0\n"
"mvva 72,40,100\nwl 8\nlc 20,21,67,1024\nmvva 80,48,100\ndt 40,7,40960\ndt 42,7,51200\nmvva 80,52,108\ndt 44,7,58368\n"
"dt 46,13,3072\nmvva 84,52,112\ns_add_u32 s66, s28, 450560\nmvva 84,48,104\nwl 8\nlc 24,21,66,0\nmvva 92,56,104\ndt 48,8,40960\n"
"dt 50,8,51200\nmvva 92,60,112\ndt 52,8,58368\ndt 54,14,3072\nmvva 88,60,108\nmvva 88,56,100\nwb 37,8\nlc 28,21,66,1024\n"
"s_barrier\nmvva 96,32,100\ndt 56,9,40960\ndt 58,9,51200\nmvva 96,36,108\ndt 60,9,58368\ndt 62,15,3072\nmvva 100,36,112\n"
"s_add_u32 s67, s28, 454656\nmvva 100,32,104\nwl 8\nlc 32,21,67,0\nmvva 108,40,104\ndt 32,6,59392\ndt 34,12,4096\nmvva 108,44,112\n"
"dt 36,6,61440\ndt 38,12,6144\nmvva 104,44,108\nmvva 104,40,100\nwl 8\nlc 36,21,67,1024\nmvva 112,48,100\ndt 40,7,59392\n"
"dt 42,13,4096\nmvva 112,52,108\ndt 44,7,61440\ndt 46,13,6144\nmvva 116,52,112\ns_add_u32 s66, s28, 176128\nmvva 116,48,104\nwl 8\n"
"lc 40,21,66,0\nmvva 124,56,104\ndt 48,8,59392\ndt 50,14,4096\nmvva 124,60,112\ndt 52,8,61440\ndt 54,14,6144\nmvva 120,60,108\n"
"mvva 120,56,100\nwb 37,8\nlc 44,21,66,1024\nmvva 64,32,116\ndt 56,9,59392\ndt 58,15,4096\nmvva 64,36,124\ns_add_u32 s67, s28, 192512\n"
"dt 60,9,61440\ndt 62,15,6144\nmvva 68,36,128\nlc 48,21,67,0\nmvva 68,32,120\nwl 8\nlc 52,21,67,1024\nmvva 76,40,120\n"
"s_mov_b32 s65, 3342336\ndt 32,6,60416\ndt 34,12,5120\nmvva 76,44,128\nsm 62464\ndt 36,12,12288\ndt 38,12,22528\nmvva 72,44,124\n"
"lb 19,4,65,0\nmvva 72,40,116\nwl 8\nmvva 80,48,116\ns_mov_b32 s64, 3604480\ndt 40,7,60416\ndt 42,13,5120\nmvva 80,52,124\n"
"sm 63488\ndt 44,13,12288\ndt 46,13,22528\nmvva 84,52,128\nlb 19,4,64,0\nmvva 84,48,120\nwl 8\nmvva 92,56,120\n"
"s_mov_b32 s65, 3866624\ndt 48,8,60416\ndt 50,14,5120\nmvva 92,60,128\nsm 64512\ndt 52,14,12288\ndt 54,14,22528\nmvva 88,60,124\n"
"lb 19,4,65,0\nmvva 88,56,116\nwl 8\nmvva 96,32,116\ns_mov_b32 s64, 4128768\ndt 56,9,60416\ndt 58,15,5120\nmvva 96,36,124\n"
"sm 65536\ndt 60,15,12288\ndt 62,15,22528\nmvva 100,36,128\nlb 19,4,64,0\nmvva 100,32,120\nwl 8\nmvva 108,40,120\n"
"s_mov_b32 s65, 7208960\ndt 32,12,13312\ndt 34,12,23552\nmvva 108,44,128\nsm 81920\ndt 36,12,15360\ndt 38,12,25600\nmvva 104,44,124\n"
"lb 18,4,65,0\nmvva 104,40,116\nwl 8\nmvva 112,48,116\ns_mov_b32 s64, 7274496\ndt 40,13,13312\ndt 42,13,23552\nmvva 112,52,124\n"
"sm 82944\ndt 44,13,15360\ndt 46,13,25600\nmvva 116,52,128\nlb 18,4,64,0\nmvva 116,48,120\nwl 8\nmvva 124,56,120\n"
"s_mov_b32 s65, 2818048\ndt 48,14,13312\ndt 50,14,23552\nmvva 124,60,128\nsm 83968\ndt 52,14,15360\ndt 54,14,25600\nmvva 120,60,124\n"
"s_mov_b32 s64, 3080192\nlb 18,4,65,0\nsm 84992\nmvva 120,56,116\nwb 43,8\nlb 18,4,64,0\nmvva 64,32,132\ndt 56,15,13312\n"
"dt 58,15,23552\nmvva 64,36,140\ndt 60,15,15360\ndt 62,15,25600\nmvva 68,36,144\ns_add_u32 s66, s28, 208896\nmvva 68,32,136\nwl 8\n"
"lc 56,21,66,0\nmvva 76,40,136\ndt 32,12,14336\ndt 34,12,24576\nmvva 76,44,144\ndt 36,12,16384\ndt 38,12,26624\nmvva 72,44,140\n"
"mvva 72,40,132\nwl 8\nlc 60,21,66,1024\nmvva 80,48,132\ndt 40,13,14336\ndt 42,13,24576\nmvva 80,52,140\ndt 44,13,16384\n"
"dt 46,13,26624\nmvva 84,52,144\ns_add_u32 s67, s28, 225280\nmvva 84,48,136\nwl 8\nlc 64,21,67,0\nmvva 92,56,136\ndt 48,14,14336\n"
"dt 50,14,24576\nmvva 92,60,144\ndt 52,14,16384\ndt 54,14,26624\nmvva 88,60,140\nmvva 88,56,132\nwb 39,8\nlc 68,21,67,1024\n"
"s_barrier\nmvva 96,32,132\ndt 56,15,14336\ndt 58,15,24576\nmvva 96,36,140\ndt 60,15,16384\ndt 62,15,26624\nmvva 100,36,144\n"
"s_add_u32 s66, s28, 241664\nmvva 100,32,136\nwl 8\nlc 72,21,66,0\nmvva 108,40,136\ndt 32,0,0\ndt 34,0,10240\nmvva 108,44,144\n"
"dt 36,0,2048\ndt 38,0,12288\nmvva 104,44,140\nmvva 104,40,132\nwl 8\nlc 76,21,66,1024\nmvva 112,48,132\ndt 40,1,0\n"
"dt 42,1,10240\nmvva 112,52,140\ns_add_u32 s67, s28, 258048\ndt 44,1,2048\ndt 46,1,12288\nmvva 116,52,144\nlc 80,21,67,0\nmvva 116,48,136\n"
"wl 8\nlc 84,21,67,1024\nmvva 124,56,136\ns_mov_b32 s65, 3342336\ndt 48,2,0\ndt 50,2,10240\nmvva 124,60,144\nsm 86016\n"
"dt 52,2,2048\ndt 54,2,12288\nmvva 120,60,140\nlb 18,4,65,0\nmvva 120,56,132\nwb 41,8\nmvva 64,32,148\ns_mov_b32 s64, 3604480\n"
"dt 56,3,0\ndt 58,3,10240\nmvva 64,36,156\nsm 102400\ndt 60,3,2048\ndt 62,3,12288\nmvva 68,36,160\nlb 18,4,64,0\n"
"mvva 68,32,152\nwl 8\nmvva 76,40,152\ns_mov_b32 s65, 3866624\ndt 32,0,1024\ndt 34,0,11264\nmvva 76,44,160\nsm 103424\n"
"dt 36,0,3072\ndt 38,0,13312\nmvva 72,44,156\nlb 18,4,65,0\nmvva 72,40,148\nwl 8\nmvva 80,48,148\ns_mov_b32 s64, 4128768\n"
"dt 40,1,1024\ndt 42,1,11264\nmvva 80,52,156\nsm 104448\ndt 44,1,3072\ndt 46,1,13312\nmvva 84,52,160\nlb 18,4,64,0\n"
"mvva 84,48,152\nwl 8\nmvva 92,56,152\ns_mov_b32 s65, 0\ndt 48,2,1024\ndt 50,2,11264\nmvva 92,60,160\nsm 105472\n"
"dt 52,2,3072\ndt 54,2,13312\nmvva 88,60,156\nbuffer_load_dwordx4 v240, s[8:11], s65 offen sc1 lds\nmvva 88,56,148\nwl 8\nmvva 96,32,148\ns_mov_b32 s64, 65536\n"
"dt 56,3,1024\ndt 58,3,11264\nmvva 96,36,156\nsm 106496\ndt 60,3,3072\ndt 62,3,13312\nmvva 100,36,160\nbuffer_load_dwordx4 v240, s[8:11], s64 offen sc1 lds\n"
"mvva 100,32,152\nwl 8\nmvva 108,40,152\ns_mov_b32 s65, 131072\ndt 32,0,4096\ndt 34,0,14336\nmvva 108,44,160\nsm 122880\n"
"dt 36,0,21504\ndt 38,0,31744\nmvva 104,44,156\ns_mov_b32 s64, 196608\nbuffer_load_dwordx4 v240, s[8:11], s65 offen sc1 lds\nsm 123904\nmvva 104,40,148\nwl 8\n"
"buffer_load_dwordx4 v240, s[8:11], s64 offen sc1 lds\nmvva 112,48,148\ns_mov_b32 s65, 262144\ndt 40,1,4096\ndt 42,1,14336\nmvva 112,52,156\nsm 124928\ndt 44,1,21504\n"
"dt 46,1,31744\nmvva 116,52,160\nbuffer_load_dwordx4 v240, s[8:11], s65 offen sc1 lds\nmvva 116,48,152\nwl 8\nv_lshrrev_b32 v20, 2, v23\nv_and_b32 v20, 60, v20\nbuffer_load_dword v31, v20, s[56:59], s77 offen offset:128 sc1\n"
"mvva 124,56,152\ns_mov_b32 s64, 327680\ndt 48,2,4096\ndt 50,2,14336\nmvva 124,60,160\nsm 125952\ndt 52,2,21504\ndt 54,2,31744\n"
"mvva 120,60,156\nbuffer_load_dwordx4 v240, s[8:11], s64 offen sc1 lds\nmvva 120,56,148\nwb 47,8\nmvva 64,32,164\ns_mov_b32 s65, 393216\ndt 56,3,4096\ndt 58,3,14336\n"
"mvva 64,36,0\nsm 126976\ndt 60,3,21504\ndt 62,3,31744\nmvva 68,36,4\nbuffer_load_dwordx4 v240, s[8:11], s65 offen sc1 lds\nmvva 68,32,168\nwl 8\n"
"mvva 76,40,168\ns_mov_b32 s64, 458752\ndt 32,0,20480\ndt 34,0,30720\nmvva 76,44,4\nsm 143360\ndt 36,0,22528\ndt 38,0,32768\n"
"mvva 72,44,0\nbuffer_load_dwordx4 v240, s[8:11], s64 offen sc1 lds\nmvva 72,40,164\nwl 8\nmvva 80,48,164\ns_mov_b32 s65, 524288\ndt 40,1,20480\ndt 42,1,30720\n"
"mvva 80,52,0\nsm 144384\ndt 44,1,22528\ndt 46,1,32768\nmvva 84,52,4\nbuffer_load_dwordx4 v240, s[8:11], s65 offen sc1 lds\nmvva 84,48,168\nwl 8\n"
"mvva 92,56,168\ndt 48,2,20480\ndt 50,2,30720\nmvva 92,60,4\ndt 52,2,22528\ndt 54,2,32768\nmvva 88,60,0\nmvva 88,56,164\n"
"wb 42,8\ns_barrier\nmvva 96,32,164\ns_mov_b32 s64, 589824\ndt 56,3,20480\ndt 58,3,30720\nmvva 96,36,0\nsm 145408\n"
"dt 60,3,22528\ndt 62,3,32768\nmvva 100,36,4\ns_mov_b32 s65, 655360\nbuffer_load_dwordx4 v240, s[8:11], s64 offen sc1 lds\nsm 146432\nmvva 100,32,168\nwl 8\n"
"buffer_load_dwordx4 v240, s[8:11], s65 offen sc1 lds\nmvva 108,40,168\ns_mov_b32 s64, 720896\ndt 32,0,23552\ndt 34,0,33792\nmvva 108,44,4\nsm 147456\ndt 36,0,40960\n"
"dt 38,0,51200\nmvva 104,44,0\nbuffer_load_dwordx4 v240, s[8:11], s64 offen sc1 lds\nmvva 104,40,164\nwl 8\nmvva 112,48,164\ns_mov_b32 s65, 786432\ndt 40,1,23552\n"
"dt 42,1,33792\nmvva 112,52,0\nsm 0\ndt 44,1,40960\ndt 46,1,51200\nmvva 116,52,4\nbuffer_load_dwordx4 v240, s[8:11], s65 offen sc1 lds\nmvva 116,48,168\n"
"wl 8\nmvva 124,56,168\ns_mov_b32 s64, 851968\ndt 48,2,23552\ndt 50,2,33792\nmvva 124,60,4\nsm 1024\ndt 52,2,40960\n"
"dt 54,2,51200\nmvva 120,60,0\nbuffer_load_dwordx4 v240, s[8:11], s64 offen sc1 lds\nmvva 120,56,164\nwb 43,8\nmvva 64,32,8\ns_mov_b32 s65, 917504\ndt 56,3,23552\n"
"dt 58,3,33792\nmvva 64,36,16\nsm 2048\ndt 60,3,40960\ndt 62,3,51200\nmvva 68,36,20\nbuffer_load_dwordx4 v240, s[8:11], s65 offen sc1 lds\nmvva 68,32,12\n"
"wl 8\nar 192,172\nmvva 76,40,12\ns_mov_b32 s64, 983040\ndt 32,0,24576\ndt 34,0,34816\nmvva 76,44,20\nsm 3072\n"
"dt 36,0,41984\ndt 38,0,52224\nmvva 72,44,16\nbuffer_load_dwordx4 v240, s[8:11], s64 offen sc1 lds\nmvva 72,40,8\nwl 8\nar 193,173\nmvva 80,48,8\n"
"s_mov_b32 s65, 1048576\ndt 40,1,24576\ndt 42,1,34816\nmvva 80,52,16\nsm 4096\ndt 44,1,41984\ndt 46,1,52224\nmvva 84,52,20\n"
"buffer_load_dwordx4 v240, s[8:11], s65 offen sc1 lds\nmvva 84,48,12\nwl 8\nar 194,174\nmvva 92,56,12\ndt 48,2,24576\ndt 50,2,34816\nmvva 92,60,20\n"
"dt 52,2,41984\ndt 54,2,52224\nmvva 88,60,16\nmvva 88,56,8\nwl 8\nar 195,175\nmvva 96,32,8\ndt 56,3,24576\n"
"dt 58,3,34816\nmvva 96,36,16\ndt 60,3,41984\ndt 62,3,52224\nmvva 100,36,20\nmvva 100,32,12\nwl 8\ns_waitcnt vmcnt(12)\n"
"v_cmp_ne_u32 vcc, s34, v31\ns_cbranch_vccz 57f\ns_mov_b32 s69, 0x80000\n56:\ns_sleep 1\nbuffer_load_dword v31, v20, s[56:59], s77 offen offset:128 sc1\ns_waitcnt vmcnt(0)\nv_cmp_ne_u32 vcc, s34, v31\n"
"s_cbranch_vccz 57f\ns_sub_u32 s69, s69, 1\ns_cbranch_scc0 56b\n57:\ns_mov_b32 s69, 0x70000000\ns_cmp_eq_u32 s79, 4333568\ns_cmov_b32 s69, 4386816\nv_mov_b32 v22, s34\n"
"buffer_store_dword v22, v30, s[56:59], s69 offen offset:0 sc1\nar 196,244\nmvva 108,40,12\ndt 32,0,43008\ndt 34,0,53248\nmvva 108,44,20\ns_add_u32 s66, s29, 0\ndt 36,0,44032\n"
"dt 38,0,54272\nmvva 104,44,16\nlc 88,21,66,0\nmvva 104,40,8\nwl 8\nlc 92,21,66,1024\nar 197,245\nmvva 112,48,8\n"
"dt 40,1,43008\ndt 42,1,53248\nmvva 112,52,16\ndt 44,1,44032\ndt 46,1,54272\nmvva 116,52,20\ns_add_u32 s67, s29, 4096\nmvva 116,48,12\n"
"wl 8\nlc 96,21,67,0\nar 198,246\nmvva 124,56,12\ndt 48,2,43008\ndt 50,2,53248\nmvva 124,60,20\ndt 52,2,44032\n"
"dt 54,2,54272\nmvva 120,60,16\nmvva 120,56,8\nwl 8\nlc 100,21,67,1024\nar 199,247\nmvva 96,32,24\ndt 56,3,43008\n"
"dt 58,3,53248\nmvva 96,36,32\ndt 60,3,44032\ndt 62,3,54272\nmvva 100,36,36\ns_add_u32 s66, s29, 8192\nmvva 100,32,28\nwl 8\n"
"lc 104,21,66,0\nar 200,248\nmvva 108,40,28\ndt 32,0,45056\ndt 34,0,55296\nmvva 108,44,36\ndt 36,0,61440\ndt 38,6,6144\n"
"mvva 104,44,32\nmvva 104,40,24\nwl 8\nlc 108,21,66,1024\nar 201,249\nmvva 112,48,24\ndt 40,1,45056\ndt 42,1,55296\n"
"mvva 112,52,32\ndt 44,1,61440\ndt 46,7,6144\nmvva 116,52,36\ns_add_u32 s67, s29, 12288\nmvva 116,48,28\nwl 8\nlc 112,21,67,0\n"
"ar 202,250\nmvva 124,56,28\ndt 48,2,45056\ndt 50,2,55296\nmvva 124,60,36\ndt 52,2,61440\ndt 54,8,6144\nmvva 120,60,32\n"
"s_add_u32 s66, s29, 16384\nlc 116,21,67,1024\nmvva 120,56,24\nwl 8\nlc 120,21,66,0\ns_barrier\nar 203,251\nmvva 96,32,40\n"
"dt 56,3,45056\ndt 58,3,55296\nmvva 96,36,48\ndt 60,3,61440\ndt 62,9,6144\nmvva 100,36,52\nmvva 100,32,44\nwl 8\n"
"lc 124,21,66,1024\nar 204,252\nmvva 108,40,44\ndt 32,0,62464\ndt 34,6,7168\nmvva 108,44,52\ndt 36,0,63488\ndt 38,6,8192\n"
"mvva 104,44,48\ns_add_u32 s67, s29, 20480\nmvva 104,40,40\nwl 8\nlc 128,21,67,0\nar 205,253\nmvva 112,48,40\ndt 40,1,62464\n"
"dt 42,7,7168\nmvva 112,52,48\ndt 44,1,63488\ndt 46,7,8192\nmvva 116,52,52\nmvva 116,48,44\nwl 8\nlc 132,21,67,1024\n"
"ar 206,254\nmvva 124,56,44\ndt 48,2,62464\ndt 50,8,7168\nmvva 124,60,52\ndt 52,2,63488\ndt 54,8,8192\nmvva 120,60,48\n"
"s_add_u32 s66, s29, 24576\nmvva 120,56,40\nwl 8\nlc 136,21,66,0\nar 207,255\nmvva 96,32,56\ndt 56,3,62464\ndt 58,9,7168\n"
"mvva 96,36,64\ndt 60,3,63488\ndt 62,9,8192\nmvva 100,36,68\nmvva 100,32,60\nwl 8\nlc 140,21,66,1024\nmvva 108,40,60\n"
"dt 32,0,64512\ndt 34,6,9216\nmvva 108,44,68\ns_add_u32 s67, s29, 28672\ndt 36,6,0\ndt 38,6,10240\nmvva 104,44,64\nlc 144,21,67,0\n"
"mvva 104,40,56\nwl 8\nlc 148,21,67,1024\nmvva 112,48,56\ndt 40,1,64512\ndt 42,7,9216\nmvva 112,52,64\ndt 44,7,0\n"
"dt 46,7,10240\nmvva 116,52,68\ns_add_u32 s66, s29, 32768\nmvva 116,48,60\nwl 8\nlc 152,21,66,0\nmvva 124,56,60\ndt 48,2,64512\n"
"dt 50,8,9216\nmvva 124,60,68\ndt 52,8,0\ndt 54,8,10240\nmvva 120,60,64\nmvva 120,56,56\nwl 8\nlc 156,21,66,1024\n"
"mvva 96,32,72\ndt 56,3,64512\ndt 58,9,9216\nmvva 96,36,80\ndt 60,9,0\ndt 62,9,10240\nmvva 100,36,84\ns_add_u32 s67, s29, 36864\n"
"mvva 100,32,76\nwl 8\nlc 160,21,67,0\nmvva 108,40,76\ndt 32,6,16384\ndt 34,6,26624\nmvva 108,44,84\ndt 36,6,17408\n"
"dt 38,6,27648\nmvva 104,44,80\nmvva 104,40,72\nwl 8\nlc 164,21,67,1024\nmvva 112,48,72\ndt 40,7,16384\ndt 42,7,26624\n"
"mvva 112,52,80\ndt 44,7,17408\ndt 46,7,27648\nmvva 116,52,84\ns_add_u32 s66, s29, 40960\nmvva 116,48,76\nwl 8\nlc 168,21,66,0\n"
"mvva 124,56,76\ndt 48,8,16384\ndt 50,8,26624\nmvva 124,60,84\ndt 52,8,17408\ndt 54,8,27648\nmvva 120,60,80\ns_add_u32 s67, s29, 45056\n"
"lc 0,21,66,1024\nmvva 120,56,72\nwl 8\nlc 4,21,67,0\nmvva 64,32,24\ndt 56,9,16384\ndt 58,9,26624\nmvva 64,36,32\n"
"dt 60,9,17408\ndt 62,9,27648\nmvva 68,36,36\nmvva 68,32,28\nwl 8\nlc 8,21,67,1024\nmvva 76,40,28\ndt 32,6,18432\n"
"dt 34,6,28672\nmvva 76,44,36\ndt 36,6,19456\ndt 38,6,29696\nmvva 72,44,32\nbuffer_store_dwordx4 v[96:99], v21, s[56:59], s30 offen offset:0 sc1\nmvva 72,40,24\nwl 8\n"
"buffer_store_dwordx4 v[100:103], v21, s[56:59], s30 offen offset:1024 sc1\nmvva 80,48,24\ndt 40,7,18432\ndt 42,7,28672\nmvva 80,52,32\ndt 44,7,19456\ndt 46,7,29696\nmvva 84,52,36\n"
"buffer_store_dwordx4 v[104:107], v21, s[56:59], s30 offen offset:2048 sc1\nmvva 84,48,28\nwl 8\nbuffer_store_dwordx4 v[108:111], v21, s[56:59], s30 offen offset:3072 sc1\nmvva 92,56,28\ndt 48,8,18432\ndt 50,8,28672\nmvva 92,60,36\n"
"s_add_u32 s30, s30, 4096\ndt 52,8,19456\ndt 54,8,29696\nmvva 88,60,32\nbuffer_store_dwordx4 v[112:115], v21, s[56:59], s30 offen offset:0 sc1\nmvva 88,56,24\nwl 8\nbuffer_store_dwordx4 v[116:119], v21, s[56:59], s30 offen offset:1024 sc1\n"
"s_barrier\nmvva 64,32,40\ndt 56,9,18432\ndt 58,9,28672\nmvva 64,36,48\ndt 60,9,19456\ndt 62,9,29696\nmvva 68,36,52\n"
"buffer_store_dwordx4 v[120:123], v21, s[56:59], s30 offen offset:2048 sc1\nmvva 68,32,44\nwl 8\nbuffer_store_dwordx4 v[124:127], v21, s[56:59], s30 offen offset:3072 sc1\nmvva 76,40,44\ns_mov_b32 s64, 1114112\ndt 32,6,20480\ndt 34,6,30720\n"
"mvva 76,44,52\nsm 20480\ndt 36,6,36864\ndt 38,6,47104\nmvva 72,44,48\ns_mov_b32 s65, 1179648\nbuffer_load_dwordx4 v240, s[8:11], s64 offen sc1 lds\nsm 21504\n"
"mvva 72,40,40\nwl 8\nbuffer_load_dwordx4 v240, s[8:11], s65 offen sc1 lds\nmvva 80,48,40\ns_mov_b32 s64, 1245184\ndt 40,7,20480\ndt 42,7,30720\nmvva 80,52,48\n"
"sm 22528\ndt 44,7,36864\ndt 46,7,47104\nmvva 84,52,52\nbuffer_load_dwordx4 v240, s[8:11], s64 offen sc1 lds\nmvva 84,48,44\nwl 8\nmvva 92,56,44\n"
"dt 48,8,20480\ndt 50,8,30720\nmvva 92,60,52\ndt 52,8,36864\ndt 54,8,47104\nmvva 88,60,48\ns_add_u32 s66, s29, 49152\nmvva 88,56,40\n"
"wl 8\nlc 12,21,66,0\nmvva 64,32,56\ndt 56,9,20480\ndt 58,9,30720\nmvva 64,36,64\ndt 60,9,36864\ndt 62,9,47104\n"
"mvva 68,36,68\nmvva 68,32,60\nwl 8\nlc 16,21,66,1024\nmvva 76,40,60\ndt 32,6,37888\ndt 34,6,48128\nmvva 76,44,68\n"
"dt 36,6,38912\ndt 38,6,49152\nmvva 72,44,64\ns_add_u32 s67, s29, 53248\nmvva 72,40,56\nwl 8\nlc 20,21,67,0\nmvva 80,48,56\n"
"dt 40,7,37888\ndt 42,7,48128\nmvva 80,52,64\ndt 44,7,38912\ndt 46,7,49152\nmvva 84,52,68\nmvva 84,48,60\nwl 8\n"
"lc 24,21,67,1024\nmvva 92,56,60\ndt 48,8,37888\ndt 50,8,48128\nmvva 92,60,68\ns_add_u32 s66, s29, 57344\ndt 52,8,38912\ndt 54,8,49152\n"
"mvva 88,60,64\nlc 28,21,66,0\nmvva 88,56,56\nwl 8\nlc 32,21,66,1024\nmvva 64,32,72\ndt 56,9,37888\ndt 58,9,48128\n"
"mvva 64,36,80\ndt 60,9,38912\ndt 62,9,49152\nmvva 68,36,84\ns_add_u32 s67, s29, 61440\nmvva 68,32,76\nwl 8\nlc 36,21,67,0\n"
"mvva 76,40,76\ndt 32,6,39936\ndt 34,6,50176\nmvva 76,44,84\ndt 36,6,40960\ndt 38,6,51200\nmvva 72,44,80\nmvva 72,40,72\n"
"wl 8\nlc 40,21,67,1024\nmvva 80,48,72\ndt 40,7,39936\ndt 42,7,50176\nmvva 80,52,80\ndt 44,7,40960\ndt 46,7,51200\n"
"mvva 84,52,84\ns_add_u32 s66, s29, 65536\nmvva 84,48,76\nwl 8\nlc 44,21,66,0\nmvva 92,56,76\ndt 48,8,39936\ndt 50,8,50176\n"
"mvva 92,60,84\ndt 52,8,40960\ndt 54,8,51200\nmvva 88,60,80\nmvva 88,56,72\nwb 40,8\nlc 48,21,66,1024\nzvva 128,32,88\n"
"dt 56,9,39936\ndt 58,9,50176\nmvva 128,36,96\ndt 60,9,40960\ndt 62,9,51200\nzvva 132,36,100\ns_add_u32 s67, s29, 69632\nmvva 132,32,92\n"
"wl 8\nlc 52,21,67,0\ns_add_u32 s95, s32, 32768\nzvva 140,40,92\ndt 32,6,57344\ndt 34,12,2048\nmvva 140,44,100\ndt 36,6,58368\n"
"dt 38,12,3072\nzvva 136,44,96\ns_add_u32 s66, s29, 73728\nlc 56,21,67,1024\nv_permlane16_swap_b32 v192, v194\nmvva 136,40,88\nwl 8\nlc 60,21,66,0\n"
"v_permlane16_swap_b32 v193, v195\nzvva 144,48,88\ndt 40,7,57344\ndt 42,13,2048\nmvva 144,52,96\ndt 44,7,58368\ndt 46,13,3072\nzvva 148,52,100\n"
"v_permlane16_swap_b32 v196, v198\nmvva 148,48,92\nwl 8\nlc 64,21,66,1024\nv_permlane16_swap_b32 v197, v199\nzvva 156,56,92\ndt 48,8,57344\ndt 50,14,2048\n"
"mvva 156,60,100\ndt 52,8,58368\ndt 54,14,3072\nzvva 152,60,96\ns_add_u32 s67, s29, 77824\nv_permlane16_swap_b32 v200, v202\nmvva 152,56,88\nwb 17,8\n"
"lc 68,21,67,0\nv_permlane16_swap_b32 v201, v203\ns_barrier\nv_mov_b32 v22, s34\nbuffer_store_dword v22, v30, s[56:59], s79 offen offset:0 sc1\nmvva 128,32,104\ndt 56,9,57344\ndt 58,15,2048\n"
"mvva 128,36,112\ndt 60,9,58368\ndt 62,15,3072\nmvva 132,36,116\nv_permlane16_swap_b32 v204, v206\nmvva 132,32,108\nwl 8\nlc 72,21,67,1024\n"
"v_permlane16_swap_b32 v205, v207\nmvva 140,40,108\ns_mov_b32 s65, 1310720\ndt 32,6,59392\ndt 34,12,4096\nmvva 140,44,116\nsm 23552\ndt 36,6,60416\n"
"dt 38,12,5120\nmvva 136,44,112\nbuffer_load_dwordx4 v240, s[8:11], s65 offen sc1 lds\nv_lshlrev_b32 v208, 16, v192\nmvva 136,40,104\nwl 8\nv_and_b32 v209, 0xffff0000, v192\nmvva 144,48,104\n"
"s_mov_b32 s64, 1376256\ndt 40,7,59392\ndt 42,13,4096\nmvva 144,52,112\nsm 24576\ndt 44,7,60416\ndt 46,13,5120\nmvva 148,52,116\n"
"buffer_load_dwordx4 v240, s[8:11], s64 offen sc1 lds\nv_lshlrev_b32 v210, 16, v193\nmvva 148,48,108\nwl 8\nv_and_b32 v211, 0xffff0000, v193\nmvva 156,56,108\ns_mov_b32 s65, 1441792\ndt 48,8,59392\n"
"dt 50,14,4096\nmvva 156,60,116\nsm 40960\ndt 52,8,60416\ndt 54,14,5120\nmvva 152,60,112\ns_mov_b32 s64, 1507328\nbuffer_load_dwordx4 v240, s[8:11], s65 offen sc1 lds\n"
"sm 41984\nv_lshlrev_b32 v212, 16, v194\nmvva 152,56,104\nwl 8\nbuffer_load_dwordx4 v240, s[8:11], s64 offen sc1 lds\nv_and_b32 v213, 0xffff0000, v194\nmvva 128,32,120\ns_mov_b32 s65, 1572864\n"
"dt 56,9,59392\ndt 58,15,4096\nmvva 128,36,128\nsm 43008\ndt 60,9,60416\ndt 62,15,5120\nmvva 132,36,132\nbuffer_load_dwordx4 v240, s[8:11], s65 offen sc1 lds\n"
"v_lshlrev_b32 v214, 16, v195\nmvva 132,32,124\nwl 8\nv_and_b32 v215, 0xffff0000, v195\nmvva 140,40,124\ns_mov_b32 s64, 1638400\ndt 32,6,61440\ndt 34,12,6144\n"
"mvva 140,44,132\nsm 44032\ndt 36,12,12288\ndt 38,12,22528\nmvva 136,44,128\nbuffer_load_dwordx4 v240, s[8:11], s64 offen sc1 lds\nv_lshlrev_b32 v216, 16, v196\nmvva 136,40,120\n"
"wl 8\nv_and_b32 v217, 0xffff0000, v196\nmvva 144,48,120\ns_mov_b32 s65, 1703936\ndt 40,7,61440\ndt 42,13,6144\nmvva 144,52,128\nsm 45056\n"
"dt 44,13,12288\ndt 46,13,22528\nmvva 148,52,132\nbuffer_load_dwordx4 v240, s[8:11], s65 offen sc1 lds\nv_lshlrev_b32 v218, 16, v197\nmvva 148,48,124\nwl 8\nv_and_b32 v219, 0xffff0000, v197\n"
"mvva 156,56,124\ns_mov_b32 s64, 1769472\ndt 48,8,61440\ndt 50,14,6144\nmvva 156,60,132\nsm 61440\ndt 52,14,12288\ndt 54,14,22528\n"
"mvva 152,60,128\nbuffer_load_dwordx4 v240, s[8:11], s64 offen sc1 lds\nv_lshlrev_b32 v220, 16, v198\nmvva 152,56,120\nwl 8\nv_and_b32 v221, 0xffff0000, v198\nmvva 128,32,136\ndt 56,9,61440\n"
"dt 58,15,6144\nmvva 128,36,144\ndt 60,15,12288\ndt 62,15,22528\nmvva 132,36,148\ns_add_u32 s66, s29, 81920\nv_lshlrev_b32 v222, 16, v199\nmvva 132,32,140\n"
"wl 8\nlc 76,21,66,0\nv_and_b32 v223, 0xffff0000, v199\nmvva 140,40,140\ndt 32,12,13312\ndt 34,12,23552\nmvva 140,44,148\ndt 36,12,14336\n"
"dt 38,12,24576\nmvva 136,44,144\ns_add_u32 s67, s29, 86016\nlc 80,21,66,1024\nv_lshlrev_b32 v224, 16, v200\nmvva 136,40,136\nwl 8\nlc 84,21,67,0\n"
"v_and_b32 v225, 0xffff0000, v200\nmvva 144,48,136\ndt 40,13,13312\ndt 42,13,23552\nmvva 144,52,144\ndt 44,13,14336\ndt 46,13,24576\nmvva 148,52,148\n"
"v_lshlrev_b32 v226, 16, v201\nmvva 148,48,140\nwl 8\nlc 88,21,67,1024\nv_and_b32 v227, 0xffff0000, v201\nmvva 156,56,140\ndt 48,14,13312\ndt 50,14,23552\n"
"mvva 156,60,148\ndt 52,14,14336\ndt 54,14,24576\nmvva 152,60,144\ns_add_u32 s66, s29, 90112\nv_lshlrev_b32 v228, 16, v202\nmvva 152,56,136\nwl 8\n"
"lc 92,21,66,0\nv_and_b32 v229, 0xffff0000, v202\nmvva 128,32,152\ndt 56,15,13312\ndt 58,15,23552\nmvva 128,36,160\ndt 60,15,14336\ndt 62,15,24576\n"
"mvva 132,36,164\nv_lshlrev_b32 v230, 16, v203\nmvva 132,32,156\nwl 8\nlc 96,21,66,1024\nv_and_b32 v231, 0xffff0000, v203\nmvva 140,40,156\ndt 32,12,15360\n"
"dt 34,12,25600\nmvva 140,44,164\ndt 36,12,16384\ndt 38,12,26624\nmvva 136,44,160\ns_add_u32 s67, s29, 94208\nv_lshlrev_b32 v232, 16, v204\nmvva 136,40,152\n"
"wl 8\nlc 100,21,67,0\nv_and_b32 v233, 0xffff0000, v204\nmvva 144,48,152\ndt 40,13,15360\ndt 42,13,25600\nmvva 144,52,160\ndt 44,13,16384\n"
"dt 46,13,26624\nmvva 148,52,164\nv_lshlrev_b32 v234, 16, v205\nmvva 148,48,156\nwl 8\nlc 104,21,67,1024\nv_and_b32 v235, 0xffff0000, v205\nmvva 156,56,156\n"
"dt 48,14,15360\ndt 50,14,25600\nmvva 156,60,164\ns_add_u32 s66, s29, 98304\ndt 52,14,16384\ndt 54,14,26624\nmvva 152,60,160\nlc 108,21,66,0\n"
"v_lshlrev_b32 v236, 16, v206\nmvva 152,56,152\nwb 34,8\nlc 112,21,66,1024\nv_and_b32 v237, 0xffff0000, v206\ns_barrier\nmvva 128,32,168\ndt 56,15,15360\n"
"dt 58,15,25600\nmvva 128,36,4\ndt 60,15,16384\ndt 62,15,26624\nmvva 132,36,8\ns_add_u32 s67, s29, 102400\nv_lshlrev_b32 v238, 16, v207\nmvva 132,32,0\n"
"wl 8\nlc 116,21,67,0\nv_and_b32 v239, 0xffff0000, v207\nmvva 140,40,0\ndt 32,0,0\ndt 34,0,10240\nmvva 140,44,8\ndt 36,0,1024\n"
"dt 38,0,11264\nmvva 136,44,4\nmvva 136,40,168\nwl 8\nlc 120,21,67,1024\ns_xor_b32 s79, s79, 512\nbuffer_load_dword v31, off, s[56:59], s79 offset:0 sc1\nmvva 144,48,168\n"
"dt 40,1,0\ndt 42,1,10240\nmvva 144,52,4\ndt 44,1,1024\ndt 46,1,11264\nmvva 148,52,8\ns_add_u32 s66, s29, 106496\nmvva 148,48,0\n"
"wl 8\nlc 124,21,66,0\nmvva 156,56,0\ndt 48,2,0\ndt 50,2,10240\nmvva 156,60,8\ndt 52,2,1024\ndt 54,2,11264\n"
"mvva 152,60,4\nmvva 152,56,168\nwb 35,8\nlc 128,21,66,1024\nmvva 128,32,12\ndt 56,3,0\ndt 58,3,10240\nmvva 128,36,20\n"
"dt 60,3,1024\ndt 62,3,11264\nmvva 132,36,24\ns_add_u32 s67, s29, 110592\nmvva 132,32,16\nwl 8\nlc 132,21,67,0\nmvva 140,40,16\n"
"s_mov_b32 s65, 1835008\ndt 32,0,2048\ndt 34,0,12288\nmvva 140,44,24\nsm 62464\ndt 36,0,3072\ndt 38,0,13312\nmvva 136,44,20\n"
"buffer_load_dwordx4 v240, s[8:11], s65 offen sc1 lds\nmvva 136,40,12\nwl 8\nlc 136,21,67,1024\nmvva 144,48,12\ns_mov_b32 s64, 1900544\ndt 40,1,2048\ndt 42,1,12288\n"
"mvva 144,52,20\nsm 63488\ndt 44,1,3072\ndt 46,1,13312\nmvva 148,52,24\nbuffer_load_dwordx4 v240, s[8:11], s64 offen sc1 lds\nmvva 148,48,16\nwl 8\n"
"mvva 156,56,16\ns_mov_b32 s65, 1966080\ndt 48,2,2048\ndt 50,2,12288\nmvva 156,60,24\nsm 64512\ndt 52,2,3072\ndt 54,2,13312\n"
"mvva 152,60,20\nbuffer_load_dwordx4 v240, s[8:11], s65 offen sc1 lds\nmvva 152,56,12\nwb 37,8\nmvva 128,32,28\ns_mov_b32 s64, 2031616\ndt 56,3,2048\ndt 58,3,12288\n"
"mvva 128,36,36\nsm 65536\ndt 60,3,3072\ndt 62,3,13312\nmvva 132,36,40\nbuffer_load_dwordx4 v240, s[8:11], s64 offen sc1 lds\nmvva 132,32,32\nwl 8\n"
"mvva 140,40,32\ndt 32,0,4096\ndt 34,0,14336\nmvva 140,44,40\ndt 36,0,20480\ndt 38,0,30720\nmvva 136,44,36\ns_add_u32 s66, s29, 114688\n"
"mvva 136,40,28\nwl 8\nlc 140,21,66,0\nmvva 144,48,28\ndt 40,1,4096\ndt 42,1,14336\nmvva 144,52,36\ndt 44,1,20480\n"
"dt 46,1,30720\nmvva 148,52,40\nmvva 148,48,32\nwl 8\nlc 144,21,66,1024\nmvva 156,56,32\ndt 48,2,4096\ndt 50,2,14336\n"
"mvva 156,60,40\ns_add_u32 s67, s29, 118784\ndt 52,2,20480\ndt 54,2,30720\nmvva 152,60,36\nlc 148,21,67,0\nmvva 152,56,28\nwb 37,8\n"
"lc 152,21,67,1024\nmvva 128,32,44\ndt 56,3,4096\ndt 58,3,14336\nmvva 128,36,52\ndt 60,3,20480\ndt 62,3,30720\nmvva 132,36,56\n"
"s_add_u32 s66, s29, 122880\nmvva 132,32,48\nwl 8\nlc 156,21,66,0\nmvva 140,40,48\ndt 32,0,21504\ndt 34,0,31744\nmvva 140,44,56\n"
"dt 36,0,22528\ndt 38,0,32768\nmvva 136,44,52\nmvva 136,40,44\nwl 8\nlc 160,21,66,1024\ns_waitcnt vmcnt(14)\nv_cmp_ne_u32 vcc, s34, v31\n"
"s_cbranch_vccz 59f\ns_mov_b32 s69, 0x80000\n58:\ns_sleep 1\nbuffer_load_dword v31, off, s[56:59], s79 offset:0 sc1\ns_waitcnt vmcnt(0)\nv_cmp_ne_u32 vcc, s34, v31\ns_cbranch_vccz 59f\n"
"s_sub_u32 s69, s69, 1\ns_cbranch_scc0 58b\n59:\nmvva 144,48,44\ndt 40,1,21504\ndt 42,1,31744\nmvva 144,52,52\ndt 44,1,22528\n"
"dt 46,1,32768\nmvva 148,52,56\nbuffer_load_dwordx4 v[160:163], v21, s[56:59], s31 offen offset:0 sc1\nmvva 148,48,48\nwl 8\nbuffer_load_dwordx4 v[164:167], v21, s[56:59], s31 offen offset:1024 sc1\nmvva 156,56,48\ndt 48,2,21504\n"
"dt 50,2,31744\nmvva 156,60,56\ndt 52,2,22528\ndt 54,2,32768\nmvva 152,60,52\nbuffer_load_dwordx4 v[168:171], v21, s[56:59], s31 offen offset:2048 sc1\nmvva 152,56,44\nwl 8\n"
"buffer_load_dwordx4 v[172:175], v21, s[56:59], s31 offen offset:3072 sc1\ns_barrier\nmvva 128,32,60\ndt 56,3,21504\ndt 58,3,31744\nmvva 128,36,68\ns_add_u32 s31, s31, 4096\ndt 60,3,22528\n"
"dt 62,3,32768\nmvva 132,36,72\nbuffer_load_dwordx4 v[176:179], v21, s[56:59], s31 offen offset:0 sc1\nmvva 132,32,64\nwl 8\nbuffer_load_dwordx4 v[180:183], v21, s[56:59], s31 offen offset:1024 sc1\nmvva 140,40,64\ndt 32,0,23552\n"
"dt 34,0,33792\nmvva 140,44,72\ndt 36,0,24576\ndt 38,0,34816\nmvva 136,44,68\nbuffer_load_dwordx4 v[184:187], v21, s[56:59], s31 offen offset:2048 sc1\nmvva 136,40,60\nwl 8\n"
"buffer_load_dwordx4 v[188:191], v21, s[56:59], s31 offen offset:3072 sc1\nmvva 144,48,60\ndt 40,1,23552\ndt 42,1,33792\nmvva 144,52,68\ns_add_u32 s67, s29, 126976\ndt 44,1,24576\ndt 46,1,34816\n"
"mvva 148,52,72\nlc 164,21,67,0\nmvva 148,48,64\nwl 8\nlc 168,21,67,1024\nmvva 156,56,64\ndt 48,2,23552\ndt 50,2,33792\n"
"mvva 156,60,72\ndt 52,2,24576\ndt 54,2,34816\nmvva 152,60,68\nmvva 152,56,60\nwl 8\nmvva 128,32,76\ndt 56,3,23552\n"
"dt 58,3,33792\nmvva 128,36,84\ndt 60,3,24576\ndt 62,3,34816\nmvva 132,36,88\nmvva 132,32,80\nwl 8\nmvva 140,40,80\n"
"dt 32,0,40960\ndt 34,0,51200\nmvva 140,44,88\ndt 36,0,41984\ndt 38,0,52224\nmvva 136,44,84\nmvva 136,40,76\nwl 8\n"
"mvva 144,48,76\ndt 40,1,40960\ndt 42,1,51200\nmvva 144,52,84\ndt 44,1,41984\ndt 46,1,52224\nmvva 148,52,88\nmvva 148,48,80\n"
"wl 8\nmvva 156,56,80\ndt 48,2,40960\ndt 50,2,51200\nmvva 156,60,88\ndt 52,2,41984\ndt 54,2,52224\nmvva 152,60,84\n"
"mvva 152,56,76\nwl 8\nmvva 128,32,92\ndt 56,3,40960\ndt 58,3,51200\ns_waitcnt vmcnt(9)\nv_add_f32 v64, v64, v160\nmvva 128,36,100\n"
"dt 60,3,41984\ndt 62,3,52224\nv_add_f32 v65, v65, v161\nmvva 132,36,104\nv_add_f32 v66, v66, v162\nv_add_f32 v67, v67, v163\nmvva 132,32,96\nwl 8\n"
"v_mul_f32 v160, s82, v64\nv_mul_f32 v161, s82, v65\nmvva 140,40,96\ndt 32,0,43008\ndt 34,0,53248\nv_mul_f32 v162, s82, v66\nmvva 140,44,104\ndt 36,0,44032\n"
"dt 38,0,54272\nv_mul_f32 v163, s82, v67\nmvva 136,44,100\nve 160\nmvva 136,40,92\nwl 8\nve 161\nmvva 144,48,92\n"
"dt 40,1,43008\ndt 42,1,53248\nve 162\nmvva 144,52,100\ndt 44,1,44032\ndt 46,1,54272\nve 163\nmvva 148,52,104\n"
"v_add_f32 v160, 1.0, v160\nv_add_f32 v161, 1.0, v161\nmvva 148,48,96\nwl 8\nv_add_f32 v162, 1.0, v162\nv_add_f32 v163, 1.0, v163\nmvva 156,56,96\ndt 48,2,43008\n"
"dt 50,2,53248\nvr 160\nmvva 156,60,104\ndt 52,2,44032\ndt 54,2,54272\nvr 161\nmvva 152,60,100\nvr 162\n"
"mvva 152,56,92\nwl 8\nvr 163\nmvva 128,32,108\ndt 56,3,43008\ndt 58,3,53248\nv_mul_f32 v64, v64, v160\nmvva 128,36,116\n"
"dt 60,3,44032\ndt 62,3,54272\nv_mul_f32 v65, v65, v161\nmvva 132,36,120\nv_mul_f32 v66, v66, v162\nv_mul_f32 v67, v67, v163\nmvva 132,32,112\nwl 8\n"
"s_waitcnt vmcnt(8)\nv_add_f32 v68, v68, v164\nv_add_f32 v69, v69, v165\nmvva 140,40,112\ndt 32,0,45056\ndt 34,0,55296\nv_add_f32 v70, v70, v166\nmvva 140,44,120\n"
"dt 36,0,61440\ndt 38,6,6144\nv_add_f32 v71, v71, v167\nmvva 136,44,116\nv_mul_f32 v164, s82, v68\nv_mul_f32 v165, s82, v69\nmvva 136,40,108\nwl 8\n"
"v_mul_f32 v166, s82, v70\nv_mul_f32 v167, s82, v71\nmvva 144,48,108\ndt 40,1,45056\ndt 42,1,55296\nve 164\nmvva 144,52,116\ndt 44,1,61440\n"
"dt 46,7,6144\nve 165\nmvva 148,52,120\nve 166\nmvva 148,48,112\nwl 8\nve 167\nmvva 156,56,112\n"
"dt 48,2,45056\ndt 50,2,55296\nv_add_f32 v164, 1.0, v164\nmvva 156,60,120\ndt 52,2,61440\ndt 54,8,6144\nv_add_f32 v165, 1.0, v165\nmvva 152,60,116\n"
"v_add_f32 v166, 1.0, v166\nv_add_f32 v167, 1.0, v167\nmvva 152,56,108\nwl 8\nvr 164\ns_barrier\nmvva 128,32,124\ndt 56,3,45056\n"
"dt 58,3,55296\nvr 165\nmvva 128,36,132\ndt 60,3,61440\ndt 62,9,6144\nvr 166\nmvva 132,36,136\nvr 167\n"
"mvva 132,32,128\nwl 8\nv_mul_f32 v68, v68, v164\nv_mul_f32 v69, v69, v165\nmvva 140,40,128\ndt 32,0,62464\ndt 34,6,7168\nv_mul_f32 v70, v70, v166\n"
"mvva 140,44,136\ndt 36,0,63488\ndt 38,6,8192\nv_mul_f32 v71, v71, v167\nmvva 136,44,132\ns_waitcnt vmcnt(7)\nv_add_f32 v72, v72, v168\nv_add_f32 v73, v73, v169\n"
"mvva 136,40,124\nwl 8\nv_add_f32 v74, v74, v170\nv_add_f32 v75, v75, v171\nmvva 144,48,124\ndt 40,1,62464\ndt 42,7,7168\nv_mul_f32 v168, s82, v72\n"
"mvva 144,52,132\ndt 44,1,63488\ndt 46,7,8192\nv_mul_f32 v169, s82, v73\nmvva 148,52,136\nv_mul_f32 v170, s82, v74\nv_mul_f32 v171, s82, v75\nmvva 148,48,128\n"
"wl 8\nve 168\nmvva 156,56,128\ndt 48,2,62464\ndt 50,8,7168\nve 169\nmvva 156,60,136\ndt 52,2,63488\n"
"dt 54,8,8192\nve 170\nmvva 152,60,132\nve 171\nmvva 152,56,124\nwl 8\nv_add_f32 v168, 1.0, v168\nv_add_f32 v169, 1.0, v169\n"
"mvva 128,32,140\ndt 56,3,62464\ndt 58,9,7168\nv_add_f32 v170, 1.0, v170\nmvva 128,36,148\ndt 60,3,63488\ndt 62,9,8192\nv_add_f32 v171, 1.0, v171\n"
"mvva 132,36,152\nvr 168\nmvva 132,32,144\nwl 8\nvr 169\nmvva 140,40,144\ndt 32,0,64512\ndt 34,6,9216\n"
"vr 170\nmvva 140,44,152\ndt 36,6,0\ndt 38,6,10240\nvr 171\nmvva 136,44,148\nv_mul_f32 v72, v72, v168\nv_mul_f32 v73, v73, v169\n"
"mvva 136,40,140\nwl 8\nv_mul_f32 v74, v74, v170\nv_mul_f32 v75, v75, v171\nmvva 144,48,140\ndt 40,1,64512\ndt 42,7,9216\ns_waitcnt vmcnt(6)\n"
"v_add_f32 v76, v76, v172\nmvva 144,52,148\ndt 44,7,0\ndt 46,7,10240\nv_add_f32 v77, v77, v173\nmvva 148,52,152\nv_add_f32 v78, v78, v174\nv_add_f32 v79, v79, v175\n"
"mvva 148,48,144\nwl 8\nv_mul_f32 v172, s82, v76\nv_mul_f32 v173, s82, v77\nmvva 156,56,144\ndt 48,2,64512\ndt 50,8,9216\nv_mul_f32 v174, s82, v78\n"
"mvva 156,60,152\ndt 52,8,0\ndt 54,8,10240\nv_mul_f32 v175, s82, v79\nmvva 152,60,148\nve 172\nmvva 152,56,140\nwb 0,8\n"
"ve 173\nmvva 128,32,156\ndt 56,3,64512\ndt 58,9,9216\nve 174\nmvva 128,36,164\ndt 60,9,0\ndt 62,9,10240\n"
"ve 175\nmvva 132,36,168\nv_add_f32 v172, 1.0, v172\nv_add_f32 v173, 1.0, v173\nmvva 132,32,160\nwl 8\nv_add_f32 v174, 1.0, v174\nv_add_f32 v175, 1.0, v175\n"
"mvva 140,40,160\nvr 172\nmvva 140,44,168\nvr 173\nmvva 136,44,164\nvr 174\nmvva 136,40,156\nwl 4\n"
"vr 175\nmvva 144,48,156\nv_mul_f32 v76, v76, v172\nmvva 144,52,164\nv_mul_f32 v77, v77, v173\nmvva 148,52,168\nv_mul_f32 v78, v78, v174\nv_mul_f32 v79, v79, v175\n"
"mvva 148,48,160\nwl 0\nmvva 156,56,160\nmvva 156,60,168\nmvva 152,60,164\nmvva 152,56,156\nv_pk_fma_f32 v[64:65], v[64:65], v[128:129], v[208:209]\nv_pk_fma_f32 v[66:67], v[66:67], v[130:131], v[210:211]\n"
"cv 192,64,65\ncv 193,66,67\nv_pk_fma_f32 v[72:73], v[72:73], v[136:137], v[212:213]\nv_pk_fma_f32 v[74:75], v[74:75], v[138:139], v[214:215]\ncv 194,72,73\ncv 195,74,75\nv_pk_add_f32 v[80:81], v[80:81], v[176:177]\nv_permlane16_swap_b32 v192, v194\n"
"v_permlane16_swap_b32 v193, v195\nbuffer_store_dwordx4 v[192:195], v29, s[16:19], s32 offen offset:0 sc1\nv_pk_add_f32 v[82:83], v[82:83], v[178:179]\nv_pk_mul_f32 v[176:177], v[80:81], s[82:83]\nv_pk_mul_f32 v[178:179], v[82:83], s[82:83]\nve 176\nve 177\nve 178\n"
"ve 179\nv_pk_add_f32 v[176:177], v[176:177], s[84:85]\nv_pk_add_f32 v[178:179], v[178:179], s[84:85]\nvr 176\nvr 177\nvr 178\nvr 179\nv_pk_mul_f32 v[80:81], v[80:81], v[176:177]\n"
"v_pk_mul_f32 v[82:83], v[82:83], v[178:179]\nv_pk_fma_f32 v[80:81], v[80:81], v[144:145], v[216:217]\nv_pk_fma_f32 v[82:83], v[82:83], v[146:147], v[218:219]\ncv 196,80,81\ncv 197,82,83\nv_pk_add_f32 v[88:89], v[88:89], v[184:185]\nv_pk_add_f32 v[90:91], v[90:91], v[186:187]\nv_pk_mul_f32 v[184:185], v[88:89], s[82:83]\n"
"v_pk_mul_f32 v[186:187], v[90:91], s[82:83]\nve 184\nve 185\nve 186\nve 187\nv_pk_add_f32 v[184:185], v[184:185], s[84:85]\nv_pk_add_f32 v[186:187], v[186:187], s[84:85]\nvr 184\n"
"vr 185\nvr 186\nvr 187\nv_pk_mul_f32 v[88:89], v[88:89], v[184:185]\nv_pk_mul_f32 v[90:91], v[90:91], v[186:187]\nv_pk_fma_f32 v[88:89], v[88:89], v[152:153], v[220:221]\nv_pk_fma_f32 v[90:91], v[90:91], v[154:155], v[222:223]\ncv 198,88,89\n"
"cv 199,90,91\nv_pk_fma_f32 v[68:69], v[68:69], v[132:133], v[224:225]\nv_permlane16_swap_b32 v196, v198\nv_permlane16_swap_b32 v197, v199\nbuffer_store_dwordx4 v[196:199], v29, s[16:19], s32 offen offset:64 sc1\nv_pk_fma_f32 v[70:71], v[70:71], v[134:135], v[226:227]\ncv 200,68,69\ncv 201,70,71\n"
"v_pk_fma_f32 v[76:77], v[76:77], v[140:141], v[228:229]\nv_pk_fma_f32 v[78:79], v[78:79], v[142:143], v[230:231]\ncv 202,76,77\ncv 203,78,79\nv_pk_add_f32 v[84:85], v[84:85], v[180:181]\nv_permlane16_swap_b32 v200, v202\nv_permlane16_swap_b32 v201, v203\nbuffer_store_dwordx4 v[200:203], v29, s[16:19], s95 offen offset:0 sc1\n"
"v_pk_add_f32 v[86:87], v[86:87], v[182:183]\nv_pk_mul_f32 v[180:181], v[84:85], s[82:83]\nv_pk_mul_f32 v[182:183], v[86:87], s[82:83]\nve 180\nve 181\nve 182\nve 183\nv_pk_add_f32 v[180:181], v[180:181], s[84:85]\n"
"v_pk_add_f32 v[182:183], v[182:183], s[84:85]\nvr 180\nvr 181\nvr 182\nvr 183\nv_pk_mul_f32 v[84:85], v[84:85], v[180:181]\nv_pk_mul_f32 v[86:87], v[86:87], v[182:183]\nv_pk_fma_f32 v[84:85], v[84:85], v[148:149], v[232:233]\n"
"v_pk_fma_f32 v[86:87], v[86:87], v[150:151], v[234:235]\ncv 204,84,85\ncv 205,86,87\nv_pk_add_f32 v[92:93], v[92:93], v[188:189]\nv_pk_add_f32 v[94:95], v[94:95], v[190:191]\nv_pk_mul_f32 v[188:189], v[92:93], s[82:83]\nv_pk_mul_f32 v[190:191], v[94:95], s[82:83]\nve 188\n"
"ve 189\nve 190\nve 191\nv_pk_add_f32 v[188:189], v[188:189], s[84:85]\nv_pk_add_f32 v[190:191], v[190:191], s[84:85]\nvr 188\nvr 189\nvr 190\n"
"vr 191\nv_pk_mul_f32 v[92:93], v[92:93], v[188:189]\nv_pk_mul_f32 v[94:95], v[94:95], v[190:191]\nv_pk_fma_f32 v[92:93], v[92:93], v[156:157], v[236:237]\nv_pk_fma_f32 v[94:95], v[94:95], v[158:159], v[238:239]\ncv 206,92,93\ncv 207,94,95\ns_nop 0\n"
"v_permlane16_swap_b32 v204, v206\nv_permlane16_swap_b32 v205, v207\nbuffer_store_dwordx4 v[204:207], v29, s[16:19], s95 offen offset:64 sc1\n39:\ns_endpgm\n.purgem lb\n.purgem sm\n.purgem lc\n"
".purgem dt\n.purgem wb\n.purgem mvva\n.purgem wl\n.purgem mava\n.purgem ve\n.purgem vr\n.purgem ar\n"
".purgem cv\n.purgem zvav\n.purgem zvva\n.purgem mvav\n.purgem zvvv\n.purgem mvvv\n"
  : : "{s[4:19]}"(g2s),"{s[28:35]}"(g2i),"{s[72:79]}"(in1),"{s[96:99]}"(in2),"{s[70:71]}"(in3),"{v[0:7]}"(dsb),"{v[18:19]}"(vb),"{v[24:27]}"(go2),"{v[28:31]}"(ax),"{s[36:51]}"(g1s),"{s[52:59]}"(g1t),"{s[60:63]}"(in0),"{v[20:23]}"(go),"{v33}"(ln1),"{v35}"(ln3),"{s[0:3]}"(spf),"{s[64:65]}"(o01),"{s66}"(o4),"{v[96:111]}"(xr),"{v136}"(wv),"{v[140:141]}"(pf) : "v8","v9","v10","v11","v12","v13","v14","v15","v16","v17","v32","v34","v36","v37","v38","v39","v40","v41","v42","v43","v44","v45","v46","v47","v48","v49","v50","v51","v52","v53","v54","v55","v56","v57","v58","v59","v60","v61","v62","v63","v64","v65","v66","v67","v68","v69","v70","v71","v72","v73","v74","v75","v76","v77","v78","v79","v80","v81","v82","v83","v84","v85","v86","v87","v88","v89","v90","v91","v92","v93","v94","v95","v112","v113","v114","v115","v116","v117","v118","v119","v120","v121","v122","v123","v124","v125","v126","v127","v128","v129","v130","v131","v132","v133","v134","v135","v137","v138","v139","v142","v143","v144","v145","v146","v147","v148","v149","v150","v151","v152","v153","v154","v155","v156","v157","v158","v159","v160","v161","v162","v163","v164","v165","v166","v167","v168","v169","v170","v171","v172","v173","v174","v175","v176","v177","v178","v179","v180","v181","v182","v183","v184","v185","v186","v187","v188","v189","v190","v191","v192","v193","v194","v195","v196","v197","v198","v199","v200","v201","v202","v203","v204","v205","v206","v207","v208","v209","v210","v211","v212","v213","v214","v215","v216","v217","v218","v219","v220","v221","v222","v223","v224","v225","v226","v227","v228","v229","v230","v231","v232","v233","v234","v235","v236","v237","v238","v239","v240","v241","v242","v243","v244","v245","v246","v247","v248","v249","v250","v251","v252","v253","v254","v255","a0","a1","a2","a3","a4","a5","a6","a7","a8","a9","a10","a11","a12","a13","a14","a15","a16","a17","a18","a19","a20","a21","a22","a23","a24","a25","a26","a27","a28","a29","a30","a31","a32","a33","a34","a35","a36","a37","a38","a39","a40","a41","a42","a43","a44","a45","a46","a47","a48","a49","a50","a51","a52","a53","a54","a55","a56","a57","a58","a59","a60","a61","a62","a63","a64","a65","a66","a67","a68","a69","a70","a71","a72","a73","a74","a75","a76","a77","a78","a79","a80","a81","a82","a83","a84","a85","a86","a87","a88","a89","a90","a91","a92","a93","a94","a95","a96","a97","a98","a99","a100","a101","a102","a103","a104","a105","a106","a107","a108","a109","a110","a111","a112","a113","a114","a115","a116","a117","a118","a119","a120","a121","a122","a123","a124","a125","a126","a127","a128","a129","a130","a131","a132","a133","a134","a135","a136","a137","a138","a139","a140","a141","a142","a143","a144","a145","a146","a147","a148","a149","a150","a151","a152","a153","a154","a155","a156","a157","a158","a159","a160","a161","a162","a163","a164","a165","a166","a167","a168","a169","a170","a171","a172","a173","a174","a175","a176","a177","a178","a179","a180","a181","a182","a183","a184","a185","a186","a187","a188","a189","a190","a191","a192","a193","a194","a195","a196","a197","a198","a199","a200","a201","a202","a203","a204","a205","a206","a207","a208","a209","a210","a211","a212","a213","a214","a215","a216","a217","a218","a219","a220","a221","a222","a223","a224","a225","a226","a227","a228","a229","a230","a231","a232","a233","a234","a235","a236","a237","a238","a239","a240","a241","a242","a243","a244","a245","a246","a247","a248","a249","a250","a251","a252","a253","a254","a255","s67","s68","s69","s80","s81","s82","s83","s84","s85","s86","s87","s88","s89","s90","s91","s92","s93","s94","s95","scc","vcc","memory");
}


extern "C" void kernel_launch(void* const* d_in, const int* in_sizes, int n_in, void* d_out, int out_size, void* d_ws, size_t ws_size, hipStream_t stream){
  const u16* x=(const u16*)d_in[0]; const u16* wqkv=(const u16*)d_in[1]; const u16* wphi=(const u16*)d_in[2]; const u16* wo=(const u16*)d_in[3];
  const u16* wg=(const u16*)d_in[5]; const u16* wu=(const u16*)d_in[6]; const u16* wd=(const u16*)d_in[7];
  char* ws=(char*)d_ws;
  hipFuncSetAttribute((const void*)k_f,hipFuncAttributeMaxDynamicSharedMemorySize,163840);

  k_f<<<256,256,163840,stream>>>(x,wqkv,wo,wg,wu,wd,(u16*)d_out,ws,wphi);
}
```
